```python
import torch

def _softmax(x, axis=-1):
    return torch.softmax(x, dim=axis)

def _gelu(x):
    return torch.nn.functional.gelu(x, approximate='tanh')

def _silu(x):
    return torch.nn.functional.silu(x)

def _sigmoid(x):
    return torch.sigmoid(x)

def _layernorm(x, axis=-1, eps=1e-05):
    m = x.mean(dim=axis, keepdim=True)
    v = x.var(dim=axis, keepdim=True, unbiased=False)
    return (x - m) / torch.sqrt(v + eps)

def _scatter_add(tgt, idx, src, axis=0):
    return tgt.clone().index_add_(axis, idx, src)

def _segment_sum(src, seg, n):
    out = torch.zeros((n,) + src.shape[1:], dtype=src.dtype)
    return out.index_add_(0, seg, src)

def _pad(x, pw):
    flat = tuple((v for pair in reversed(pw) for v in pair))
    return torch.nn.functional.pad(x, flat)
kernel_name = 'adaLN_cond__linear_kernel_hedgehog__glu_gated_bilinear__prompt'

def setup_inputs():

    _w = lambda a: (lambda t: t.to(torch.bfloat16) if t.is_floating_point() else t)(torch.as_tensor(a))

    def _A_setup():
        B, S, D, H = (1, 2048, 512, 32)
        Dh = D // H
        return {'x': _w((lambda *s: torch.randn(*s, dtype=torch.bfloat16) * 0.1)(B, S, D)), 'wqkv': _w((lambda *s: torch.randn(*s, dtype=torch.bfloat16) * 0.2)(D, 3 * D)), 'w_phi': _w((lambda *s: torch.randn(*s, dtype=torch.bfloat16) * 0.2)(H, Dh, Dh) * Dh ** (-0.5)), 'wo': _w((lambda *s: torch.randn(*s, dtype=torch.bfloat16) * 0.2)(D, D))}
    _A_in = _A_setup()

    def _B_setup():
        B, S, D, F = (1, 2048, 512, 1376)
        return {'x': _w((lambda *s: torch.randn(*s, dtype=torch.bfloat16) * 0.1)(B, S, D)), 'w_gate': _w((lambda *s: torch.randn(*s, dtype=torch.bfloat16) * 3.2)(D, F) * D ** (-0.5)), 'w_up': _w((lambda *s: torch.randn(*s, dtype=torch.bfloat16) * 3.2)(D, F) * D ** (-0.5)), 'w_down': _w((lambda *s: torch.randn(*s, dtype=torch.bfloat16) * 3.2)(F, D) * F ** (-0.5))}
    _B_in = _B_setup()
    return {'A_x': _A_in['x'], 'A_wqkv': _A_in['wqkv'], 'A_w_phi': _A_in['w_phi'], 'A_wo': _A_in['wo'], 'B_x': _B_in['x'], 'B_w_gate': _B_in['w_gate'], 'B_w_up': _B_in['w_up'], 'B_w_down': _B_in['w_down'], 'c': _w((lambda *s: torch.randn(*s, dtype=torch.bfloat16) * 0.4)(1, 512) * 8.0), 'w_mod': _w((lambda *s: torch.randn(*s, dtype=torch.bfloat16) * 0.4)(512, 6 * 512) * 512 ** (-0.5) * 8.0)}

def reference(A_x, A_wqkv, A_w_phi, A_wo, B_x, B_w_gate, B_w_up, B_w_down, c, w_mod):

    def _A_ref(x, wqkv, w_phi, wo):
        B, S, D = x.shape
        H = 32
        Dh = D // H
        qkv = (lambda x, *d: x.permute(*d))((x @ wqkv).reshape(B, S, 3, H, Dh), 2, 0, 3, 1, 4)
        q, k, v = (qkv[0], qkv[1], qkv[2])
        pq = torch.einsum('bhsd,hdf->bhsf', q, w_phi)
        pk = torch.einsum('bhsd,hdf->bhsf', k, w_phi)
        phi_q = torch.concatenate([_softmax(pq, axis=-1), _softmax(-pq, axis=-1)], axis=-1)
        phi_k = torch.concatenate([_softmax(pk, axis=-1), _softmax(-pk, axis=-1)], axis=-1)
        kv = torch.einsum('bhsf,bhsd->bhfd', phi_k, v)
        z = torch.einsum('bhsf->bhf', phi_k)
        num = torch.einsum('bhqf,bhfd->bhqd', phi_q, kv)
        den = torch.einsum('bhqf,bhf->bhq', phi_q, z)[..., None] + 1e-06
        o = num / den
        return (lambda x, *d: x.permute(*d))(o, 0, 2, 1, 3).reshape(B, S, D) @ wo

    def _B_ref(x, w_gate, w_up, w_down):
        g = torch.einsum('bsd,df->bsf', x, w_gate)
        u = torch.einsum('bsd,df->bsf', x, w_up)
        h = g * u
        return torch.einsum('bsf,fd->bsd', h, w_down)
    x = A_x
    mod = (_silu(c) @ w_mod).reshape(c.shape[0], 6, 1, -1)
    sa, ca, ga, sb, cb, gb = (mod[:, 0].clone(), mod[:, 1].clone(), mod[:, 2].clone(), mod[:, 3].clone(), mod[:, 4].clone(), mod[:, 5].clone())
    h = _layernorm(x, axis=-1) * (1 + ca) + sa
    x = x + ga * _A_ref(h, A_wqkv, A_w_phi, A_wo)
    h = _layernorm(x, axis=-1) * (1 + cb) + sb
    x = x + gb * _B_ref(h, B_w_gate, B_w_up, B_w_down)
    return x
```

```cpp
#include <hip/hip_runtime.h>
#include <hip/hip_bf16.h>
typedef unsigned short u16;
__device__ __forceinline__ float bf2f(u16 b){ return __uint_as_float(((unsigned)b)<<16); }
__device__ __forceinline__ u16 f2bf(float f){ unsigned u=__float_as_uint(f); u += 0x7FFFu + ((u>>16)&1u); return (u16)(u>>16); }
__device__ __forceinline__ float rbf(float f){ return bf2f(f2bf(f)); }
constexpr int S=2048, D=512, H=32, DH=16, F=1376;

__global__ void k_mod(const u16* c, const u16* wmod, float* mod){
  int n = blockIdx.x*blockDim.x+threadIdx.x; if(n>=6*D) return;
  float acc=0.f;
  for(int k=0;k<D;++k){ float cv=bf2f(c[k]); float s=rbf(cv/(1.f+__expf(-cv))); acc += s*bf2f(wmod[(size_t)k*6*D+n]); }
  mod[n]=rbf(acc);
}
__global__ void k_ln(const float* xin, const u16* xin_bf, const float* sh, const float* sc, u16* h){
  int t=blockIdx.x; __shared__ float red[2][64];
  int tid=threadIdx.x; float v[8]; float s=0,ss=0;
  for(int j=0;j<8;++j){ int k=tid*8+j; v[j]= xin? xin[(size_t)t*D+k] : bf2f(xin_bf[(size_t)t*D+k]); s+=v[j]; }
  red[0][tid]=s; __syncthreads(); s=0; for(int i=0;i<64;++i) s+=red[0][i];
  float m=s/D;
  for(int j=0;j<8;++j){ float d=v[j]-m; ss+=d*d; }
  red[1][tid]=ss; __syncthreads(); ss=0; for(int i=0;i<64;++i) ss+=red[1][i];
  float r=rsqrtf(ss/D+1e-5f);
  for(int j=0;j<8;++j){ int k=tid*8+j; h[(size_t)t*D+k]=f2bf((v[j]-m)*r*rbf(1.f+sc[k])+sh[k]); }
}
__global__ void k_gemm(const u16* A, const u16* B, float* C, int M,int N,int K){
  int n=blockIdx.x*blockDim.x+threadIdx.x; int m=blockIdx.y; if(n>=N) return;
  float acc=0; for(int k=0;k<K;++k) acc+=bf2f(A[(size_t)m*K+k])*bf2f(B[(size_t)k*N+n]);
  C[(size_t)m*N+n]=acc;
}
__global__ void k_phi(const float* qkv, const u16* wphi, float* phiq, float* phik){
  int idx=blockIdx.x*blockDim.x+threadIdx.x;
  int h=idx%H, which=(idx/H)%2, t=idx/(2*H); if(t>=S) return;
  float p[16];
  for(int f=0;f<16;++f){ float a=0; for(int d=0;d<16;++d) a+=rbf(qkv[(size_t)t*3*D+which*D+h*16+d])*bf2f(wphi[h*256+d*16+f]); p[f]=rbf(a);}
  float mx=p[0],mn=p[0]; for(int f=1;f<16;++f){mx=fmaxf(mx,p[f]);mn=fminf(mn,p[f]);}
  float e1[16],e2[16],s1=0,s2=0; for(int f=0;f<16;++f){e1[f]=__expf(p[f]-mx);e2[f]=__expf(mn-p[f]);s1+=e1[f];s2+=e2[f];}
  float* o=(which?phik:phiq)+((size_t)t*H+h)*32;
  for(int f=0;f<16;++f){o[f]=rbf(e1[f]/s1);o[16+f]=rbf(e2[f]/s2);}
}
__global__ void k_kv(const float* phik,const float* qkv,float* kv){
  int idx=blockIdx.x*blockDim.x+threadIdx.x; if(idx>=H*32*17) return;
  int d=idx%17,f=(idx/17)%32,h=idx/(17*32); float a=0;
  for(int t=0;t<S;++t){ float pk=phik[((size_t)t*H+h)*32+f]; a+= d<16? pk*rbf(qkv[(size_t)t*3*D+2*D+h*16+d]) : pk; }
  kv[idx]=rbf(a);
}
__global__ void k_o(const float* phiq,const float* kv,u16* o){
  int idx=blockIdx.x*blockDim.x+threadIdx.x; int d=idx%16,h=(idx/16)%H,t=idx/D; if(t>=S) return;
  float num=0,den=0; for(int f=0;f<32;++f){ float p=phiq[((size_t)t*H+h)*32+f]; num+=p*kv[(h*32+f)*17+d]; den+=p*kv[(h*32+f)*17+16]; }
  o[idx]=f2bf(rbf(num)/rbf(rbf(den)+1e-6f));
}
__global__ void k_res1(const u16* x,const float* a,const float* g,float* x1){
  int idx=blockIdx.x*blockDim.x+threadIdx.x; x1[idx]=rbf(bf2f(x[idx])+rbf(g[idx%D]*rbf(a[idx])));
}
__global__ void k_gu(const float* g,const float* u,u16* hh){ int idx=blockIdx.x*blockDim.x+threadIdx.x; if(idx<S*F) hh[idx]=f2bf(rbf(g[idx])*rbf(u[idx])); }
__global__ void k_res2(const float* x1,const float* y,const float* g,u16* out){
  int idx=blockIdx.x*blockDim.x+threadIdx.x; out[idx]=f2bf(x1[idx]+rbf(g[idx%D]*rbf(y[idx])));
}
extern "C" void kernel_launch(void* const* d_in, const int* in_sizes, int n_in, void* d_out, int out_size, void* d_ws, size_t ws_size, hipStream_t stream){
  const u16* x=(const u16*)d_in[0]; const u16* wqkv=(const u16*)d_in[1]; const u16* wphi=(const u16*)d_in[2]; const u16* wo=(const u16*)d_in[3];
  const u16* wg=(const u16*)d_in[5]; const u16* wu=(const u16*)d_in[6]; const u16* wd=(const u16*)d_in[7]; const u16* c=(const u16*)d_in[8]; const u16* wmod=(const u16*)d_in[9];
  char* w=(char*)d_ws; size_t MB=1<<20;
  float* mod=(float*)(w); u16* h=(u16*)(w+1*MB); float* qkv=(float*)(w+4*MB); float* phiq=(float*)(w+20*MB); float* phik=(float*)(w+30*MB);
  float* kv=(float*)(w+40*MB); u16* o=(u16*)(w+41*MB); float* att=(float*)(w+44*MB); float* x1=(float*)(w+50*MB); float* g=(float*)(w+60*MB); float* u=(float*)(w+80*MB);
  u16* hh=(u16*)(w+100*MB); float* y=(float*)(w+110*MB);
  k_mod<<<12,256,0,stream>>>(c,wmod,mod);
  k_ln<<<S,64,0,stream>>>(nullptr,x,mod,mod+D,h);
  k_gemm<<<dim3(6,S),256,0,stream>>>(h,wqkv,qkv,S,3*D,D);
  k_phi<<<S*H*2/256,256,0,stream>>>(qkv,wphi,phiq,phik);
  k_kv<<<(H*32*17+63)/64,64,0,stream>>>(phik,qkv,kv);
  k_o<<<S*D/256,256,0,stream>>>(phiq,kv,o);
  k_gemm<<<dim3(2,S),256,0,stream>>>(o,wo,att,S,D,D);
  k_res1<<<S*D/256,256,0,stream>>>(x,att,mod+2*D,x1);
  k_ln<<<S,64,0,stream>>>(x1,nullptr,mod+3*D,mod+4*D,h);
  k_gemm<<<dim3(6,S),256,0,stream>>>(h,wg,g,S,F,D);
  k_gemm<<<dim3(6,S),256,0,stream>>>(h,wu,u,S,F,D);
  k_gu<<<(S*F+255)/256,256,0,stream>>>(g,u,hh);
  k_gemm<<<dim3(2,S),256,0,stream>>>(hh,wd,y,S,D,F);
  k_res2<<<S*D/256,256,0,stream>>>(x1,y,mod+5*D,(u16*)d_out);
}
```

```cpp
#include <hip/hip_runtime.h>
#include <hip/hip_bf16.h>
typedef unsigned short u16;
#define MODE 0
__device__ __forceinline__ float bf2f(u16 b){ return __uint_as_float(((unsigned)b)<<16); }
__device__ __forceinline__ u16 f2bf(float f){ unsigned u=__float_as_uint(f); u += 0x7FFFu + ((u>>16)&1u); return (u16)(u>>16); }
__device__ __forceinline__ float rbf_(float f){ return bf2f(f2bf(f)); }
__device__ __forceinline__ float RA(float f){ return MODE>=1 ? rbf_(f) : f; }
__device__ __forceinline__ float RB(float f){ return MODE>=2 ? rbf_(f) : f; }
constexpr int S=2048, D=512, H=32, DH=16, F=1376;

__global__ void k_mod(const u16* c, const u16* wmod, float* mod){
  int n = blockIdx.x*blockDim.x+threadIdx.x; if(n>=6*D) return;
  float acc=0.f;
  for(int k=0;k<D;++k){ float cv=bf2f(c[k]); float s=RB(cv/(1.f+__expf(-cv))); acc += s*bf2f(wmod[(size_t)k*6*D+n]); }
  mod[n]=RB(acc);
}
__global__ void k_ln(const float* xin, const u16* xin_bf, const float* sh, const float* sc, float* h){
  int t=blockIdx.x; __shared__ float red[2][64];
  int tid=threadIdx.x; float v[8]; float s=0,ss=0;
  for(int j=0;j<8;++j){ int k=tid*8+j; v[j]= xin? xin[(size_t)t*D+k] : bf2f(xin_bf[(size_t)t*D+k]); s+=v[j]; }
  red[0][tid]=s; __syncthreads(); s=0; for(int i=0;i<64;++i) s+=red[0][i];
  float m=s/D;
  for(int j=0;j<8;++j){ float d=v[j]-m; ss+=d*d; }
  red[1][tid]=ss; __syncthreads(); ss=0; for(int i=0;i<64;++i) ss+=red[1][i];
  float r=rsqrtf(ss/D+1e-5f);
  for(int j=0;j<8;++j){ int k=tid*8+j; h[(size_t)t*D+k]=RA((v[j]-m)*r*RB(1.f+sc[k])+sh[k]); }
}
__global__ void k_gemm(const float* A, const u16* B, float* C, int M,int N,int K){
  int n=blockIdx.x*blockDim.x+threadIdx.x; int m=blockIdx.y; if(n>=N) return;
  float acc=0; for(int k=0;k<K;++k) acc+=A[(size_t)m*K+k]*bf2f(B[(size_t)k*N+n]);
  C[(size_t)m*N+n]=acc;
}
__global__ void k_phi(const float* qkv, const u16* wphi, float* phiq, float* phik){
  int idx=blockIdx.x*blockDim.x+threadIdx.x;
  int h=idx%H, which=(idx/H)%2, t=idx/(2*H); if(t>=S) return;
  float p[16];
  for(int f=0;f<16;++f){ float a=0; for(int d=0;d<16;++d) a+=RA(qkv[(size_t)t*3*D+which*D+h*16+d])*bf2f(wphi[h*256+d*16+f]); p[f]=RB(a);}
  float mx=p[0],mn=p[0]; for(int f=1;f<16;++f){mx=fmaxf(mx,p[f]);mn=fminf(mn,p[f]);}
  float e1[16],e2[16],s1=0,s2=0; for(int f=0;f<16;++f){e1[f]=__expf(p[f]-mx);e2[f]=__expf(mn-p[f]);s1+=e1[f];s2+=e2[f];}
  float* o=(which?phik:phiq)+((size_t)t*H+h)*32;
  for(int f=0;f<16;++f){o[f]=RA(e1[f]/s1);o[16+f]=RA(e2[f]/s2);}
}
__global__ void k_kv(const float* phik,const float* qkv,float* kv){
  int idx=blockIdx.x*blockDim.x+threadIdx.x; if(idx>=H*32*17) return;
  int d=idx%17,f=(idx/17)%32,h=idx/(17*32); float a=0;
  for(int t=0;t<S;++t){ float pk=phik[((size_t)t*H+h)*32+f]; a+= d<16? pk*RA(qkv[(size_t)t*3*D+2*D+h*16+d]) : pk; }
  kv[idx]=RA(a);
}
__global__ void k_o(const float* phiq,const float* kv,float* o){
  int idx=blockIdx.x*blockDim.x+threadIdx.x; int d=idx%16,h=(idx/16)%H,t=idx/D; if(t>=S) return;
  float num=0,den=0; for(int f=0;f<32;++f){ float p=phiq[((size_t)t*H+h)*32+f]; num+=p*kv[(h*32+f)*17+d]; den+=p*kv[(h*32+f)*17+16]; }
  o[idx]=RA(RB(num)/RB(RB(den)+1e-6f));
}
__global__ void k_res1(const u16* x,const float* a,const float* g,float* x1){
  int idx=blockIdx.x*blockDim.x+threadIdx.x; x1[idx]=RB(bf2f(x[idx])+RB(g[idx%D]*RB(a[idx])));
}
__global__ void k_gu(const float* g,const float* u,float* hh){ int idx=blockIdx.x*blockDim.x+threadIdx.x; if(idx<S*F) hh[idx]=RA(RB(g[idx])*RB(u[idx])); }
__global__ void k_res2(const float* x1,const float* y,const float* g,u16* out){
  int idx=blockIdx.x*blockDim.x+threadIdx.x; out[idx]=f2bf(x1[idx]+RB(g[idx%D]*RB(y[idx])));
}
extern "C" void kernel_launch(void* const* d_in, const int* in_sizes, int n_in, void* d_out, int out_size, void* d_ws, size_t ws_size, hipStream_t stream){
  const u16* x=(const u16*)d_in[0]; const u16* wqkv=(const u16*)d_in[1]; const u16* wphi=(const u16*)d_in[2]; const u16* wo=(const u16*)d_in[3];
  const u16* wg=(const u16*)d_in[5]; const u16* wu=(const u16*)d_in[6]; const u16* wd=(const u16*)d_in[7]; const u16* c=(const u16*)d_in[8]; const u16* wmod=(const u16*)d_in[9];
  char* w=(char*)d_ws; size_t MB=1<<20;
  float* mod=(float*)(w); float* h=(float*)(w+130*MB); float* qkv=(float*)(w+4*MB); float* phiq=(float*)(w+20*MB); float* phik=(float*)(w+30*MB);
  float* kv=(float*)(w+40*MB); float* o=(float*)(w+140*MB); float* att=(float*)(w+44*MB); float* x1=(float*)(w+50*MB); float* g=(float*)(w+60*MB); float* u=(float*)(w+80*MB);
  float* hh=(float*)(w+150*MB); float* y=(float*)(w+110*MB);
  k_mod<<<12,256,0,stream>>>(c,wmod,mod);
  k_ln<<<S,64,0,stream>>>(nullptr,x,mod,mod+D,h);
  k_gemm<<<dim3(6,S),256,0,stream>>>(h,wqkv,qkv,S,3*D,D);
  k_phi<<<S*H*2/256,256,0,stream>>>(qkv,wphi,phiq,phik);
  k_kv<<<(H*32*17+63)/64,64,0,stream>>>(phik,qkv,kv);
  k_o<<<S*D/256,256,0,stream>>>(phiq,kv,o);
  k_gemm<<<dim3(2,S),256,0,stream>>>(o,wo,att,S,D,D);
  k_res1<<<S*D/256,256,0,stream>>>(x,att,mod+2*D,x1);
  k_ln<<<S,64,0,stream>>>(x1,nullptr,mod+3*D,mod+4*D,h);
  k_gemm<<<dim3(6,S),256,0,stream>>>(h,wg,g,S,F,D);
  k_gemm<<<dim3(6,S),256,0,stream>>>(h,wu,u,S,F,D);
  k_gu<<<(S*F+255)/256,256,0,stream>>>(g,u,hh);
  k_gemm<<<dim3(2,S),256,0,stream>>>(hh,wd,y,S,D,F);
  k_res2<<<S*D/256,256,0,stream>>>(x1,y,mod+5*D,(u16*)d_out);
}
```

```cpp
#include <hip/hip_runtime.h>

typedef unsigned short u16;
typedef unsigned int u32;
typedef u32 v4u __attribute__((ext_vector_type(4)));
typedef u32 v2u __attribute__((ext_vector_type(2)));
typedef float f32x2 __attribute__((ext_vector_type(2)));
typedef float f32x4 __attribute__((ext_vector_type(4)));
typedef float f32x16 __attribute__((ext_vector_type(16)));
typedef short bf16x8 __attribute__((ext_vector_type(8)));
typedef short s16x4 __attribute__((ext_vector_type(4)));
typedef __bf16 bf16x2 __attribute__((ext_vector_type(2)));
#define GAS __attribute__((address_space(1)))
#define LAS __attribute__((address_space(3)))
#define DI __device__ __forceinline__
#define RLX_AGENT __ATOMIC_RELAXED, __HIP_MEMORY_SCOPE_AGENT
typedef GAS u32 gu32;

constexpr int S_ = 2048, D_ = 512, F_ = 1376;
constexpr int WS_EPOCH = 0, WS_CNT = 256, CNT_STRIDE = 256;
constexpr int WS_GRAN = 32768, WS_ROWST = 98304, WS_MFLAG = 20480, WS_SLAB = 131072, WS_Z = 655360, WS_STAT = 720896;
constexpr int WS_O = 1 << 20, WS_Y1 = 3 << 20, WS_HH = 5 << 20, WS_GT = 11 << 20, WS_DBG = 15 << 20, WS_BYTES = 16 << 20;
constexpr int L_B = 65536, L_T = 131072, L_MISC = 155648, L_TOTAL = 163840;
constexpr int L_CS = L_MISC, L_VEC = L_MISC, L_RED = L_MISC + 4608, L_STATX = L_MISC + 4096, L_STAT2 = L_MISC + 4096, L_CWSW = L_MISC + 5120;
constexpr int L_ARR = L_MISC + 6912, L_WPHI = L_MISC + 7168;
constexpr int L_EX = 0, L_KVT = 139264, L_WO = L_B, L_EX3 = 139264;
constexpr unsigned CODE_SLICES = 19;
constexpr unsigned SPIN_CAP = 1u << 18;

DI float asf(u32 v) { return __builtin_bit_cast(float, v); }
DI u32 asu(float v) { return __builtin_bit_cast(u32, v); }
DI u32 asu(bf16x2 v) { return __builtin_bit_cast(u32, v); }
DI float bf2f(u32 b) { return asf(b << 16); }
DI float bflo(u32 p) { return asf(p << 16); }
DI float bfhi(u32 p) { return asf(p & 0xffff0000u); }
DI u32 pk2(float a, float b) { f32x2 v = {a, b}; return asu(__builtin_convertvector(v, bf16x2)); }
DI float rb(float a) { return bflo(pk2(a, a) & 0xffffu); }
DI v4u mk_srd(const void* p, u32 bytes) {
  unsigned long long a = (unsigned long long)p;
  return v4u{(u32)a, (u32)(a >> 32) & 0xffffu, bytes, 0x00020000u};
}
DI void glds(v4u rs, u32 voff, u32 soff, u32 ldsaddr) {
  asm volatile("s_mov_b32 m0, %3\n\ts_nop 0\n\tbuffer_load_dwordx4 %0, %1, %2 offen lds" ::"v"(voff), "s"(rs), "s"(soff), "s"(ldsaddr) : "memory");
}
template <int SO, int LO> DI void gldsi(v4u rs, u32 voff, u32 soff, u32 ldsaddr) {
  u32 t;
  asm volatile("s_add_u32 m0, %4, %6\n\ts_add_u32 %0, %3, %5\n\tbuffer_load_dwordx4 %1, %2, %0 offen lds"
               : "=&s"(t) : "v"(voff), "s"(rs), "s"(soff), "s"(ldsaddr), "i"(SO), "i"(LO) : "memory", "scc");
}
DI v2u ld64(v4u rs, u32 voff, u32 soff) {
  v2u r;
  asm volatile("buffer_load_dwordx2 %0, %1, %2, %3 offen" : "=v"(r) : "v"(voff), "s"(rs), "s"(soff) : "memory");
  return r;
}
DI v4u ld128(v4u rs, u32 voff, u32 soff) {
  v4u r;
  asm volatile("buffer_load_dwordx4 %0, %1, %2, %3 offen" : "=v"(r) : "v"(voff), "s"(rs), "s"(soff) : "memory");
  return r;
}
DI v4u ld128c(v4u rs, u32 voff, u32 soff) {
  v4u r;
  asm volatile("buffer_load_dwordx4 %0, %1, %2, %3 offen sc1" : "=v"(r) : "v"(voff), "s"(rs), "s"(soff) : "memory");
  return r;
}
DI v2u ld64c(v4u rs, u32 voff, u32 soff) {
  v2u r;
  asm volatile("buffer_load_dwordx2 %0, %1, %2, %3 offen sc1" : "=v"(r) : "v"(voff), "s"(rs), "s"(soff) : "memory");
  return r;
}
DI void st128c(v4u v, v4u rs, u32 voff, u32 soff) {
  asm volatile("buffer_store_dwordx4 %0, %1, %2, %3 offen sc1\n\ts_nop 1" ::"v"(v), "v"(voff), "s"(rs), "s"(soff) : "memory");
}
DI void st64c(v2u v, v4u rs, u32 voff, u32 soff) {
  asm volatile("buffer_store_dwordx2 %0, %1, %2, %3 offen sc1" ::"v"(v), "v"(voff), "s"(rs), "s"(soff) : "memory");
}
#define WAITV(n) asm volatile("s_waitcnt vmcnt(" #n ")" ::: "memory")
#define BAR() asm volatile("s_waitcnt lgkmcnt(0)\n\ts_barrier" ::: "memory")
#define PIN(x) asm volatile("" : "+v"(x))
#define SB() __builtin_amdgcn_sched_barrier(0)
#define RBAR() asm volatile("s_barrier" ::: "memory")
template <int N> DI void waitv() { asm volatile("s_waitcnt vmcnt(%0)" ::"i"(N) : "memory"); }

DI v4u lds128(u32 a) { return *(const LAS v4u*)(size_t)a; }
DI v2u lds64(u32 a) { return *(const LAS v2u*)(size_t)a; }
DI void sts128(u32 a, v4u v) { *(LAS v4u*)(size_t)a = v; }
DI void sts64(u32 a, v2u v) { *(LAS v2u*)(size_t)a = v; }
DI v2u trd(u32 a) { return __builtin_bit_cast(v2u, __builtin_amdgcn_ds_read_tr16_b64_v4i16((LAS s16x4*)(size_t)a)); }
DI v4u cat(v2u a, v2u b) { return v4u{a.x, a.y, b.x, b.y}; }
DI f32x16 mfma32(v4u a, v4u b, f32x16 c) {
  return __builtin_amdgcn_mfma_f32_32x32x16_bf16(__builtin_bit_cast(bf16x8, a), __builtin_bit_cast(bf16x8, b), c, 0, 0, 0);
}
DI f32x4 mfma16(v4u a, v4u b, f32x4 c) {
  return __builtin_amdgcn_mfma_f32_16x16x32_bf16(__builtin_bit_cast(bf16x8, a), __builtin_bit_cast(bf16x8, b), c, 0, 0, 0);
}
DI float sum32(float v) {
  u32 a = asu(v), c = a;
  auto r = __builtin_amdgcn_permlane32_swap(a, c, false, false);
  return asf(r[0]) + asf(r[1]);
}
DI float sum16(float v) {
  u32 a = asu(v), c = a;
  auto r = __builtin_amdgcn_permlane16_swap(a, c, false, false);
  return asf(r[0]) + asf(r[1]);
}
DI float sum8(float v) {
  return v + asf(__builtin_amdgcn_update_dpp(0, __builtin_bit_cast(int, v), 0x128, 0xf, 0xf, false));
}
DI float sum4(float v) {
  return v + asf(__builtin_amdgcn_update_dpp(0, __builtin_bit_cast(int, v), 0x124, 0xf, 0xf, false));
}
DI void ln_stats(float s, float q, float& mean, float& sd) {
  const float m = s * (1.0f / D_);
  float var = q * (1.0f / D_) - m * m;
  var = fmaxf(var, 0.0f);
  mean = rb(m);
  sd = rb(__builtin_amdgcn_sqrtf(rb(rb(var) + 1e-5f)));
}
DI void poll_cnt(gu32* cnt, u32 target) {
  for (u32 spins = 0; spins < SPIN_CAP; ++spins) {
    const u32 v = __builtin_amdgcn_readfirstlane(__hip_atomic_load(cnt, RLX_AGENT));
    if (v >= target) break;
  }
  __builtin_amdgcn_fence(__ATOMIC_ACQUIRE, "wavefront");
}
DI void hedgehog(const f32x16& p, u32 (&ph)[16]) {
  float mx = p[0], mn = p[0];
#pragma unroll
  for (int i = 1; i < 16; ++i) { mx = fmaxf(mx, p[i]); mn = fminf(mn, p[i]); }
  const float L = 1.44269504088896341f;
  const float a = -mx * L, b = mn * L;
  float e1[16], e2[16], s1 = 0.f, s2 = 0.f;
#pragma unroll
  for (int i = 0; i < 16; ++i) {
    e1[i] = __builtin_amdgcn_exp2f(__builtin_fmaf(p[i], L, a));
    e2[i] = __builtin_amdgcn_exp2f(__builtin_fmaf(p[i], -L, b));
    s1 += e1[i]; s2 += e2[i];
  }
  const float r1 = __builtin_amdgcn_rcpf(s1), r2 = __builtin_amdgcn_rcpf(s2);
#pragma unroll
  for (int i = 0; i < 8; ++i) {
    ph[i] = pk2(e1[2 * i] * r1, e1[2 * i + 1] * r1);
    ph[8 + i] = pk2(e2[2 * i] * r2, e2[2 * i + 1] * r2);
  }
}
DI void softmax16(const f32x16& p, u32 (&ph)[8]) {
  float mx = p[0];
#pragma unroll
  for (int i = 1; i < 16; ++i) mx = fmaxf(mx, p[i]);
  const float L = 1.44269504088896341f;
  const float a = -mx * L;
  float e[16], s0 = 0.f, s1 = 0.f;
#pragma unroll
  for (int i = 0; i < 16; ++i) {
    e[i] = __builtin_amdgcn_exp2f(__builtin_fmaf(p[i], L, a));
    if (i & 1) s1 += e[i]; else s0 += e[i];
  }
  const float r = __builtin_amdgcn_rcpf(s0 + s1);
#pragma unroll
  for (int i = 0; i < 8; ++i) ph[i] = pk2(e[2 * i] * r, e[2 * i + 1] * r);
}
DI v4u pack8(const f32x16& x, int s) {
  return v4u{pk2(x[8 * s], x[8 * s + 1]), pk2(x[8 * s + 2], x[8 * s + 3]), pk2(x[8 * s + 4], x[8 * s + 5]), pk2(x[8 * s + 6], x[8 * s + 7])};
}
DI void ex_put(u32 a, const f32x16& x) {
#pragma unroll
  for (int i = 0; i < 4; ++i) sts128(a + i * 1024, __builtin_bit_cast(v4u, f32x4{x[4 * i], x[4 * i + 1], x[4 * i + 2], x[4 * i + 3]}));
}
DI void ex_add(u32 a, f32x16& x) {
#pragma unroll
  for (int i = 0; i < 4; ++i) {
    const f32x4 t = __builtin_bit_cast(f32x4, lds128(a + i * 1024));
    x[4 * i] += t[0]; x[4 * i + 1] += t[1]; x[4 * i + 2] += t[2]; x[4 * i + 3] += t[3];
  }
}

#define STAMP(i) do {} while (0)

template <int I> struct IC { static constexpr int v = I; };
template <int I, int N, class Fn> DI void sfor(Fn&& f) {
  if constexpr (I < N) { f(IC<I>{}); sfor<I + 1, N>(f); }
}
#define AI __attribute__((always_inline))
#define PRIO(n) __builtin_amdgcn_s_setprio(n)
constexpr int wd_slot(int rd) {
  if (rd & 1) return -1;
  if (rd >= 60 && rd < 124) return (rd - 60) >> 1;
  return -1;
}
constexpr int wq_slot(int s) { return s < 12 ? L_T + s * 2048 : 57344 + (s - 12) * 2048; }
constexpr int y_put(int i) { return i < 4 ? 4 + i : ((i & 1) || i > 52 || i < 6 ? -1 : 8 + ((i - 6) >> 1)); }
constexpr int y_issued(int i) { return i < 4 ? 16 : (i < 8 ? 20 : (i < 14 ? 24 : (i < 22 ? 28 : 32))); }
constexpr int wd_sg(int i) { return i < 3 ? 16 + i : i - 3; }
template <int N> DI void fmac_bc(float& h, float c, float x) {
  asm("v_fmac_f32_dpp %0, %1, %2 row_newbcast:%3 row_mask:0xf bank_mask:0xf" : "+v"(h) : "v"(c), "v"(x), "i"(N));
}

__global__ void __launch_bounds__(512, 2)
fused_block(const u16* __restrict__ x, const u16* __restrict__ wqkv, const u16* __restrict__ wphi, const u16* __restrict__ wo,
            const u16* __restrict__ wg, const u16* __restrict__ wu, const u16* __restrict__ wd, const u16* __restrict__ cvec,
            const u16* __restrict__ wmod, u16* __restrict__ out, char* __restrict__ ws) {
  extern __shared__ __attribute__((aligned(16))) char smem[];
  const u32 LB = (u32)(size_t)(LAS char*)smem;
  STAMP(0);
  u32 cpf;
  {
    unsigned long long pc;
    u32 sl = ((blockIdx.x >> 3) % CODE_SLICES) * 2048u, off;
    asm volatile(
        "s_getpc_b64 %2\n\tv_lshlrev_b32 %1, 7, %3\n\tv_cmp_lt_u32 vcc, 63, %3\n\tv_mov_b32 %0, %4\n\ts_nop 0\n\tv_cndmask_b32 %0, 0, %0, vcc\n\t"
        "v_add_u32 %1, %1, %0\n\tv_cmp_gt_u32 vcc, 0x50, %3\n\ts_nop 1\n\tv_cndmask_b32 %1, 0, %1, vcc\n\tglobal_load_dword %0, %1, %2"
        : "=&v"(cpf), "=&v"(off), "=&s"(pc) : "v"(threadIdx.x), "s"(sl) : "memory", "vcc");
    SB();
  }
  asm volatile("" ::"s"(x), "s"(wqkv), "s"(wphi), "s"(wo), "s"(wg), "s"(wu), "s"(wd), "s"(cvec), "s"(wmod), "s"(out), "s"(ws));
  const int tid = threadIdx.x, lane = tid & 63;
  int w = __builtin_amdgcn_readfirstlane(tid >> 6);
  asm volatile("s_nop 4" : "+s"(w));
  const int b = blockIdx.x, g = b & 7, bi = b >> 3;
  const int m2 = 4 * (g >> 1) + (bi >> 3), j2 = 8 * (g & 1) + (bi & 7);
  const int pnl = 2 * g + (bi >> 4), rh = (bi >> 3) & 1, cb = bi & 7;
  const v4u rs_x = mk_srd(x, S_ * D_ * 2), rs_ws = mk_srd(ws, WS_BYTES);
  gu32* const ctl = (gu32*)ws;
  const int tg = w & 3, kh = w >> 2;
  const int c32 = lane & 31, hh = lane >> 5;
  const int l15 = lane & 15, g4 = lane >> 4, tq = (lane & 15) >> 2, tp = lane & 3;

  const bool prod = b < 96, sprod = b >= 96 && b < 224;
  const int cg = b % 48, kp = b / 48;
  const u32 xld = LB + w * 16384;
  v4u xr[4];
  u32 tagv;
  if (b == 0 && w == 0) __hip_atomic_store(ctl + (WS_CNT + lane * CNT_STRIDE) / 4, 0u, RLX_AGENT);
  asm volatile("buffer_load_dword %0, off, %1, 0 sc1" : "=v"(tagv) : "s"(rs_ws) : "memory");
  v2u mc2;
  if (prod && w == 0) {
    const v4u rs_m = mk_srd(wmod, D_ * 6 * D_ * 2), rs_c = mk_srd(cvec, D_ * 2);
    mc2 = ld64(rs_c, lane * 8, kp * 512);
    const u32 vo = (lane >> 3) * 6144 + (((lane & 7) ^ (((lane >> 4) & 1) << 1)) << 4);
    const u32 so = 256 * kp * 6144 + cg * 128;
    sfor<0, 32>([&](auto ic) AI { gldsi<ic.v * 8 * 6144, (ic.v < 24 ? L_T + ic.v * 1024 : (ic.v - 24) * 1024)>(rs_m, (ic.v & 1) ? vo ^ 64 : vo, so, LB); });
  }
  if (kh == 0 && sprod) {
#pragma unroll
    for (int r = 0; r < 4; ++r) xr[r] = ld128(rs_x, lane * 16, (16 * (b - 96) + 4 * w + r) * 1024);
  }
  if (w == 5) glds(mk_srd(wphi, 32 * 256 * 2), lane * 16, j2 * 1024, LB + L_WPHI);
  if (prod) RBAR();
  const u32 xvo0 = (lane >> 3) * 1024 + (((lane & 7) ^ (lane >> 4)) << 4), xvo1 = xvo0 ^ 64;
  const int rot = bi & 3;
  const u32 xso = (128 * m2 + 32 * tg) * 1024 + kh * 512 + rot * 128;
#define X_ISSUE(q4)                                                                       \
  do {                                                                                    \
    gldsi<0, (q4) * 4096>(rs_x, xvo0, xso ^ ((q4) * 128), xld);                           \
    gldsi<8192, (q4) * 4096 + 1024>(rs_x, xvo1, xso ^ ((q4) * 128), xld);                 \
    gldsi<16384, (q4) * 4096 + 2048>(rs_x, xvo0, xso ^ ((q4) * 128), xld);                \
    gldsi<24576, (q4) * 4096 + 3072>(rs_x, xvo1, xso ^ ((q4) * 128), xld);                \
  } while (0)
  if (kh == 0 && sprod) {
    asm volatile("s_waitcnt vmcnt(0)" : "+v"(xr[0]), "+v"(xr[1]), "+v"(xr[2]), "+v"(xr[3]), "+v"(tagv)::"memory");
    float s[4], q[4], t[2];
#pragma unroll
    for (int r = 0; r < 4; ++r) {
      s[r] = 0.f; q[r] = 0.f;
#pragma unroll
      for (int e = 0; e < 4; ++e) {
        const float lo = bflo(xr[r][e]), hi = bfhi(xr[r][e]);
        s[r] += lo + hi; q[r] = __builtin_fmaf(lo, lo, __builtin_fmaf(hi, hi, q[r]));
      }
    }
#pragma unroll
    for (int r = 0; r < 2; ++r) {
      auto rs = __builtin_amdgcn_permlane32_swap(asu(s[r]), asu(s[r + 2]), false, false);
      auto rq = __builtin_amdgcn_permlane32_swap(asu(q[r]), asu(q[r + 2]), false, false);
      s[r] = asf(rs[0]) + asf(rs[1]); q[r] = asf(rq[0]) + asf(rq[1]);
    }
    {
      auto rs = __builtin_amdgcn_permlane16_swap(asu(s[0]), asu(s[1]), false, false);
      auto rq = __builtin_amdgcn_permlane16_swap(asu(q[0]), asu(q[1]), false, false);
      t[0] = asf(rs[0]) + asf(rs[1]); t[1] = asf(rq[0]) + asf(rq[1]);
    }
#pragma unroll
    for (int r = 0; r < 2; ++r) {
      t[r] = sum4(sum8(t[r]));
      t[r] += asf(__builtin_amdgcn_update_dpp(0, (int)asu(t[r]), 0x122, 0xf, 0xf, false));
      t[r] += asf(__builtin_amdgcn_update_dpp(0, (int)asu(t[r]), 0x121, 0xf, 0xf, false));
    }
    float mean, sd;
    ln_stats(t[0], t[1], mean, sd);
    if (l15 == 0) st64c(v2u{pk2(mean, sd), tagv + 1u}, rs_ws, g4 * 8, WS_ROWST + (16 * (b - 96) + 4 * w) * 8);
  }
  if (prod && w == 0) {
    asm volatile("s_waitcnt vmcnt(32)" : "+v"(mc2), "+v"(tagv)::"memory");
    float sv[4] = {bflo(mc2.x), bfhi(mc2.x), bflo(mc2.y), bfhi(mc2.y)};
#pragma unroll
    for (int e = 0; e < 4; ++e) sv[e] = sv[e] * __builtin_amdgcn_rcpf(1.0f + __expf(-sv[e]));
    sts64(LB + 8192 + lane * 8, v2u{pk2(sv[0], sv[1]), pk2(sv[2], sv[3])});
    v4u sb[8];
#pragma unroll
    for (int ks = 0; ks < 8; ++ks) sb[ks] = lds128(LB + 8192 + ks * 64 + g4 * 16);
    u32 ma[4];
#pragma unroll
    for (int c = 0; c < 4; ++c) ma[c] = LB + (8 * g4 + tq) * 128 + tp * 8 + ((c ^ (((tq >> 1) & 1) | ((g4 & 1) << 1))) << 5);
    f32x4 acc[4];
#pragma unroll
    for (int c = 0; c < 4; ++c) acc[c] = f32x4{0.f, 0.f, 0.f, 0.f};
    sfor<0, 4>([&](auto ic) AI {
      constexpr int h2 = ic.v;
      waitv<24 - 8 * h2>();
      v4u fm[2][4];
#pragma unroll
      for (int k2 = 0; k2 < 2; ++k2)
#pragma unroll
        for (int c = 0; c < 4; ++c) {
          const u32 ad = ma[c] + (h2 < 3 ? L_T + (2 * h2 + k2) * 4096 : k2 * 4096);
          fm[k2][c] = cat(trd(ad), trd(ad + 512));
        }
#pragma unroll
      for (int k2 = 0; k2 < 2; ++k2)
#pragma unroll
        for (int c = 0; c < 4; ++c) acc[c] = mfma16(sb[2 * h2 + k2], fm[k2][c], acc[c]);
    });
    const float lo = (g4 & 1) ? acc[1][0] : acc[0][0], hi = (g4 & 1) ? acc[3][0] : acc[2][0];
    st64c(v2u{asu(hh ? hi : lo), tagv + 1u}, rs_ws, lane * 8, WS_GRAN + kp * 24576 + cg * 512);
  }
  STAMP(1);
  X_ISSUE(0); X_ISSUE(1); X_ISSUE(2);
  const v4u rs_w = mk_srd(wqkv, D_ * 3 * D_ * 2);
  const u32 wvo = ((lane >> 2) * 1536 + 8 * (lane & 3)) * 2;
  const u32 kso = (256 * (tg >> 1) * 1536 + 512 * (1 + (tg & 1)) + 32 * j2) * 2, kld = LB + L_B + tg * 16384;
  const u32 qso = ((256 * (tg & 1) + 16 * (tg >> 1)) * 1536 + 32 * j2) * 2, qld = LB + (tg >> 1) * 2048 + (tg & 1) * 1024;
#define K_ISSUE(s) gldsi<((s) & 3) * 16 * 3072, (s) * 1024>(rs_w, wvo, kso + (((s) >> 2) ^ rot) * (64 * 3072), kld)
#define Q_ISSUE(n) gldsi<((n) & 1) * 32 * 3072, wq_slot(2 * (n))>(rs_w, wvo, qso + (((n) >> 1) ^ rot) * (64 * 3072), qld)
  const bool p4 = j2 < 15;
  const int un0 = w < 4 ? 2 * w : 4 + w;
  float xf[16][8];
  u32 tagl;
  {
    u32 xa[4];
#pragma unroll
    for (int sp = 0; sp < 4; ++sp) { xa[sp] = LB + w * 16384 + c32 * 128 + (((2 * sp + hh) ^ ((c32 >> 1) & 7)) << 4); PIN(xa[sp]); }
#pragma unroll
    for (int q4 = 0; q4 < 4; ++q4) {
      if (q4 == 0) { X_ISSUE(3); WAITV(12); }
      if (q4 == 1) WAITV(8);
      if (q4 == 2) WAITV(4);
      if (q4 == 3) WAITV(0);
      v4u t[4];
#pragma unroll
      for (int sp = 0; sp < 4; ++sp) t[sp] = lds128(xa[sp] + q4 * 4096);
#pragma unroll
      for (int sp = 0; sp < 4; ++sp) {
#pragma unroll
        for (int e = 0; e < 4; ++e) { xf[4 * q4 + sp][2 * e] = bflo(t[sp][e]); xf[4 * q4 + sp][2 * e + 1] = bfhi(t[sp][e]); }
        float(&f)[8] = xf[4 * q4 + sp];
        asm volatile("" : "+v"(f[0]), "+v"(f[1]), "+v"(f[2]), "+v"(f[3]), "+v"(f[4]), "+v"(f[5]), "+v"(f[6]), "+v"(f[7]));
      }
      SB();
    }
    PIN(tagv);
    PIN(cpf);
    tagl = tagv + 1u;
    STAMP(22);
    if (kh == 1) {
      v4u gr[4];
      v2u gs;
      PRIO(3);
      gs = ld64c(rs_ws, c32 * 8, WS_ROWST + (128 * m2 + 32 * tg) * 8);
#pragma unroll
      for (int n = 0; n < 4; ++n) gr[n] = ld128c(rs_ws, lane * 16, WS_GRAN + (n & 1) * 24576 + (tg * 2 + (n >> 1)) * 1024);
      PRIO(0);
      K_ISSUE(0); K_ISSUE(1); K_ISSUE(2); K_ISSUE(3); K_ISSUE(4); K_ISSUE(5);
      asm volatile("s_waitcnt vmcnt(6)" : "+v"(gs), "+v"(gr[0]), "+v"(gr[1]), "+v"(gr[2]), "+v"(gr[3])::"memory");
      for (u32 spins = 0;; ++spins) {
        bool ok = gs.y == tagl;
#pragma unroll
        for (int n = 0; n < 4; ++n) ok = ok && gr[n].y == tagl && gr[n].w == tagl;
        if (__all(ok) || spins >= SPIN_CAP) break;
        __builtin_amdgcn_s_sleep(4);
        gs = ld64c(rs_ws, c32 * 8, WS_ROWST + (128 * m2 + 32 * tg) * 8);
#pragma unroll
        for (int n = 0; n < 4; ++n) gr[n] = ld128c(rs_ws, lane * 16, WS_GRAN + (n & 1) * 24576 + (tg * 2 + (n >> 1)) * 1024);
        asm volatile("s_waitcnt vmcnt(0)" : "+v"(gs), "+v"(gr[0]), "+v"(gr[1]), "+v"(gr[2]), "+v"(gr[3])::"memory");
      }
      *(LAS u32*)(size_t)(LB + L_STATX + (tg * 32 + c32) * 4) = gs.x;
      const float one = tg >= 2 ? 1.0f : 0.0f;
#pragma unroll
      for (int n = 0; n < 2; ++n) {
        const u32 p = pk2(asf(gr[2 * n].x) + asf(gr[2 * n + 1].x), asf(gr[2 * n].z) + asf(gr[2 * n + 1].z));
        const u32 q = pk2(bflo(p) + one, bfhi(p) + one);
        sts64(LB + L_CS + tg * 1024 + (((2 * n + hh) ^ rot) << 8) + c32 * 8, v2u{q << 16, q & 0xffff0000u});
      }
    }
    BAR();
    STAMP(2);
    if (p4 && kh == 0 && tg != 0) {
      const v4u rs_g = mk_srd(wg, D_ * F_ * 2), rs_u = mk_srd(wu, D_ * F_ * 2);
      const u32 P = (tg - 1) * 64 + lane, gvo = (P / 12) * (F_ * 2) + (((P % 12) ^ (((P / 12) >> 2) & 1)) << 4);
      const u32 gso = 32 * m2 * (F_ * 2) + j2 * 192, gld = LB + tg * 1024 - 1024;
      gldsi<0, 0>(rs_g, gvo, gso, gld); gldsi<16 * F_ * 2, 3072>(rs_g, gvo, gso, gld);
      gldsi<0, 6144>(rs_u, gvo ^ 32, gso, gld); gldsi<16 * F_ * 2, 9216>(rs_u, gvo ^ 32, gso, gld);
    }
    const u32 st = *(const LAS u32*)(size_t)(LB + L_STATX + (tg * 32 + c32) * 4);
    const float r = __builtin_amdgcn_rcpf(bfhi(st)), mr = -bflo(st) * r;
    sfor<0, 16>([&](auto ic) AI {
      constexpr int s = ic.v;
#pragma unroll
      for (int e = 0; e < 8; ++e) asm volatile("v_fma_f32 %0, %0, %1, %2" : "+v"(xf[s][e]) : "v"(r), "v"(mr));
      if constexpr ((s & 3) == 3) { if (kh == 1) K_ISSUE(6 + (s >> 2)); }
    });
  }
  const u32 tag = __builtin_amdgcn_readfirstlane(tagl);
  f32x16 ak, av;
  v4u hf[16];
#pragma unroll
  for (int i = 0; i < 16; ++i) { ak[i] = 0.f; av[i] = 0.f; }
  const u32 wlane = (8 * hh + tq) * 64 + (g4 & 1) * 32 + tp * 8;
  {
    u32 wa = LB + L_B + kh * 32768 + wlane, wv = (wa + 16384) ^ (kh * 32);
    u32 ca = LB + L_CS + (256 * kh + 8 * hh) * 4, cda = ca + 2048 + (l15 >> 3) * 64 + (l15 & 7) * 4;
    PIN(wa); PIN(wv); PIN(ca); PIN(cda);
    SB();
    WAITV(6);
    BAR();
    STAMP(3);
    float c1[8];
#pragma unroll
    for (int r = 0; r < 8; ++r) c1[r] = *(const LAS float*)(size_t)(cda + r * 128);
    f32x4 hs[2][2];
    v4u fw[2][2], gt0, gt1;
#define H_READ(s) do { hs[(s) & 1][0] = __builtin_bit_cast(f32x4, lds128(ca + (s) * 64)); hs[(s) & 1][1] = __builtin_bit_cast(f32x4, lds128(ca + (s) * 64 + 16)); } while (0)
#define W_READ(s, p) do { const u32 ub_ = ((p) ? wv : wa) + (s) * 1024; fw[(s) & 1][p] = cat(trd(ub_), trd(ub_ + 256)); } while (0)
#define H_FMA(s, hf_) do { float t0_ = hs[(s) & 1][hf_][0], t1_ = hs[(s) & 1][hf_][1], t2_ = hs[(s) & 1][hf_][2], t3_ = hs[(s) & 1][hf_][3]; \
    fmac_bc<8 * ((s) & 1) + 4 * (hf_) + 0>(t0_, c1[(s) >> 1], xf[s][4 * (hf_) + 0]); fmac_bc<8 * ((s) & 1) + 4 * (hf_) + 1>(t1_, c1[(s) >> 1], xf[s][4 * (hf_) + 1]); \
    fmac_bc<8 * ((s) & 1) + 4 * (hf_) + 2>(t2_, c1[(s) >> 1], xf[s][4 * (hf_) + 2]); fmac_bc<8 * ((s) & 1) + 4 * (hf_) + 3>(t3_, c1[(s) >> 1], xf[s][4 * (hf_) + 3]); \
    u32 p0_ = pk2(t0_, t1_), p1_ = pk2(t2_, t3_); PIN(p0_); PIN(p1_); \
    hf[s][2 * (hf_)] = p0_; hf[s][2 * (hf_) + 1] = p1_; } while (0)
    H_READ(0); W_READ(0, 0); W_READ(0, 1); H_READ(1);
    SB();
    H_FMA(0, 0); H_FMA(0, 1);
    SB();
    sfor<0, 16>([&](auto ic) AI {
      constexpr int s = ic.v;
      if constexpr (s == 3) { WAITV(5); RBAR(); }
      if constexpr (s == 7) { WAITV(4); RBAR(); }
      if constexpr (s == 11) { WAITV(0); RBAR(); }
      if constexpr (s == 14) RBAR();
      if constexpr (s == 11) {
        const u32 ga = LB + (4 * g4 + tq) * 192 + (tp >> 1) * 6144 + 8 * (tp & 1) + ((un0 ^ (g4 & 1) ^ (tp & 2)) << 4);
        gt0 = cat(trd(ga), trd(ga + 3072));
        gt1 = cat(trd(ga ^ 16), trd((ga ^ 16) + 3072));
      }
      if constexpr (s == 13) {
        if (p4) {
          const u32 so = WS_GT + (j2 * 12 + un0) * 16384 + m2 * 1024;
          st128c(gt0, rs_ws, lane * 16, so);
          if (kh == 0) st128c(gt1, rs_ws, lane * 16, so + 16384);
        }
      }
      if constexpr (s < 15) { W_READ(s + 1, 0); W_READ(s + 1, 1); }
      SB();
      ak = mfma32(fw[s & 1][0], hf[s], ak);
      SB();
      if constexpr (s < 15) H_FMA((s + 1) & 15, 0);
      SB();
      av = mfma32(fw[s & 1][1], hf[s], av);
      SB();
      if constexpr (s < 15) H_FMA((s + 1) & 15, 1);
      if constexpr (s < 14) H_READ((s + 2) & 15);
      if constexpr (s < 6) { if (kh == 1) K_ISSUE(s + 10); }
      if constexpr (s >= 12) { if (kh == 0) { Q_ISSUE(2 * (s - 12)); Q_ISSUE(2 * (s - 12) + 1); } }
      SB();
    });
  }
  STAMP(4);
  const v4u rs_wo = mk_srd(wo, D_ * D_ * 2);
  u32 dvo;
  {
    const u32 r8 = lane >> 3, pc = lane & 7;
    const u32 f = ((r8 >> 1) & 1) | ((w & 1) << 1);
    dvo = r8 * 1024 + ((((pc >> 1) ^ f) * 2 + (pc & 1)) << 4);
  }
#define SIGNAL(cnt) __hip_atomic_fetch_add(ctl + (WS_CNT + (cnt) * CNT_STRIDE) / 4, 1u, RLX_AGENT)
  v4u wf[2][16];
  const u32 wfo = WS_GT + (j2 * 12 + un0) * 16384;
#define WF_GET(n, lo, hi) _Pragma("unroll") for (int k_ = (lo); k_ < (hi); ++k_) wf[n][k_] = ld128c(rs_ws, lane * 16, wfo + (n) * 16384 + k_ * 1024)
  f32x16 zero;
#pragma unroll
  for (int i = 0; i < 16; ++i) zero[i] = 0.f;
  v4u pf0, pf1;
  {
    const u32 eo = LB + L_EX + w * 6144, ep = LB + L_EX + (w ^ 4) * 6144;
    ex_put(eo + lane * 16, ak);
#pragma unroll
    for (int i = 0; i < 2; ++i) {
      const f32x4 c = {av[8 + 4 * i], av[9 + 4 * i], av[10 + 4 * i], av[11 + 4 * i]};
      sts128(eo + 4096 + i * 1024 + lane * 16, __builtin_bit_cast(v4u, c));
    }
    const u32 pa = LB + L_WPHI + (tp & 1) * 512 + (4 * hh + tq) * 32 + (8 * (g4 & 1) + 4 * (tp >> 1)) * 2;
    const v4u pf = cat(trd(pa), trd(pa + 256));
    u32 m1 = 0u - ((lane >> 2) & 1);
    PIN(m1);
    pf1 = pf & m1; pf0 = pf ^ pf1;
    const u32 sg = kh ? 0x80008000u : 0u;
    const v4u sg4 = v4u{sg, sg, sg, sg};
    BAR();
    ex_add(ep + lane * 16, ak);
    const f32x4 v0 = __builtin_bit_cast(f32x4, lds128(ep + 4096 + lane * 16)), v1 = __builtin_bit_cast(f32x4, lds128(ep + 5120 + lane * 16));
    f32x16 p = mfma32(pf0 ^ sg4, pack8(ak, 0), zero);
    p = mfma32(pf1 ^ sg4, pack8(ak, 1), p);
    float vv[8];
#pragma unroll
    for (int e = 0; e < 4; ++e) { vv[e] = av[e] + v0[e]; vv[4 + e] = av[4 + e] + v1[e]; }
    const u32 vrow = ep + 2048 + c32 * 32 + 8 * hh, vx = (c32 >> 2) & 1;
    sts64(vrow + (vx << 4), v2u{pk2(vv[0], vv[1]), pk2(vv[2], vv[3])});
    sts64(vrow + ((vx ^ 1) << 4), v2u{pk2(vv[4], vv[5]), pk2(vv[6], vv[7])});
    u32 ph[8];
    softmax16(p, ph);
    const u32 row = ep + c32 * 64;
    sts128(row + (((2 * hh) ^ ((c32 >> 1) & 3)) << 4), v4u{ph[0], ph[1], ph[2], ph[3]});
    sts128(row + (((2 * hh + 1) ^ ((c32 >> 1) & 3)) << 4), v4u{ph[4], ph[5], ph[6], ph[7]});
    BAR();
    STAMP(18);
    if (kh == 0) {
      PRIO(3);
      const int hd = w >> 1, fh = w & 1;
      f32x4 kv = {0.f, 0.f, 0.f, 0.f}, zz = {0.f, 0.f, 0.f, 0.f};
      const v4u ones = v4u{0x3f803f80u, 0x3f803f80u, 0x3f803f80u, 0x3f803f80u};
      v2u a[4][2], bb[4][2];
#pragma unroll
      for (int t = 0; t < 2; ++t) {
        const u32 tok = 8 * g4 + 4 * t + tq;
        u32 aa = LB + L_EX + (fh ^ 1) * 24576 + tok * 64 + (((2 * hd + (tp >> 1)) ^ ((tok >> 1) & 3)) << 4) + 8 * (tp & 1);
        u32 ba = LB + L_EX + (hd ^ 1) * 24576 + 2048 + tok * 32 + (((tp >> 1) ^ t) << 4) + 8 * (tp & 1);
        PIN(aa); PIN(ba);
#pragma unroll
        for (int ks = 0; ks < 4; ++ks) { a[ks][t] = trd(aa + ks * 6144); bb[ks][t] = trd(ba + ks * 6144); }
      }
      SB();
#pragma unroll
      for (int ks = 0; ks < 4; ++ks) {
        kv = mfma16(cat(a[ks][0], a[ks][1]), cat(bb[ks][0], bb[ks][1]), kv);
        zz = mfma16(cat(a[ks][0], a[ks][1]), ones, zz);
      }
      st64c(v2u{pk2(kv[0], kv[1]), pk2(kv[2], kv[3])}, rs_ws, lane * 8, WS_SLAB + ((j2 * 4 + w) * 16 + m2) * 512);
      if (l15 == 0) st64c(v2u{pk2(zz[0], zz[1]), pk2(zz[2], zz[3])}, rs_ws, g4 * 8, WS_Z + ((j2 * 4 + w) * 16 + m2) * 32);
      WAITV(0);
      PRIO(0);
    } else {
      const u32 so = tg * 8192 + cb * 128, ld = LB + L_WO + tg * 1024;
      sfor<0, 12>([&](auto ic) AI { gldsi<ic.v * 32768, ic.v * 4096>(rs_wo, dvo, so, ld); });
      WAITV(12);
    }
    BAR();
    if (tid == 64) SIGNAL(0 * 16 + j2);
  }
  STAMP(5);
  const u32 orow = (128 * pnl + 64 * rh + 16 * tg + l15) * 1024 + (64 * cb + 32 * kh + 16 * (g4 & 1) + 8 * (g4 >> 1)) * 2;
  v4u xres;
  u32 phq[16];
  {
    f32x16 aq = zero;
    u32 qa = LB + L_T + kh * 1024 + wlane, qb = LB + kh * 1024 + wlane;
    PIN(qa); PIN(qb);
#define Q_READ(s) ((s) < 12 ? cat(trd(qa + (s) * 2048), trd(qa + (s) * 2048 + 256)) : cat(trd(qb + wq_slot(s)), trd(qb + wq_slot(s) + 256)))
    v4u fq[4];
#pragma unroll
    for (int s = 0; s < 3; ++s) fq[s] = Q_READ(s);
    const u32 so = tg * 8192 + cb * 128, ld = LB + L_WO + tg * 1024;
    xres = ld128(rs_x, orow, 0);
    sfor<0, 16>([&](auto ic) AI {
      constexpr int s = ic.v;
      if constexpr (s + 3 < 16) fq[(s + 3) & 3] = Q_READ(s + 3);
      if constexpr (s < 4) { if (kh == 1) gldsi<(12 + s) * 32768, (12 + s) * 4096>(rs_wo, dvo, so, ld); }
      SB();
      aq = mfma32(fq[s & 3], hf[s], aq);
      SB();
    });
    const u32 ea = LB + L_EX + tg * 4096 + lane * 16;
    if (kh == 0) ex_put(ea, aq);
    if (w == 0) { PRIO(3); poll_cnt(ctl + (WS_CNT + (0 * 16 + j2) * CNT_STRIDE) / 4, 16u); PRIO(0); }
    BAR();
    STAMP(6);
    if (kh == 1) {
      ex_add(ea, aq);
      if (lane < 16) *(LAS u32*)(size_t)(LB + L_KVT + 34 * 64 + lane * 4) = 0u;
      WF_GET(0, 0, 8);
      f32x16 p = mfma32(pf0, pack8(aq, 0), zero);
      p = mfma32(pf1, pack8(aq, 1), p);
      hedgehog(p, phq);
    }
  }
  if (kh == 0) {
    PRIO(3);
    v4u sl[8];
    v2u zl = ld64c(rs_ws, lane * 8, WS_Z + (j2 * 4 + w) * 512);
#pragma unroll
    for (int m = 0; m < 8; ++m) sl[m] = ld128c(rs_ws, lane * 16, WS_SLAB + ((j2 * 4 + w) * 8 + m) * 1024);
    asm volatile("s_waitcnt vmcnt(8)" : "+v"(zl)::"memory");
    const int hd = w >> 1, fh = w & 1;
    {
      auto r = __builtin_amdgcn_permlane32_swap(zl.x, zl.y, false, false);
      float z0 = bflo(r[0]) + bflo(r[1]), z1 = bfhi(r[0]) + bfhi(r[1]);
      z0 = sum16(sum8(sum4(z0))); z1 = sum16(sum8(sum4(z1)));
      *(LAS u32*)(size_t)(LB + L_KVT + (hd * 17 + 16) * 64 + (16 * fh + 4 * tp + 2 * hh) * 2) = pk2(z0, z1);
    }
    float sa[8] = {0.f, 0.f, 0.f, 0.f, 0.f, 0.f, 0.f, 0.f};
    sfor<0, 8>([&](auto ic) AI {
      constexpr int m = ic.v;
      asm volatile("s_waitcnt vmcnt(%1)" : "+v"(sl[m]) : "i"(7 - m) : "memory");
#pragma unroll
      for (int e = 0; e < 4; ++e) { sa[2 * e] += bflo(sl[m][e]); sa[2 * e + 1] += bfhi(sl[m][e]); }
    });
#pragma unroll
    for (int e = 0; e < 4; ++e) {
      auto r = __builtin_amdgcn_permlane32_swap(asu(sa[e]), asu(sa[e + 4]), false, false);
      sa[e] = asf(r[0]) + asf(r[1]);
    }
    sts64(LB + L_KVT + (hd * 17 + 2 * (lane & 7) + hh) * 64 + (16 * fh + 4 * (c32 >> 3)) * 2, v2u{pk2(sa[0], sa[1]), pk2(sa[2], sa[3])});
    PRIO(0);
  }
  BAR();
  STAMP(7);
  if (kh == 1) {
    PRIO(3);
    const bool own = ((lane >> 2) & 1) == hh;
    const u32 d = (c32 & 3) + 4 * (c32 >> 3);
    const u32 ka = LB + L_KVT + (own ? hh * 17 + d : 34) * 64, za = LB + L_KVT + (own ? hh * 17 + 16 : 34) * 64;
    f32x16 num = zero, den = zero;
    v4u a[4], z[4];
#pragma unroll
    for (int s = 0; s < 4; ++s) z[s] = lds128(za + s * 16);
#pragma unroll
    for (int s = 0; s < 4; ++s) a[s] = lds128(ka + s * 16);
#pragma unroll
    for (int s = 0; s < 4; ++s) den = mfma32(z[s], v4u{phq[4 * s], phq[4 * s + 1], phq[4 * s + 2], phq[4 * s + 3]}, den);
#pragma unroll
    for (int s = 0; s < 4; ++s) num = mfma32(a[s], v4u{phq[4 * s], phq[4 * s + 1], phq[4 * s + 2], phq[4 * s + 3]}, num);
    const float r = __builtin_amdgcn_rcpf(den[0] + 1e-6f);
    f32x16 o;
#pragma unroll
    for (int i = 0; i < 16; ++i) o[i] = num[i] * r;
    const u32 vo = (c32 >> 4) * 16384 + hh * 512 + l15 * 16;
    const u32 so = WS_O + ((8 * m2 + 2 * tg) * 16 + j2) * 1024;
    st128c(pack8(o, 0), rs_ws, vo, so);
    st128c(pack8(o, 1), rs_ws, vo + 256, so);
    WAITV(0);
    PRIO(0);
  } else {
    WF_GET(0, 0, 8); WF_GET(1, 0, 8);
  }
  RBAR();
  if (tid == 4 * 64) SIGNAL(1 * 16 + m2);
  STAMP(8);
  if (kh == 1) {
    v4u gr[5];
    for (u32 spins = 0;; ++spins) {
#pragma unroll
      for (int n = 0; n < 4; ++n) gr[n] = ld128c(rs_ws, lane * 16, WS_GRAN + (n & 1) * 24576 + (12 + tg * 2 + (n >> 1)) * 1024);
      gr[4] = ld128c(rs_ws, c32 * 16 + hh * 24576, WS_GRAN + ((tg & 1) ? 40 + cb : 16 + cb) * 512);
      asm volatile("s_waitcnt vmcnt(0)" : "+v"(gr[0]), "+v"(gr[1]), "+v"(gr[2]), "+v"(gr[3]), "+v"(gr[4])::"memory");
      bool ok = true;
#pragma unroll
      for (int n = 0; n < 5; ++n) ok = ok && gr[n].y == tag && gr[n].w == tag;
      if (__all(ok) || spins >= SPIN_CAP) break;
    }
    const float one = tg >= 2 ? 1.0f : 0.0f;
#pragma unroll
    for (int n = 0; n < 2; ++n) {
      const u32 p = pk2(asf(gr[2 * n].x) + asf(gr[2 * n + 1].x), asf(gr[2 * n].z) + asf(gr[2 * n + 1].z));
      *(LAS u32*)(size_t)(LB + L_VEC + 1024 + (tg * 2 + n) * 256 + lane * 4) = pk2(bflo(p) + one, bfhi(p) + one);
    }
    *(LAS u32*)(size_t)(LB + L_VEC + (tg & 1) * 128 + c32 * 4) = pk2(sum32(asf(gr[4].x)), sum32(asf(gr[4].z)));
  } else if (w == 0) {
    PRIO(3); poll_cnt(ctl + (WS_CNT + (1 * 16 + pnl) * CNT_STRIDE) / 4, 16u); PRIO(0);
  }
  BAR();
  STAMP(9);
  f32x4 cs0 = {0.f, 0.f, 0.f, 0.f}, cs1 = {0.f, 0.f, 0.f, 0.f};
  u32 cva = LB + L_VEC + (l15 < 8 ? 2048 : 1024) + g4 * 8;
#define CS_STEP(ks)                                                                       \
  do {                                                                                    \
    const v4u f_ = cat(lds64(cva + (ks) * 64), lds64(cva + (ks) * 64 + 32));              \
    cs0 = mfma16(wf[0][ks], f_, cs0);                                                     \
    if (w < 4) cs1 = mfma16(wf[1][ks], f_, cs1);                                          \
  } while (0)
  u32 x1p[4];
  u32 wra[4];
#pragma unroll
  for (int c = 0; c < 4; ++c) wra[c] = LB + (8 * g4 + tq) * 128 + tp * 8 + ((c ^ (2 * kh) ^ (((tq >> 1) & 1) | ((g4 & 1) << 1))) << 5);
  {
    v4u of[8];
#pragma unroll
    for (int i = 0; i < 8; ++i) of[i] = ld128c(rs_ws, lane * 16, WS_O + ((8 * pnl + 4 * rh + tg) * 16 + 8 * kh + i) * 1024);
    u32 wo_a[4];
#pragma unroll
    for (int c = 0; c < 4; ++c) { wo_a[c] = wra[c] + L_WO + kh * 32768; PIN(wo_a[c]); }
    f32x4 acc[4];
#pragma unroll
    for (int c = 0; c < 4; ++c) acc[c] = f32x4{0.f, 0.f, 0.f, 0.f};
    v4u fa[3][4];
#pragma unroll
    for (int i = 0; i < 2; ++i)
#pragma unroll
      for (int c = 0; c < 4; ++c) fa[i][c] = cat(trd(wo_a[c] + i * 4096), trd(wo_a[c] + i * 4096 + 512));
#pragma unroll
    for (int i = 0; i < 8; ++i) {
      if (i + 2 < 8) {
#pragma unroll
        for (int c = 0; c < 4; ++c) fa[(i + 2) % 3][c] = cat(trd(wo_a[c] + (i + 2) * 4096), trd(wo_a[c] + (i + 2) * 4096 + 512));
      }
      asm volatile("s_waitcnt vmcnt(%1)" : "+v"(of[i]) : "i"(7 - i) : "memory");
      SB();
#pragma unroll
      for (int c = 0; c < 4; ++c) acc[c] = mfma16(fa[i % 3][c], of[i], acc[c]);
      SB();
    }
    STAMP(10);
    WF_GET(0, 8, 12);
    const u32 ea = LB + L_EX3 + tg * 4096 + lane * 16;
    sts128(ea + kh * 2048, __builtin_bit_cast(v4u, acc[2]));
    sts128(ea + kh * 2048 + 1024, __builtin_bit_cast(v4u, acc[3]));
    const u32 va = LB + L_VEC + (32 * kh + 4 * g4) * 2;
    const v2u ga0 = lds64(va), ga1 = lds64(va + 32), cb0 = lds64(va + 2048 + cb * 128), cb1 = lds64(va + 2048 + cb * 128 + 32);
    PIN(xres);
    auto r0 = __builtin_amdgcn_permlane16_swap(xres.x, xres.z, false, false);
    auto r1 = __builtin_amdgcn_permlane16_swap(xres.y, xres.w, false, false);
    BAR();
    const f32x4 t0 = acc[0] + __builtin_bit_cast(f32x4, lds128(ea + (kh ^ 1) * 2048));
    const f32x4 t1 = acc[1] + __builtin_bit_cast(f32x4, lds128(ea + (kh ^ 1) * 2048 + 1024));
    const float xv[8] = {bflo(r0[0]), bfhi(r0[0]), bflo(r1[0]), bfhi(r1[0]), bflo(r0[1]), bfhi(r0[1]), bflo(r1[1]), bfhi(r1[1])};
    const float gv[8] = {bflo(ga0.x), bfhi(ga0.x), bflo(ga0.y), bfhi(ga0.y), bflo(ga1.x), bfhi(ga1.x), bflo(ga1.y), bfhi(ga1.y)};
    const float cv[8] = {bflo(cb0.x), bfhi(cb0.x), bflo(cb0.y), bfhi(cb0.y), bflo(cb1.x), bfhi(cb1.x), bflo(cb1.y), bfhi(cb1.y)};
    float sum = 0.f, sq = 0.f;
    v4u y;
#pragma unroll
    for (int e = 0; e < 4; ++e) {
      const u32 ap = e < 2 ? pk2(t0[2 * e], t0[2 * e + 1]) : pk2(t1[2 * e - 4], t1[2 * e - 3]);
      const u32 gp = pk2(gv[2 * e] * bflo(ap), gv[2 * e + 1] * bfhi(ap));
      x1p[e] = pk2(xv[2 * e] + bflo(gp), xv[2 * e + 1] + bfhi(gp));
      const float xl = bflo(x1p[e]), xh = bfhi(x1p[e]);
      y[e] = pk2(xl * cv[2 * e], xh * cv[2 * e + 1]);
      sum += xl + xh; sq = __builtin_fmaf(xl, xl, __builtin_fmaf(xh, xh, sq));
    }
    st128c(y, rs_ws, lane * 16, WS_Y1 + ((8 * pnl + 4 * rh + tg) * 16 + 2 * cb + kh) * 1024);
    sum = sum32(sum16(sum)); sq = sum32(sum16(sq));
    if (lane < 16) st64c(v2u{asu(sum), asu(sq)}, rs_ws, lane * 8, WS_STAT + ((8 * pnl + 4 * rh + tg) * 16 + 2 * cb + kh) * 128);
    WAITV(0);
  }
  BAR();
  if (tid == 4 * 64) SIGNAL(2 * 16 + pnl);
  STAMP(11);
  const v4u rs_wd = mk_srd(wd, F_ * D_ * 2);
  const u32 dso = tg * 8192 + cb * 128, dld = LB + tg * 1024;
#define WD_ISSUE(i, h) gldsi<(22 * (h) + (i)) * 32768, (2 * wd_sg(i) + (h)) * 4096>(rs_wd, dvo, dso, dld)
#define WD_ISSUE_K(i) gldsi<(i) * 32768, 2 * wd_sg(i) * 4096>(rs_wd, dvo, dso + kh * (22 * 32768), dld + kh * 4096)
  if (p4) {
    WF_GET(0, 12, 16);
    if (kh == 0) { WF_GET(1, 8, 16); }
#pragma unroll
    for (int ks = 0; ks < 8; ++ks) { PIN(wf[0][ks]); PIN(wf[1][ks]); }
    PIN(cva);
#pragma unroll
    for (int ks = 0; ks < 8; ++ks) CS_STEP(ks);
    WAITV(0);
#pragma unroll
    for (int ks = 8; ks < 16; ++ks) { PIN(wf[0][ks]); PIN(wf[1][ks]); }
    STAMP(21);
    if (w == 7) { PRIO(3); poll_cnt(ctl + (WS_CNT + (2 * 16 + m2) * CNT_STRIDE) / 4, 16u); PRIO(0); }
    BAR();
    STAMP(12);
    u32 ya0 = LB + lane * 16, ya1 = ya0 + 65536;
    PIN(ya0); PIN(ya1);
#define Y_READ(i) lds128(((i) < 64 ? ya0 : ya1) + ((i) & 63) * 1024)
#define CS_TAIL()                                                                                        \
  do {                                                                                                   \
    _Pragma("unroll") for (int ks = 8; ks < 16; ++ks) CS_STEP(ks);                        \
    const u32 pa_ = LB + L_CWSW + un0 * 128 + g4 * 32 + (l15 >> 3) * 16;                                 \
    sts128(pa_, __builtin_bit_cast(v4u, cs0));                                                           \
    if (w < 4) sts128(pa_ + 128, __builtin_bit_cast(v4u, cs1));                                          \
  } while (0)
    auto gu_out = [&](const f32x4 (&ac)[8], int un) AI {
      const f32x4 cwn = __builtin_bit_cast(f32x4, lds128(LB + L_CWSW + un * 128 + g4 * 32));
      const f32x4 swn = __builtin_bit_cast(f32x4, lds128(LB + L_CWSW + un * 128 + g4 * 32 + 16));
      u32 hp[4][2];
#pragma unroll
      for (int tp2 = 0; tp2 < 4; ++tp2) {
        const v2u sa = lds64(LB + L_STAT2 + (2 * tp2) * 128 + l15 * 8), sb = lds64(LB + L_STAT2 + (2 * tp2 + 1) * 128 + l15 * 8);
        const float ma = asf(sa.x), da = asf(sa.y), mb = asf(sb.x), db = asf(sb.y);
        float pr[4];
#pragma unroll
        for (int e = 0; e < 4; ++e) {
          const float va = __builtin_fmaf(__builtin_fmaf(ma, cwn[e], ac[2 * tp2][e]), da, swn[e]);
          const float vb = __builtin_fmaf(__builtin_fmaf(mb, cwn[e], ac[2 * tp2 + 1][e]), db, swn[e]);
          auto r = __builtin_amdgcn_permlane32_swap(asu(va), asu(vb), false, false);
          pr[e] = asf(r[0]) * asf(r[1]);
        }
        hp[tp2][0] = pk2(pr[0], pr[1]); hp[tp2][1] = pk2(pr[2], pr[3]);
      }
      const int ug = 12 * j2 + un;
      if (ug < 172) {
#pragma unroll
        for (int qd = 0; qd < 2; ++qd) {
          auto r0 = __builtin_amdgcn_permlane16_swap(hp[2 * qd][0], hp[2 * qd + 1][0], false, false);
          auto r1 = __builtin_amdgcn_permlane16_swap(hp[2 * qd][1], hp[2 * qd + 1][1], false, false);
          const u32 tile = 2 * (g4 & 1) + (g4 >> 1);
          st128c(v4u{r0[0], r1[0], r0[1], r1[1]}, rs_ws, tile * (43 * 1024) + l15 * 16,
                 WS_HH + (8 * m2 + 4 * qd) * (43 * 1024) + (ug >> 2) * 1024 + (ug & 3) * 256);
        }
      }
    };
    constexpr int PD = 5, RING = 6;
    if (w >= 4) {
      f32x4 acc0[8];
#pragma unroll
      for (int t = 0; t < 8; ++t) acc0[t] = f32x4{0.f, 0.f, 0.f, 0.f};
      v4u yl[24], yr[RING];
#define Y_GET(j)                                                                                                                     \
  _Pragma("unroll") for (int i = 0; i < 4; ++i)                                                                                      \
      yl[4 * ((j) % 6) + i] = ld128c(rs_ws, lane * 16, WS_Y1 + ((8 * m2 + 2 * tg + (i & 1)) * 16 + 2 * (j) + (i >> 1)) * 1024)
#define Y_PUT1(n, N)                                                                                                  \
  do {                                                                                                                \
    asm volatile("s_waitcnt vmcnt(%1)" : "+v"(yl[(n) % 24]) : "i"(N) : "memory");                                     \
    sts128(((n) < 16 ? yw0 : yw1) + ((((n) & 15) >> 1) * 8 + ((n) & 1)) * 1024, yl[(n) % 24]);                        \
  } while (0)
      u32 yw0 = ya0 + tg * 2048, yw1 = ya1 + tg * 2048;
      PIN(yw0); PIN(yw1);
      PRIO(2);
      Y_GET(0); Y_GET(1); Y_GET(2);
      CS_TAIL();
      Y_PUT1(0, 11); Y_PUT1(1, 10); Y_PUT1(2, 9); Y_PUT1(3, 8);
      BAR();
      STAMP(19);
#pragma unroll
      for (int i = 0; i < PD; ++i) yr[i] = Y_READ(i);
      Y_GET(3);
      sfor<0, 128>([&](auto ic) AI {
        constexpr int i = ic.v, rd = i + PD;
        if constexpr (rd < 128) {
          if constexpr ((rd & 15) == 0) RBAR();
          if constexpr (rd == 64) STAMP(20);
          yr[rd % RING] = Y_READ(rd);
        }
        if constexpr (i == 4 || i == 8 || i == 14 || i == 22) { constexpr int jj = i == 4 ? 4 : (i == 8 ? 5 : (i == 14 ? 6 : 7)); Y_GET(jj); }
        if constexpr (y_put(i) >= 0) {
          static_assert(rd + 6 <= 16 * (y_put(i) >> 2), "piece must be in LDS a few reads before its barrier");
          Y_PUT1(y_put(i), y_issued(i) - 1 - y_put(i));
        }
        if constexpr (i == 54) PRIO(0);
        if constexpr (wd_slot(rd) >= 0) {
          static_assert((wd_slot(rd) >> 1) - 3 <= 2 * (rd >> 4) - 2, "slot still holds activations");
          WD_ISSUE(wd_slot(rd) >> 1, wd_slot(rd) & 1);
        }
        SB();
        acc0[i & 7] = mfma16(wf[0][i >> 3], yr[i % RING], acc0[i & 7]);
        SB();
      });
      STAMP(13);
      gu_out(acc0, un0);
#pragma unroll
      for (int ks = 0; ks < 16; ++ks) { asm volatile("" : "=v"(wf[0][ks])); asm volatile("" : "=v"(wf[1][ks])); }
      asm volatile("" : "=v"(cs0), "=v"(cs1));
    }
    int w2 = w;
    asm volatile("" : "+s"(w2));
    if (w2 < 4) {
      f32x4 acc0[8], acc1[8];
#pragma unroll
      for (int t = 0; t < 8; ++t) { acc0[t] = f32x4{0.f, 0.f, 0.f, 0.f}; acc1[t] = f32x4{0.f, 0.f, 0.f, 0.f}; }
      v4u st[4], yr[RING];
#pragma unroll
      for (int n = 0; n < 4; ++n) st[n] = ld128c(rs_ws, lane * 16, WS_STAT + (8 * m2 + 2 * w) * 2048 + n * 1024);
      CS_TAIL();
      asm volatile("s_waitcnt vmcnt(0)" : "+v"(st[0]), "+v"(st[1]), "+v"(st[2]), "+v"(st[3])::"memory");
      {
        const f32x4 t0 = __builtin_bit_cast(f32x4, st[0]) + __builtin_bit_cast(f32x4, st[1]);
        const f32x4 t1 = __builtin_bit_cast(f32x4, st[2]) + __builtin_bit_cast(f32x4, st[3]);
        float t[4], q[2];
#pragma unroll
        for (int e = 0; e < 4; ++e) {
          auto r = __builtin_amdgcn_permlane32_swap(asu(t0[e]), asu(t1[e]), false, false);
          t[e] = asf(r[0]) + asf(r[1]);
        }
#pragma unroll
        for (int e = 0; e < 2; ++e) {
          auto r = __builtin_amdgcn_permlane16_swap(asu(t[e]), asu(t[e + 2]), false, false);
          q[e] = sum8(asf(r[0]) + asf(r[1]));
        }
        float m0, d0;
        ln_stats(q[0], q[1], m0, d0);
        sts64(LB + L_STAT2 + (2 * w + hh) * 128 + (lane & 7) * 16 + (g4 & 1) * 8, v2u{asu(-m0), asu(__builtin_amdgcn_rcpf(d0))});
      }
      BAR();
      STAMP(19);
#pragma unroll
      for (int i = 0; i < PD; ++i) yr[i] = Y_READ(i);
      sfor<0, 128>([&](auto ic) AI {
        constexpr int i = ic.v, rd = i + PD;
        if constexpr (rd < 128) {
          if constexpr ((rd & 15) == 0) RBAR();
          if constexpr (rd == 64) STAMP(20);
          yr[rd % RING] = Y_READ(rd);
        }
        SB();
        acc0[i & 7] = mfma16(wf[0][i >> 3], yr[i % RING], acc0[i & 7]);
        acc1[i & 7] = mfma16(wf[1][i >> 3], yr[i % RING], acc1[i & 7]);
        SB();
      });
      STAMP(13);
      gu_out(acc0, un0);
      gu_out(acc1, un0 + 1);
    }
    WAITV(0);
    BAR();
    if (tid == 64) __hip_atomic_fetch_add(ctl + (WS_CNT + (3 * 16 + m2) * CNT_STRIDE) / 4, 1u, RLX_AGENT);
  } else {
    sfor<0, 16>([&](auto ic) AI { WD_ISSUE_K(ic.v); });
    WAITV(0);
    BAR();
  }
  STAMP(14);
  {
    WD_ISSUE_K(16); WD_ISSUE_K(17); WD_ISSUE_K(18);
    u32 da0[4], da1[4], da2[4];
#pragma unroll
    for (int c = 0; c < 4; ++c) { da0[c] = wra[c] + kh * 4096; da1[c] = da0[c] + 65536; da2[c] = da0[c] + 131072; PIN(da0[c]); PIN(da1[c]); PIN(da2[c]); }
#define D_ADDR(i, c) (wd_sg(i) < 8 ? da0[c] + wd_sg(i) * 8192 : (wd_sg(i) < 16 ? da1[c] + (wd_sg(i) - 8) * 8192 : da2[c] + (wd_sg(i) - 16) * 8192))
#define D_READ(i, c) cat(trd(D_ADDR(i, c)), trd(D_ADDR(i, c) + 512))
    v4u fa[5][4];
#pragma unroll
    for (int i = 0; i < 5; ++i)
#pragma unroll
      for (int c = 0; c < 4; ++c) fa[i][c] = D_READ(i, c);
    BAR();
    WD_ISSUE_K(19); WD_ISSUE_K(20); WD_ISSUE_K(21);
    if (w == 0) { PRIO(3); poll_cnt(ctl + (WS_CNT + (3 * 16 + pnl) * CNT_STRIDE) / 4, 15u); PRIO(0); }
    WAITV(0);
    RBAR();
    STAMP(15);
    constexpr int HA = 6;
    v4u hf2[HA + 1];
    const u32 hso = WS_HH + (8 * pnl + 4 * rh + tg) * (43 * 1024) + kh * (22 * 1024);
#define H_GET(i) hf2[(i) % (HA + 1)] = ld128c(rs_ws, lane * 16, (i) < 21 ? hso + (i) * 1024 : hso + (21 - kh) * 1024)
#pragma unroll
    for (int i = 0; i < HA; ++i) H_GET(i);
    f32x4 acc[4];
#pragma unroll
    for (int c = 0; c < 4; ++c) acc[c] = f32x4{0.f, 0.f, 0.f, 0.f};
    sfor<0, 22>([&](auto ic) AI {
      constexpr int i = ic.v;
      if constexpr (i + HA < 22) H_GET(i + HA);
      asm volatile("s_waitcnt vmcnt(%1)" : "+v"(hf2[i % (HA + 1)]) : "i"(i + HA < 22 ? HA : 21 - i) : "memory");
      SB();
#pragma unroll
      for (int c = 0; c < 4; ++c) acc[c] = mfma16(fa[i % 5][c], hf2[i % (HA + 1)], acc[c]);
      SB();
      if constexpr (i + 5 < 22) {
#pragma unroll
        for (int c = 0; c < 4; ++c) fa[i % 5][c] = D_READ(i + 5, c);
      }
    });
    STAMP(16);
    const u32 ea = LB + tg * 4096 + lane * 16;
    sts128(ea + kh * 2048, __builtin_bit_cast(v4u, acc[2]));
    sts128(ea + kh * 2048 + 1024, __builtin_bit_cast(v4u, acc[3]));
    const u32 va = LB + L_VEC + 128 + (32 * kh + 4 * g4) * 2;
    const v2u g0 = lds64(va), g1 = lds64(va + 32);
    BAR();
    const f32x4 t0 = acc[0] + __builtin_bit_cast(f32x4, lds128(ea + (kh ^ 1) * 2048));
    const f32x4 t1 = acc[1] + __builtin_bit_cast(f32x4, lds128(ea + (kh ^ 1) * 2048 + 1024));
    const float gv[8] = {bflo(g0.x), bfhi(g0.x), bflo(g0.y), bfhi(g0.y), bflo(g1.x), bfhi(g1.x), bflo(g1.y), bfhi(g1.y)};
    u32 op[4];
#pragma unroll
    for (int e = 0; e < 4; ++e) {
      const u32 ap = e < 2 ? pk2(t0[2 * e], t0[2 * e + 1]) : pk2(t1[2 * e - 4], t1[2 * e - 3]);
      const u32 gp = pk2(gv[2 * e] * bflo(ap), gv[2 * e + 1] * bfhi(ap));
      op[e] = pk2(bflo(x1p[e]) + bflo(gp), bfhi(x1p[e]) + bfhi(gp));
    }
    auto r0 = __builtin_amdgcn_permlane16_swap(op[0], op[2], false, false);
    auto r1 = __builtin_amdgcn_permlane16_swap(op[1], op[3], false, false);
    *(GAS v4u*)((GAS char*)out + orow) = v4u{r0[0], r1[0], r0[1], r1[1]};
  }
  if (b == 0 && tid == 0) __hip_atomic_store(ctl + WS_EPOCH / 4, tag, RLX_AGENT);
}

extern "C" void kernel_launch(void* const* d_in, const int* in_sizes, int n_in, void* d_out, int out_size, void* d_ws, size_t ws_size,
                              hipStream_t stream) {
  (void)in_sizes; (void)n_in; (void)out_size; (void)ws_size;
  (void)hipFuncSetAttribute((const void*)fused_block, hipFuncAttributeMaxDynamicSharedMemorySize, L_TOTAL);
  fused_block<<<256, 512, L_TOTAL, stream>>>((const u16*)d_in[0], (const u16*)d_in[1], (const u16*)d_in[2], (const u16*)d_in[3],
                                             (const u16*)d_in[5], (const u16*)d_in[6], (const u16*)d_in[7], (const u16*)d_in[8],
                                             (const u16*)d_in[9], (u16*)d_out, (char*)d_ws);
}
```

```cpp
#include <hip/hip_runtime.h>

typedef unsigned short u16;
typedef unsigned int u32;
typedef u32 v4u __attribute__((ext_vector_type(4)));
typedef u32 v2u __attribute__((ext_vector_type(2)));
typedef float f32x2 __attribute__((ext_vector_type(2)));
typedef float f32x4 __attribute__((ext_vector_type(4)));
typedef float f32x16 __attribute__((ext_vector_type(16)));
typedef short bf16x8 __attribute__((ext_vector_type(8)));
typedef short s16x4 __attribute__((ext_vector_type(4)));
typedef __bf16 bf16x2 __attribute__((ext_vector_type(2)));
#define GAS __attribute__((address_space(1)))
#define LAS __attribute__((address_space(3)))
#define DI __device__ __forceinline__
#define RLX_AGENT __ATOMIC_RELAXED, __HIP_MEMORY_SCOPE_AGENT
typedef GAS u32 gu32;

constexpr int S_ = 2048, D_ = 512, F_ = 1376;
constexpr int WS_EPOCH = 0, WS_CNT = 983040, CNT_STRIDE = 512;
constexpr int WS_GRAN = 32768, WS_ROWST = 98304, WS_MFLAG = 20480, WS_SLAB = 131072, WS_Z = 655360, WS_STAT = 720896;
constexpr int WS_O = 1 << 20, WS_Y1 = 3 << 20, WS_HH = 5 << 20, WS_GT = 11 << 20, WS_DBG = 15 << 20, WS_BYTES = 16 << 20;
constexpr int L_B = 65536, L_T = 131072, L_MISC = 155648, L_TOTAL = 163840;
constexpr int L_CS = L_MISC, L_VEC = L_MISC, L_RED = L_MISC + 4608, L_STATX = L_MISC + 4096, L_STAT2 = L_MISC + 4096, L_CWSW = L_MISC + 5120;
constexpr int L_ARR = L_MISC + 6912, L_WPHI = L_MISC + 7168;
constexpr int L_KVT = 139264, L_QEX = 147456, L_EX3 = 139264;
constexpr unsigned CODE_SLICES = 20;
constexpr unsigned SPIN_CAP = 1u << 18;

DI float asf(u32 v) { return __builtin_bit_cast(float, v); }
DI u32 asu(float v) { return __builtin_bit_cast(u32, v); }
DI u32 asu(bf16x2 v) { return __builtin_bit_cast(u32, v); }
DI float bf2f(u32 b) { return asf(b << 16); }
DI float bflo(u32 p) { return asf(p << 16); }
DI float bfhi(u32 p) { return asf(p & 0xffff0000u); }
DI u32 pk2(float a, float b) { f32x2 v = {a, b}; return asu(__builtin_convertvector(v, bf16x2)); }
DI float rb(float a) { return bflo(pk2(a, a) & 0xffffu); }
DI v4u mk_srd(const void* p, u32 bytes) {
  unsigned long long a = (unsigned long long)p;
  return v4u{(u32)a, (u32)(a >> 32) & 0xffffu, bytes, 0x00020000u};
}
DI void glds(v4u rs, u32 voff, u32 soff, u32 ldsaddr) {
  asm volatile("s_mov_b32 m0, %3\n\ts_nop 0\n\tbuffer_load_dwordx4 %0, %1, %2 offen lds" ::"v"(voff), "s"(rs), "s"(soff), "s"(ldsaddr) : "memory");
}
template <int SO, int LO> DI void gldsi(v4u rs, u32 voff, u32 soff, u32 ldsaddr) {
  u32 t;
  asm volatile("s_add_u32 m0, %4, %6\n\ts_add_u32 %0, %3, %5\n\tbuffer_load_dwordx4 %1, %2, %0 offen lds"
               : "=&s"(t) : "v"(voff), "s"(rs), "s"(soff), "s"(ldsaddr), "i"(SO), "i"(LO) : "memory", "scc");
}
template <int SO, int LO> DI void gldsic(v4u rs, u32 voff, u32 soff, u32 ldsaddr) {
  u32 t;
  asm volatile("s_add_u32 m0, %4, %6\n\ts_add_u32 %0, %3, %5\n\tbuffer_load_dwordx4 %1, %2, %0 offen sc1 lds"
               : "=&s"(t) : "v"(voff), "s"(rs), "s"(soff), "s"(ldsaddr), "i"(SO), "i"(LO) : "memory", "scc");
}
DI v2u ld64(v4u rs, u32 voff, u32 soff) {
  v2u r;
  asm volatile("buffer_load_dwordx2 %0, %1, %2, %3 offen" : "=v"(r) : "v"(voff), "s"(rs), "s"(soff) : "memory");
  return r;
}
DI v4u ld128(v4u rs, u32 voff, u32 soff) {
  v4u r;
  asm volatile("buffer_load_dwordx4 %0, %1, %2, %3 offen" : "=v"(r) : "v"(voff), "s"(rs), "s"(soff) : "memory");
  return r;
}
DI v4u ld128c(v4u rs, u32 voff, u32 soff) {
  v4u r;
  asm volatile("buffer_load_dwordx4 %0, %1, %2, %3 offen sc1" : "=v"(r) : "v"(voff), "s"(rs), "s"(soff) : "memory");
  return r;
}
DI v2u ld64c(v4u rs, u32 voff, u32 soff) {
  v2u r;
  asm volatile("buffer_load_dwordx2 %0, %1, %2, %3 offen sc1" : "=v"(r) : "v"(voff), "s"(rs), "s"(soff) : "memory");
  return r;
}
DI void st128c(v4u v, v4u rs, u32 voff, u32 soff) {
  asm volatile("buffer_store_dwordx4 %0, %1, %2, %3 offen sc1\n\ts_nop 1" ::"v"(v), "v"(voff), "s"(rs), "s"(soff) : "memory");
}
DI void st64c(v2u v, v4u rs, u32 voff, u32 soff) {
  asm volatile("buffer_store_dwordx2 %0, %1, %2, %3 offen sc1" ::"v"(v), "v"(voff), "s"(rs), "s"(soff) : "memory");
}
#define WAITV(n) asm volatile("s_waitcnt vmcnt(" #n ")" ::: "memory")
#define BAR() asm volatile("s_waitcnt lgkmcnt(0)\n\ts_barrier" ::: "memory")
#define PIN(x) asm volatile("" : "+v"(x))
#define SB() __builtin_amdgcn_sched_barrier(0)
#define RBAR() asm volatile("s_barrier" ::: "memory")
template <int N> DI void waitv() { asm volatile("s_waitcnt vmcnt(%0)" ::"i"(N) : "memory"); }

DI v4u lds128(u32 a) { return *(const LAS v4u*)(size_t)a; }
DI v2u lds64(u32 a) { return *(const LAS v2u*)(size_t)a; }
DI void sts128(u32 a, v4u v) { *(LAS v4u*)(size_t)a = v; }
DI void sts64(u32 a, v2u v) { *(LAS v2u*)(size_t)a = v; }
DI v2u trd(u32 a) { return __builtin_bit_cast(v2u, __builtin_amdgcn_ds_read_tr16_b64_v4i16((LAS s16x4*)(size_t)a)); }
DI v4u cat(v2u a, v2u b) { return v4u{a.x, a.y, b.x, b.y}; }
DI f32x16 mfma32(v4u a, v4u b, f32x16 c) {
  return __builtin_amdgcn_mfma_f32_32x32x16_bf16(__builtin_bit_cast(bf16x8, a), __builtin_bit_cast(bf16x8, b), c, 0, 0, 0);
}
DI f32x4 mfma16(v4u a, v4u b, f32x4 c) {
  return __builtin_amdgcn_mfma_f32_16x16x32_bf16(__builtin_bit_cast(bf16x8, a), __builtin_bit_cast(bf16x8, b), c, 0, 0, 0);
}
DI float sum32(float v) {
  u32 a = asu(v), c = a;
  auto r = __builtin_amdgcn_permlane32_swap(a, c, false, false);
  return asf(r[0]) + asf(r[1]);
}
DI float sum16(float v) {
  u32 a = asu(v), c = a;
  auto r = __builtin_amdgcn_permlane16_swap(a, c, false, false);
  return asf(r[0]) + asf(r[1]);
}
DI float sum8(float v) {
  return v + asf(__builtin_amdgcn_update_dpp(0, __builtin_bit_cast(int, v), 0x128, 0xf, 0xf, false));
}
DI float sum4(float v) {
  return v + asf(__builtin_amdgcn_update_dpp(0, __builtin_bit_cast(int, v), 0x124, 0xf, 0xf, false));
}
DI void ln_stats(float s, float q, float& mean, float& sd) {
  const float m = s * (1.0f / D_);
  float var = q * (1.0f / D_) - m * m;
  var = fmaxf(var, 0.0f);
  mean = rb(m);
  sd = rb(__builtin_amdgcn_sqrtf(rb(rb(var) + 1e-5f)));
}
template <int I> DI void poll_kick(gu32* cnt) {
  unsigned long long ex;
  asm volatile("v_mov_b32 v255, 0\n\ts_mov_b64 %0, exec\n\ts_mov_b64 exec, 1\n\tglobal_atomic_add v%c2, v255, v255, %1 sc0\n\ts_mov_b64 exec, %0"
               : "=&s"(ex) : "s"(cnt), "i"(252 + I) : "memory", "v252", "v253", "v254", "v255");
}
template <unsigned CAP = SPIN_CAP> DI void poll_wait(gu32* cnt, u32 target) {
  unsigned long long ex;
  u32 v, n;
  asm volatile(
      "s_mov_b64 %0, exec\n\ts_mov_b64 exec, 1\n\ts_mov_b32 %2, %5\n"
      "1:\n\t"
      "s_waitcnt vmcnt(2)\n\tv_readfirstlane_b32 %1, v252\n\ts_nop 1\n\ts_cmp_eq_u32 %1, %4\n\ts_cbranch_scc1 2f\n\t"
      "global_atomic_add v252, v255, v255, %3 sc0\n\t"
      "s_waitcnt vmcnt(2)\n\tv_readfirstlane_b32 %1, v253\n\ts_nop 1\n\ts_cmp_eq_u32 %1, %4\n\ts_cbranch_scc1 2f\n\t"
      "global_atomic_add v253, v255, v255, %3 sc0\n\t"
      "s_waitcnt vmcnt(2)\n\tv_readfirstlane_b32 %1, v254\n\ts_nop 1\n\ts_cmp_eq_u32 %1, %4\n\ts_cbranch_scc1 2f\n\t"
      "global_atomic_add v254, v255, v255, %3 sc0\n\t"
      "s_sub_u32 %2, %2, 1\n\ts_cmp_lg_u32 %2, 0\n\ts_cbranch_scc1 1b\n"
      "2:\n\t"
      "s_mov_b64 exec, %0"
      : "=&s"(ex), "=&s"(v), "=&s"(n)
      : "s"(cnt), "s"(target), "i"(CAP)
      : "memory", "scc", "v252", "v253", "v254", "v255");
}
template <unsigned CAP = SPIN_CAP> DI void poll_cnt(gu32* cnt, u32 target) {
  poll_kick<0>(cnt); __builtin_amdgcn_s_sleep(2);
  poll_kick<1>(cnt); __builtin_amdgcn_s_sleep(2);
  poll_kick<2>(cnt);
  poll_wait<CAP>(cnt, target);
}
DI float max16(const f32x16& p) {
  const float a = fmaxf(fmaxf(p[0], p[1]), p[2]), b = fmaxf(fmaxf(p[3], p[4]), p[5]), c = fmaxf(fmaxf(p[6], p[7]), p[8]);
  const float d = fmaxf(fmaxf(p[9], p[10]), p[11]), e = fmaxf(fmaxf(p[12], p[13]), p[14]);
  return fmaxf(fmaxf(fmaxf(a, b), c), fmaxf(fmaxf(d, e), p[15]));
}
DI float min16(const f32x16& p) {
  const float a = fminf(fminf(p[0], p[1]), p[2]), b = fminf(fminf(p[3], p[4]), p[5]), c = fminf(fminf(p[6], p[7]), p[8]);
  const float d = fminf(fminf(p[9], p[10]), p[11]), e = fminf(fminf(p[12], p[13]), p[14]);
  return fminf(fminf(fminf(a, b), c), fminf(fminf(d, e), p[15]));
}
DI void expnorm(const f32x16& p, float l, float a, u32* ph) {
  f32x2 e[8];
#pragma unroll
  for (int i = 0; i < 8; ++i) {
    const f32x2 t = __builtin_elementwise_fma(f32x2{p[2 * i], p[2 * i + 1]}, f32x2{l, l}, f32x2{a, a});
    e[i] = f32x2{__builtin_amdgcn_exp2f(t[0]), __builtin_amdgcn_exp2f(t[1])};
  }
  const f32x2 s = ((e[0] + e[1]) + (e[2] + e[3])) + ((e[4] + e[5]) + (e[6] + e[7]));
  const float r = __builtin_amdgcn_rcpf(s[0] + s[1]);
#pragma unroll
  for (int i = 0; i < 8; ++i) { const f32x2 t = e[i] * f32x2{r, r}; ph[i] = pk2(t[0], t[1]); }
}
constexpr float LOG2E = 1.44269504088896341f;
DI void softmax16(const f32x16& p, u32 (&ph)[8]) { expnorm(p, LOG2E, -max16(p) * LOG2E, ph); }
DI void hedgehog(const f32x16& p, u32 (&ph)[16]) {
  expnorm(p, LOG2E, -max16(p) * LOG2E, ph);
  expnorm(p, -LOG2E, min16(p) * LOG2E, ph + 8);
}
DI v4u pack8(const f32x16& x, int s) {
  return v4u{pk2(x[8 * s], x[8 * s + 1]), pk2(x[8 * s + 2], x[8 * s + 3]), pk2(x[8 * s + 4], x[8 * s + 5]), pk2(x[8 * s + 6], x[8 * s + 7])};
}
DI void ex_put(u32 a, const f32x16& x) {
#pragma unroll
  for (int i = 0; i < 4; ++i) sts128(a + i * 1024, __builtin_bit_cast(v4u, f32x4{x[4 * i], x[4 * i + 1], x[4 * i + 2], x[4 * i + 3]}));
}
DI void ex_add(u32 a, f32x16& x) {
#pragma unroll
  for (int i = 0; i < 4; ++i) {
    const f32x4 t = __builtin_bit_cast(f32x4, lds128(a + i * 1024));
    x[4 * i] += t[0]; x[4 * i + 1] += t[1]; x[4 * i + 2] += t[2]; x[4 * i + 3] += t[3];
  }
}

#define STAMP(i) do {} while (0)

template <int I> struct IC { static constexpr int v = I; };
template <int I, int N, class Fn> DI void sfor(Fn&& f) {
  if constexpr (I < N) { f(IC<I>{}); sfor<I + 1, N>(f); }
}
#define AI __attribute__((always_inline))
#define PRIO(n) __builtin_amdgcn_s_setprio(n)
constexpr int wd_slot(int i) { return (i >= 24 && i < 120 && (i - 24) % 3 == 0) ? (i - 24) / 3 : -1; }
constexpr int ops_before(int i) { return i <= 24 ? 12 + i : (i >= 118 ? 68 : 36 + (i - 24 + 2) / 3); }
constexpr int wd_sg(int i) { return i < 3 ? 16 + i : i - 3; }
template <int N> DI float bcast(float v) { return asf(__builtin_amdgcn_mov_dpp((int)asu(v), 0x150 + N, 0xf, 0xf, true)); }
DI f32x2 unp(u32 p) { return f32x2{bflo(p), bfhi(p)}; }
DI f32x2 fma2(f32x2 a, f32x2 b, f32x2 c) { return __builtin_elementwise_fma(a, b, c); }
DI void mfma16_ip(f32x4& c, v4u a, v4u b) { asm volatile("v_mfma_f32_16x16x32_bf16 %0, %1, %2, %0" : "+v"(c) : "v"(a), "v"(b)); }
DI void mfma16_z(f32x4& c, v4u a, v4u b) { asm volatile("v_mfma_f32_16x16x32_bf16 %0, %1, %2, 0" : "=&v"(c) : "v"(a), "v"(b)); }
DI void pinf(f32x4& v) { PIN(v); }
DI void pin4(v4u& v) { PIN(v); }
DI void fma2_ip(f32x2& x, f32x2 a, f32x2 c) { asm volatile("v_pk_fma_f32 %0, %0, %1, %2" : "+v"(x) : "v"(a), "v"(c)); }
DI void mfma32_ip(f32x16& c, v4u a, v4u b) { asm volatile("v_mfma_f32_32x32x16_bf16 %0, %1, %2, %0" : "+v"(c) : "v"(a), "v"(b)); }
template <int N> DI void fmac_bc(float& h, float c, float x) {
  asm("v_fmac_f32_dpp %0, %1, %2 row_newbcast:%3 row_mask:0xf bank_mask:0xf" : "+v"(h) : "v"(c), "v"(x), "i"(N));
}

__global__ void __launch_bounds__(512, 2) __attribute__((amdgpu_num_vgpr(248)))
fused_block(const u16* __restrict__ x, const u16* __restrict__ wqkv, const u16* __restrict__ wphi, const u16* __restrict__ wo,
            const u16* __restrict__ wg, const u16* __restrict__ wu, const u16* __restrict__ wd, const u16* __restrict__ cvec,
            const u16* __restrict__ wmod, u16* __restrict__ out, char* __restrict__ ws) {
  extern __shared__ __attribute__((aligned(16))) char smem[];
  const u32 LB = (u32)(size_t)(LAS char*)smem;
  STAMP(0);
  u32 cpf;
  {
    unsigned long long pc;
    u32 sl = ((blockIdx.x >> 3) % CODE_SLICES) * 2048u, off;
    asm volatile(
        "s_getpc_b64 %2\n\tv_lshlrev_b32 %1, 7, %3\n\tv_cmp_lt_u32 vcc, 63, %3\n\tv_mov_b32 %0, %4\n\ts_nop 0\n\tv_cndmask_b32 %0, 0, %0, vcc\n\t"
        "v_add_u32 %1, %1, %0\n\tv_cmp_gt_u32 vcc, 0x50, %3\n\ts_nop 1\n\tv_cndmask_b32 %1, 0, %1, vcc\n\tglobal_load_dword %0, %1, %2"
        : "=&v"(cpf), "=&v"(off), "=&s"(pc) : "v"(threadIdx.x), "s"(sl) : "memory", "vcc");
    SB();
  }
  asm volatile("" ::"s"(x), "s"(wqkv), "s"(wphi), "s"(wo), "s"(wg), "s"(wu), "s"(wd), "s"(cvec), "s"(wmod), "s"(out), "s"(ws));
  const int tid = threadIdx.x, lane = tid & 63;
  int w = __builtin_amdgcn_readfirstlane(tid >> 6);
  asm volatile("s_nop 4" : "+s"(w));
  const int b = blockIdx.x, g = b & 7, bi = b >> 3;
  const int m2 = 4 * (g >> 1) + (bi >> 3), j2 = 8 * (g & 1) + (bi & 7);
  const int pnl = 2 * g + (bi >> 4), rh = (bi >> 3) & 1, cb = bi & 7;
  const v4u rs_x = mk_srd(x, S_ * D_ * 2), rs_ws = mk_srd(ws, WS_BYTES);
  gu32* const ctl = (gu32*)ws;
  const int tg = w & 3, kh = w >> 2;
  const int c32 = lane & 31, hh = lane >> 5;
  const int l15 = lane & 15, g4 = lane >> 4, tq = (lane & 15) >> 2, tp = lane & 3;

  const bool prod = b < 96, sprod = b >= 96 && b < 224;
  const int cg = b >> 1, kp = b & 1;
  const u32 xld = LB + w * 16384;
  v4u xr[4];
  u32 tagv;
  if (w == 0 && b < 4) __hip_atomic_store(ctl + (WS_CNT + (b * 64 + lane) * 128) / 4, 0u, RLX_AGENT);
  asm volatile("buffer_load_dword %0, off, %1, 0 sc1" : "=v"(tagv) : "s"(rs_ws) : "memory");
  v2u mc2;
  if (prod && w == 0) {
    const v4u rs_m = mk_srd(wmod, D_ * 6 * D_ * 2), rs_c = mk_srd(cvec, D_ * 2);
    mc2 = ld64(rs_c, lane * 8, kp * 512);
    const u32 vo = (lane >> 3) * 6144 + (((lane & 7) ^ (((lane >> 4) & 1) << 1)) << 4);
    const u32 so = 256 * kp * 6144 + cg * 128;
    sfor<0, 32>([&](auto ic) AI { gldsi<ic.v * 8 * 6144, (ic.v < 24 ? L_T + ic.v * 1024 : (ic.v - 24) * 1024)>(rs_m, (ic.v & 1) ? vo ^ 64 : vo, so, LB); });
  }
  if (kh == 0 && sprod) {
#pragma unroll
    for (int r = 0; r < 4; ++r) xr[r] = ld128(rs_x, lane * 16, (16 * (b - 96) + 4 * w + r) * 1024);
  }
  if (w == 5) glds(mk_srd(wphi, 32 * 256 * 2), lane * 16, j2 * 1024, LB + L_WPHI);
  if (prod) {
    if (w == 0) {
      asm volatile("s_waitcnt vmcnt(32)" : "+v"(mc2), "+v"(tagv)::"memory");
      float sv[4] = {bflo(mc2.x), bfhi(mc2.x), bflo(mc2.y), bfhi(mc2.y)};
#pragma unroll
      for (int e = 0; e < 4; ++e) sv[e] = sv[e] * __builtin_amdgcn_rcpf(1.0f + __expf(-sv[e]));
      sts64(LB + 8192 + lane * 8, v2u{pk2(sv[0], sv[1]), pk2(sv[2], sv[3])});
    }
    RBAR();
  }
  const u32 xvo0 = (lane >> 3) * 1024 + (((lane & 7) ^ (lane >> 4)) << 4), xvo1 = xvo0 ^ 64;
  const int rot = bi & 3;
  const u32 xso = (128 * m2 + 32 * tg) * 1024 + kh * 512 + rot * 128;
#define X_ISSUE(q4)                                                                       \
  do {                                                                                    \
    gldsi<0, (q4) * 4096>(rs_x, xvo0, xso ^ ((q4) * 128), xld);                           \
    gldsi<8192, (q4) * 4096 + 1024>(rs_x, xvo1, xso ^ ((q4) * 128), xld);                 \
    gldsi<16384, (q4) * 4096 + 2048>(rs_x, xvo0, xso ^ ((q4) * 128), xld);                \
    gldsi<24576, (q4) * 4096 + 3072>(rs_x, xvo1, xso ^ ((q4) * 128), xld);                \
  } while (0)
  if (kh == 0 && sprod) {
    asm volatile("s_waitcnt vmcnt(0)" : "+v"(xr[0]), "+v"(xr[1]), "+v"(xr[2]), "+v"(xr[3]), "+v"(tagv)::"memory");
    float s[4], q[4], t[2];
#pragma unroll
    for (int r = 0; r < 4; ++r) {
      s[r] = 0.f; q[r] = 0.f;
#pragma unroll
      for (int e = 0; e < 4; ++e) {
        const float lo = bflo(xr[r][e]), hi = bfhi(xr[r][e]);
        s[r] += lo + hi; q[r] = __builtin_fmaf(lo, lo, __builtin_fmaf(hi, hi, q[r]));
      }
    }
#pragma unroll
    for (int r = 0; r < 2; ++r) {
      auto rs = __builtin_amdgcn_permlane32_swap(asu(s[r]), asu(s[r + 2]), false, false);
      auto rq = __builtin_amdgcn_permlane32_swap(asu(q[r]), asu(q[r + 2]), false, false);
      s[r] = asf(rs[0]) + asf(rs[1]); q[r] = asf(rq[0]) + asf(rq[1]);
    }
    {
      auto rs = __builtin_amdgcn_permlane16_swap(asu(s[0]), asu(s[1]), false, false);
      auto rq = __builtin_amdgcn_permlane16_swap(asu(q[0]), asu(q[1]), false, false);
      t[0] = asf(rs[0]) + asf(rs[1]); t[1] = asf(rq[0]) + asf(rq[1]);
    }
#pragma unroll
    for (int r = 0; r < 2; ++r) {
      t[r] = sum4(sum8(t[r]));
      t[r] += asf(__builtin_amdgcn_update_dpp(0, (int)asu(t[r]), 0x122, 0xf, 0xf, false));
      t[r] += asf(__builtin_amdgcn_update_dpp(0, (int)asu(t[r]), 0x121, 0xf, 0xf, false));
    }
    float mean, sd;
    ln_stats(t[0], t[1], mean, sd);
    if (l15 == 0) st64c(v2u{pk2(mean, sd), tagv + 1u}, rs_ws, g4 * 8, WS_ROWST + (16 * (b - 96) + 4 * w) * 8);
  }
  if (prod && w == 0) {
    PRIO(3);
    v4u sb[8];
#pragma unroll
    for (int ks = 0; ks < 8; ++ks) sb[ks] = lds128(LB + 8192 + ks * 64 + g4 * 16);
    u32 ma[4], mb[4];
#pragma unroll
    for (int c = 0; c < 4; ++c) {
      mb[c] = LB + (8 * g4 + tq) * 128 + tp * 8 + ((c ^ (((tq >> 1) & 1) | ((g4 & 1) << 1))) << 5); ma[c] = mb[c] + L_T;
      PIN(ma[c]); PIN(mb[c]);
    }
    f32x4 acc[4];
#pragma unroll
    for (int c = 0; c < 4; ++c) acc[c] = f32x4{0.f, 0.f, 0.f, 0.f};
    v4u fm[2][2][4];
#define M_READ(h2) _Pragma("unroll") for (int k2 = 0; k2 < 2; ++k2) _Pragma("unroll") for (int c = 0; c < 4; ++c) {               \
      const u32 ad = (h2) < 3 ? ma[c] + (2 * (h2) + k2) * 4096 : mb[c] + k2 * 4096;                                                \
      fm[(h2) & 1][k2][c] = cat(trd(ad), trd(ad + 512)); }
    WAITV(24);
    M_READ(0);
    sfor<0, 4>([&](auto ic) AI {
      constexpr int h2 = ic.v;
      if constexpr (h2 < 3) { waitv<16 - 8 * h2>(); M_READ(h2 + 1); }
      SB();
#pragma unroll
      for (int k2 = 0; k2 < 2; ++k2)
#pragma unroll
        for (int c = 0; c < 4; ++c) acc[c] = mfma16(sb[2 * h2 + k2], fm[h2 & 1][k2][c], acc[c]);
      SB();
    });
    const float lo = (g4 & 1) ? acc[1][0] : acc[0][0], hi = (g4 & 1) ? acc[3][0] : acc[2][0];
    st64c(v2u{asu(hh ? hi : lo), tagv + 1u}, rs_ws, lane * 8, WS_GRAN + kp * 24576 + cg * 512);
    PRIO(0);
  }
  STAMP(1);
  X_ISSUE(0); X_ISSUE(1); X_ISSUE(2);
  const v4u rs_w = mk_srd(wqkv, D_ * 3 * D_ * 2);
  const u32 wvo = ((lane >> 2) * 1536 + 8 * (lane & 3)) * 2;
  const u32 wso = ((256 * (tg & 1) + 16 * (tg >> 1)) * 1536 + 32 * j2) * 2;
#define W_ISSUE(part, n) gldsi<((n) & 1) * 32 * 3072 + (part) * 1024, (n) * 1024 + ((part) == 0 ? 8192 : 0)>(rs_w, wvo, wso + (((n) >> 1) ^ rot) * (64 * 3072), xld)
  const bool p4 = j2 < 15;
  const int un0 = w < 4 ? 2 * w : 4 + w;
  f32x2 xf[16][4];
  u32 tagl;
  {
    u32 xa[4];
#pragma unroll
    for (int sp = 0; sp < 4; ++sp) { xa[sp] = LB + w * 16384 + c32 * 128 + (((2 * sp + hh) ^ ((c32 >> 1) & 7)) << 4); PIN(xa[sp]); }
    v4u gr[4];
    v2u gs;
#pragma unroll
    for (int q4 = 0; q4 < 4; ++q4) {
      if (q4 == 0) { X_ISSUE(3); WAITV(12); }
      if (q4 == 1) WAITV(8);
      if (q4 == 2) WAITV(4);
      if (q4 == 3) {
        WAITV(0);
        if (kh == 1) {
          PRIO(3);
          gs = ld64c(rs_ws, c32 * 8, WS_ROWST + (128 * m2 + 32 * tg) * 8);
#pragma unroll
          for (int n = 0; n < 4; ++n) gr[n] = ld128c(rs_ws, lane * 16, WS_GRAN + (n & 1) * 24576 + (tg * 2 + (n >> 1)) * 1024);
          PRIO(0);
        }
      }
      v4u t[4];
#pragma unroll
      for (int sp = 0; sp < 4; ++sp) t[sp] = lds128(xa[sp] + q4 * 4096);
#pragma unroll
      for (int sp = 0; sp < 4; ++sp) {
#pragma unroll
        for (int e = 0; e < 4; ++e) xf[4 * q4 + sp][e] = f32x2{bflo(t[sp][e]), bfhi(t[sp][e])};
        f32x2(&f)[4] = xf[4 * q4 + sp];
        asm volatile("" : "+v"(f[0]), "+v"(f[1]), "+v"(f[2]), "+v"(f[3]));
      }
      SB();
    }
    if (kh == 0) {
      if (w == 1) asm volatile("buffer_load_dword v251, %0, %1, %2 offen" ::"v"(lane * 128), "s"(mk_srd(wd, F_ * D_ * 2)), "s"(b * 5504) : "memory", "v251");
      if (w == 2) asm volatile("buffer_load_dword v251, %0, %1, %2 offen" ::"v"(l15 * 128), "s"(mk_srd(wo, D_ * D_ * 2)), "s"(b * 2048) : "memory", "v251");
    }
    PIN(tagv);
    PIN(cpf);
    tagl = tagv + 1u;
    STAMP(22);
    if (kh == 0) sfor<0, 8>([&](auto ic) AI { W_ISSUE(1, ic.v); });
    if (kh == 1) {
      asm volatile("s_waitcnt vmcnt(0)" : "+v"(gs), "+v"(gr[0]), "+v"(gr[1]), "+v"(gr[2]), "+v"(gr[3])::"memory");
      for (u32 spins = 0;; ++spins) {
        bool ok = gs.y == tagl;
#pragma unroll
        for (int n = 0; n < 4; ++n) ok = ok && gr[n].y == tagl && gr[n].w == tagl;
        if (__all(ok) || spins >= SPIN_CAP) break;
        if (spins) __builtin_amdgcn_s_sleep(4);
        gs = ld64c(rs_ws, c32 * 8, WS_ROWST + (128 * m2 + 32 * tg) * 8);
#pragma unroll
        for (int n = 0; n < 4; ++n) gr[n] = ld128c(rs_ws, lane * 16, WS_GRAN + (n & 1) * 24576 + (tg * 2 + (n >> 1)) * 1024);
        asm volatile("s_waitcnt vmcnt(0)" : "+v"(gs), "+v"(gr[0]), "+v"(gr[1]), "+v"(gr[2]), "+v"(gr[3])::"memory");
      }
      *(LAS u32*)(size_t)(LB + L_STATX + (tg * 32 + c32) * 4) = gs.x;
      const float one = tg >= 2 ? 1.0f : 0.0f;
#pragma unroll
      for (int n = 0; n < 2; ++n) {
        const u32 p = pk2(asf(gr[2 * n].x) + asf(gr[2 * n + 1].x), asf(gr[2 * n].z) + asf(gr[2 * n + 1].z));
        const u32 q = pk2(bflo(p) + one, bfhi(p) + one);
        sts64(LB + L_CS + tg * 1024 + (((2 * n + hh) ^ rot) << 8) + c32 * 8, v2u{q << 16, q & 0xffff0000u});
      }
    }
    BAR();
    STAMP(2);
    const u32 st = *(const LAS u32*)(size_t)(LB + L_STATX + (tg * 32 + c32) * 4);
    const float r = __builtin_amdgcn_rcpf(bfhi(st)), mr = -bflo(st) * r;
    const f32x2 r2 = {r, r}, mr2 = {mr, mr};
    if (kh == 1) sfor<0, 4>([&](auto ic) AI { W_ISSUE(2, ic.v); });
#pragma unroll
    for (int s = 0; s < 16; ++s)
#pragma unroll
      for (int e = 0; e < 4; ++e) fma2_ip(xf[s][e], r2, mr2);
  }
  const u32 tag = __builtin_amdgcn_readfirstlane(tagl);
  f32x16 zero;
#pragma unroll
  for (int i = 0; i < 16; ++i) zero[i] = 0.f;
  v4u hf[16];
  u32 wk = LB + kh * 16384 + (8 * hh + tq) * 64 + (g4 & 1) * 32 + tp * 8, wv = wk + L_B;
#define W_FRAG(base, s) cat(trd((base) + ((s) & 1) * 32768 + ((s) >> 1) * 1024), trd((base) + ((s) & 1) * 32768 + ((s) >> 1) * 1024 + 256))
  f32x16 ak = zero;
  {
    u32 ca = LB + L_CS + (256 * kh + 8 * hh) * 4, cda = ca + 2048 + (l15 >> 3) * 64 + (l15 & 7) * 4;
    PIN(wk); PIN(wv); PIN(ca); PIN(cda);
    float c1[8];
#pragma unroll
    for (int r = 0; r < 8; ++r) c1[r] = *(const LAS float*)(size_t)(cda + r * 128);
    f32x4 hs[2][2];
    v4u fw[2];
#define H_READ(s) do { hs[(s) & 1][0] = __builtin_bit_cast(f32x4, lds128(ca + (s) * 64)); hs[(s) & 1][1] = __builtin_bit_cast(f32x4, lds128(ca + (s) * 64 + 16)); } while (0)
#define H_FMA(s, hf_) do { float t0_ = hs[(s) & 1][hf_][0], t1_ = hs[(s) & 1][hf_][1], t2_ = hs[(s) & 1][hf_][2], t3_ = hs[(s) & 1][hf_][3]; \
    fmac_bc<8 * ((s) & 1) + 4 * (hf_) + 0>(t0_, c1[(s) >> 1], xf[s][2 * (hf_) + 0][0]); fmac_bc<8 * ((s) & 1) + 4 * (hf_) + 1>(t1_, c1[(s) >> 1], xf[s][2 * (hf_) + 0][1]); \
    fmac_bc<8 * ((s) & 1) + 4 * (hf_) + 2>(t2_, c1[(s) >> 1], xf[s][2 * (hf_) + 1][0]); fmac_bc<8 * ((s) & 1) + 4 * (hf_) + 3>(t3_, c1[(s) >> 1], xf[s][2 * (hf_) + 1][1]); \
    u32 p0_ = pk2(t0_, t1_), p1_ = pk2(t2_, t3_); PIN(p0_); PIN(p1_); \
    hf[s][2 * (hf_)] = p0_; hf[s][2 * (hf_) + 1] = p1_; } while (0)
    H_READ(0); H_READ(1);
    SB();
    H_FMA(0, 0); H_FMA(0, 1);
    if (kh == 0) WAITV(4);
    else sfor<4, 8>([&](auto ic) AI { W_ISSUE(2, ic.v); });
    RBAR();
    STAMP(3);
    fw[0] = W_FRAG(wk, 0);
    SB();
    sfor<0, 16>([&](auto ic) AI {
      constexpr int s = ic.v;
      if constexpr (s == 7) {
        if (kh == 0) {
          WAITV(0);
          if (p4 && tg != 0) {
            const v4u rs_g = mk_srd(wg, D_ * F_ * 2), rs_u = mk_srd(wu, D_ * F_ * 2);
            const u32 P = (tg - 1) * 64 + lane, gvo = (P / 12) * (F_ * 2) + (((P % 12) ^ (((P / 12) >> 2) & 1)) << 4);
            const u32 gso = 32 * m2 * (F_ * 2) + j2 * 192, gld = LB + L_T + tg * 1024 - 1024;
            gldsi<0, 0>(rs_g, gvo, gso, gld); gldsi<16 * F_ * 2, 3072>(rs_g, gvo, gso, gld);
            gldsi<0, 6144>(rs_u, gvo ^ 32, gso, gld); gldsi<16 * F_ * 2, 9216>(rs_u, gvo ^ 32, gso, gld);
          }
        }
        RBAR();
      }
      if constexpr (s < 15) fw[(s + 1) & 1] = W_FRAG(wk, s + 1);
      if constexpr (s < 14) H_READ((s + 2) & 15);
      SB();
      if constexpr (s < 15) H_FMA((s + 1) & 15, 0);
      SB();
      ak = mfma32(fw[s & 1], hf[s], ak);
      SB();
      if constexpr (s < 15) H_FMA((s + 1) & 15, 1);
      SB();
    });
  }
  STAMP(4);
  const v4u rs_wo = mk_srd(wo, D_ * D_ * 2);
  u32 dvo;
  {
    const u32 r8 = lane >> 3, pc = lane & 7;
    const u32 f = ((r8 >> 1) & 1) | ((w & 1) << 1);
    dvo = r8 * 1024 + ((((pc >> 1) ^ f) * 2 + (pc & 1)) << 4);
  }
#define SIGNAL(wv, cnt) do { if (tid >= (wv) * 64 && tid < (wv) * 64 + 4) __hip_atomic_fetch_add(ctl + (WS_CNT + (cnt) * CNT_STRIDE + lane * 128) / 4, 1u, RLX_AGENT); } while (0)
#define POLL(cnt, rep, target) do { PRIO(3); poll_cnt(ctl + (WS_CNT + (cnt) * CNT_STRIDE + (rep) * 128) / 4, target); PRIO(0); } while (0)
  v4u wf[2][16];
  const u32 wfo = WS_GT + (j2 * 12 + un0) * 16384;
#define WF_GET(n, lo, hi) _Pragma("unroll") for (int k_ = (lo); k_ < (hi); ++k_) wf[n][k_] = ld128c(rs_ws, lane * 16, wfo + (n) * 16384 + k_ * 1024)
#define WO_ISSUE(n) gldsi<(n) * 32768, ((n) >> 1) * 16384 + ((n) & 1) * 4096>(rs_wo, dvo, tg * 8192 + cb * 128, LB + tg * 1024)
  auto proj = [&](u32 base, auto hook) AI {
    f32x16 acc = zero;
    v4u fq[4];
#pragma unroll
    for (int s = 0; s < 3; ++s) fq[s] = W_FRAG(base, s);
    sfor<0, 16>([&](auto ic) AI {
      constexpr int s = ic.v;
      if constexpr (s + 3 < 16) fq[(s + 3) & 3] = W_FRAG(base, s + 3);
      hook(ic);
      SB();
      acc = mfma32(fq[s & 3], hf[s], acc);
      SB();
    });
    return acc;
  };
  v4u pf0, pf1;
  {
    const u32 sc = LB + tg * 16384 + 8192;
    const u32 pa = LB + L_WPHI + (tp & 1) * 512 + (4 * hh + tq) * 32 + (8 * (g4 & 1) + 4 * (tp >> 1)) * 2;
    const v4u pf = cat(trd(pa), trd(pa + 256));
    u32 m1 = 0u - ((lane >> 2) & 1);
    PIN(m1);
    pf1 = pf & m1; pf0 = pf ^ pf1;
    const u32 sg = kh ? 0x80008000u : 0u;
    const v4u pg0 = pf0 ^ v4u{sg, sg, sg, sg}, pg1 = pf1 ^ v4u{sg, sg, sg, sg};
    const v4u k0 = pack8(ak, 0), k1 = pack8(ak, 1);
    sts128(sc + (kh ^ 1) * 2048 + lane * 16, k0);
    sts128(sc + (kh ^ 1) * 2048 + 1024 + lane * 16, k1);
    f32x16 p = mfma32(pg0, k0, zero);
    p = mfma32(pg1, k1, p);
    if (kh == 1) WAITV(0);
    BAR();
    STAMP(23);
    {
      const v4u o0 = lds128(sc + kh * 2048 + lane * 16), o1 = lds128(sc + kh * 2048 + 1024 + lane * 16);
      p = mfma32(pg0, o0, p);
      p = mfma32(pg1, o1, p);
      SB();
    }
    auto feat = [&]() AI {
      u32 ph[8];
      softmax16(p, ph);
      const u32 row = sc + kh * 2048 + c32 * 64;
      sts128(row + (((2 * hh) ^ ((c32 >> 1) & 3)) << 4), v4u{ph[0], ph[1], ph[2], ph[3]});
      sts128(row + (((2 * hh + 1) ^ ((c32 >> 1) & 3)) << 4), v4u{ph[4], ph[5], ph[6], ph[7]});
    };
    if (kh == 0) feat();
    else sfor<0, 8>([&](auto ic) AI { W_ISSUE(0, ic.v); });
    {
      const f32x16 av = proj(wv, [](auto) {});
      const u32 row = sc + 4096 + kh * 2048 + c32 * 64 + 8 * hh;
#pragma unroll
      for (int i = 0; i < 4; ++i)
        sts64(row + ((i ^ ((c32 >> 1) & 3)) << 4), v2u{pk2(av[4 * i], av[4 * i + 1]), pk2(av[4 * i + 2], av[4 * i + 3])});
    }
    if (kh == 1) feat();
    u32 aa[2], ba[2];
#pragma unroll
    for (int t = 0; t < 2; ++t) {
      const u32 tok = 8 * g4 + 4 * t + tq;
      const u32 o = LB + 8192 + tok * 64 + (((2 * (tg >> 1) + (tp >> 1)) ^ ((tok >> 1) & 3)) << 4) + 8 * (tp & 1);
      aa[t] = o + (tg & 1) * 2048; ba[t] = o + 4096;
      PIN(aa[t]); PIN(ba[t]);
    }
    if (kh == 0) WAITV(0);
    BAR();
    STAMP(18);
    if (kh == 0) {
      PRIO(3);
      f32x4 kv0 = {0.f, 0.f, 0.f, 0.f}, kv1 = kv0, zz = kv0;
      const v4u ones = v4u{0x3f803f80u, 0x3f803f80u, 0x3f803f80u, 0x3f803f80u};
      v4u a[4], b0[4], b1[4];
#pragma unroll
      for (int ks = 0; ks < 4; ++ks) {
        a[ks] = cat(trd(aa[0] + ks * 16384), trd(aa[1] + ks * 16384));
        b0[ks] = cat(trd(ba[0] + ks * 16384), trd(ba[1] + ks * 16384));
        b1[ks] = cat(trd(ba[0] + ks * 16384 + 2048), trd(ba[1] + ks * 16384 + 2048));
      }
      SB();
#pragma unroll
      for (int ks = 0; ks < 4; ++ks) {
        kv0 = mfma16(a[ks], b0[ks], kv0); kv1 = mfma16(a[ks], b1[ks], kv1); zz = mfma16(a[ks], ones, zz);
      }
      kv0 += kv1;
      st64c(v2u{pk2(kv0[0], kv0[1]), pk2(kv0[2], kv0[3])}, rs_ws, lane * 8, WS_SLAB + ((j2 * 4 + w) * 16 + m2) * 512);
      if (l15 == 0) st64c(v2u{pk2(zz[0], zz[1]), pk2(zz[2], zz[3])}, rs_ws, g4 * 8, WS_Z + ((j2 * 4 + w) * 16 + m2) * 32);
      sfor<0, 8>([&](auto ic) AI { WO_ISSUE(ic.v); });
      WAITV(8);
      PRIO(0);
    } else {
      const u32 gb = LB + L_T + (4 * g4 + tq) * 192 + 8 * (tp & 1);
      const u32 ga = gb + (((2 * tg + (tp >> 1)) ^ (g4 & 1)) << 4), gc = gb + (tp >> 1) * 6144 + (((8 + tg) ^ (g4 & 1) ^ (tp & 2)) << 4);
      const v4u gt0 = cat(trd(ga), trd(ga + 3072)), gt1 = cat(trd((ga ^ 32) + 6144), trd((ga ^ 32) + 9216)), gt2 = cat(trd(gc), trd(gc + 3072));
      const u32 gto = p4 ? WS_GT + j2 * (12 * 16384) + m2 * 1024 : WS_BYTES;
      st128c(gt0, rs_ws, lane * 16, gto + tg * 32768);
      st128c(gt1, rs_ws, lane * 16, gto + tg * 32768 + 16384);
      st128c(gt2, rs_ws, lane * 16, gto + (8 + tg) * 16384);
      WAITV(0);
    }
    RBAR();
    SIGNAL(1, 0 * 16 + j2);
  }
  STAMP(5);
  const u32 orow = (128 * pnl + 64 * rh + 16 * tg + l15) * 1024 + (64 * cb + 32 * kh + 16 * (g4 & 1) + 8 * (g4 >> 1)) * 2;
  v4u xres;
  u32 phq[16];
  {
    if (kh == 1) sfor<8, 16>([&](auto ic) AI { WO_ISSUE(ic.v); });
    xres = ld128(rs_x, orow, 0);
    gu32* const e2 = ctl + (WS_CNT + (0 * 16 + j2) * CNT_STRIDE + (m2 & 3) * 128) / 4;
    const f32x16 aq = proj(wv + 8192, [&](auto ic) AI {
      if constexpr (ic.v == 7 || ic.v == 11 || ic.v == 15) { if (w == 0) poll_kick<((ic.v - 7) >> 2) & 3>(e2); }
    });
    const v4u q0 = pack8(aq, 0), q1 = pack8(aq, 1);
    const u32 ea = LB + L_QEX + tg * 2048 + lane * 16;
    f32x16 p;
    if (kh == 0) { sts128(ea, q0); sts128(ea + 1024, q1); }
    else {
      p = mfma32(pf0, q0, zero);
      p = mfma32(pf1, q1, p);
      if (lane < 16) *(LAS u32*)(size_t)(LB + L_KVT + 34 * 64 + lane * 4) = 0u;
    }
    if (w == 0) poll_wait(e2, 16u);
    BAR();
    STAMP(6);
    if (kh == 1) {
      const v4u o0 = lds128(ea), o1 = lds128(ea + 1024);
      p = mfma32(pf0, o0, p);
      p = mfma32(pf1, o1, p);
      hedgehog(p, phq);
    }
  }
  if (kh == 0) {
    PRIO(3);
    v4u sl[8];
    v2u zl = ld64c(rs_ws, lane * 8, WS_Z + (j2 * 4 + w) * 512);
#pragma unroll
    for (int m = 0; m < 8; ++m) sl[m] = ld128c(rs_ws, lane * 16, WS_SLAB + ((j2 * 4 + w) * 8 + m) * 1024);
    WF_GET(0, 0, 4); WF_GET(1, 0, 4);
    asm volatile("s_waitcnt vmcnt(16)" : "+v"(zl)::"memory");
    const int hd = w >> 1, fh = w & 1;
    {
      auto r = __builtin_amdgcn_permlane32_swap(zl.x, zl.y, false, false);
      float z0 = bflo(r[0]) + bflo(r[1]), z1 = bfhi(r[0]) + bfhi(r[1]);
      z0 = sum16(sum8(sum4(z0))); z1 = sum16(sum8(sum4(z1)));
      *(LAS u32*)(size_t)(LB + L_KVT + (hd * 17 + 16) * 64 + (16 * fh + 4 * tp + 2 * hh) * 2) = pk2(z0, z1);
    }
    float sa[8] = {0.f, 0.f, 0.f, 0.f, 0.f, 0.f, 0.f, 0.f};
    sfor<0, 8>([&](auto ic) AI {
      constexpr int m = ic.v;
      asm volatile("s_waitcnt vmcnt(%1)" : "+v"(sl[m]) : "i"(15 - m) : "memory");
#pragma unroll
      for (int e = 0; e < 4; ++e) { sa[2 * e] += bflo(sl[m][e]); sa[2 * e + 1] += bfhi(sl[m][e]); }
    });
#pragma unroll
    for (int e = 0; e < 4; ++e) {
      auto r = __builtin_amdgcn_permlane32_swap(asu(sa[e]), asu(sa[e + 4]), false, false);
      sa[e] = asf(r[0]) + asf(r[1]);
    }
    sts64(LB + L_KVT + (hd * 17 + 2 * (lane & 7) + hh) * 64 + (16 * fh + 4 * (c32 >> 3)) * 2, v2u{pk2(sa[0], sa[1]), pk2(sa[2], sa[3])});
    PRIO(0);
  }
  u32 ka, za;
  {
    const bool own = ((lane >> 2) & 1) == hh;
    const u32 d = (c32 & 3) + 4 * (c32 >> 3);
    ka = LB + L_KVT + (own ? hh * 17 + d : 34) * 64; za = LB + L_KVT + (own ? hh * 17 + 16 : 34) * 64;
    PIN(ka); PIN(za);
  }
  BAR();
  STAMP(7);
  if (kh == 1) {
    PRIO(3);
    f32x16 num = zero, den = zero;
    v4u a[4], z[4];
#pragma unroll
    for (int s = 0; s < 4; ++s) z[s] = lds128(za + s * 16);
#pragma unroll
    for (int s = 0; s < 4; ++s) a[s] = lds128(ka + s * 16);
    SB();
#pragma unroll
    for (int s = 0; s < 4; ++s) den = mfma32(z[s], v4u{phq[4 * s], phq[4 * s + 1], phq[4 * s + 2], phq[4 * s + 3]}, den);
    SB();
#pragma unroll
    for (int s = 0; s < 4; ++s) num = mfma32(a[s], v4u{phq[4 * s], phq[4 * s + 1], phq[4 * s + 2], phq[4 * s + 3]}, num);
    const float r = __builtin_amdgcn_rcpf(den[0] + 1e-6f);
    f32x16 o;
#pragma unroll
    for (int i = 0; i < 16; ++i) o[i] = num[i] * r;
    const u32 vo = (c32 >> 4) * 16384 + hh * 512 + l15 * 16;
    const u32 so = WS_O + ((8 * m2 + 2 * tg) * 16 + j2) * 1024;
    st128c(pack8(o, 0), rs_ws, vo, so);
    st128c(pack8(o, 1), rs_ws, vo + 256, so);
    WF_GET(0, 0, 8);
    WAITV(8);
    PRIO(0);
  }
  RBAR();
  SIGNAL(1, 1 * 16 + m2);
  STAMP(8);
  if (kh == 1) {
    v4u gr[5];
    for (u32 spins = 0;; ++spins) {
#pragma unroll
      for (int n = 0; n < 4; ++n) gr[n] = ld128c(rs_ws, lane * 16, WS_GRAN + (n & 1) * 24576 + (12 + tg * 2 + (n >> 1)) * 1024);
      gr[4] = ld128c(rs_ws, c32 * 16 + hh * 24576, WS_GRAN + ((tg & 1) ? 40 + cb : 16 + cb) * 512);
      asm volatile("s_waitcnt vmcnt(0)" : "+v"(gr[0]), "+v"(gr[1]), "+v"(gr[2]), "+v"(gr[3]), "+v"(gr[4])::"memory");
      bool ok = true;
#pragma unroll
      for (int n = 0; n < 5; ++n) ok = ok && gr[n].y == tag && gr[n].w == tag;
      if (__all(ok) || spins >= SPIN_CAP) break;
    }
    const float one = tg >= 2 ? 1.0f : 0.0f;
    const u32 vperm = g4 * 64 + ((lane >> 1) & 3) * 16 + ((lane >> 3) & 1) * 8 + (lane & 1) * 4;
#pragma unroll
    for (int n = 0; n < 2; ++n) {
      const u32 p = pk2(asf(gr[2 * n].x) + asf(gr[2 * n + 1].x), asf(gr[2 * n].z) + asf(gr[2 * n + 1].z));
      *(LAS u32*)(size_t)(LB + L_VEC + 1024 + (tg * 2 + n) * 256 + vperm) = pk2(bflo(p) + one, bfhi(p) + one);
    }
    *(LAS u32*)(size_t)(LB + L_VEC + (tg & 1) * 128 + (vperm & 127)) = pk2(sum32(asf(gr[4].x)), sum32(asf(gr[4].z)));
  } else {
    WF_GET(0, 4, 8);
    WAITV(4);
    if (w == 0) POLL(1 * 16 + pnl, bi & 3, 16u);
  }
  u32 wra[4], wo_a[4];
#pragma unroll
  for (int c = 0; c < 4; ++c) {
    wra[c] = LB + (8 * g4 + tq) * 128 + tp * 8 + ((c ^ (2 * kh) ^ (((tq >> 1) & 1) | ((g4 & 1) << 1))) << 5);
    wo_a[c] = wra[c] + kh * 65536;
    PIN(wo_a[c]);
  }
  BAR();
  STAMP(9);
  f32x4 cs0 = {0.f, 0.f, 0.f, 0.f}, cs1 = {0.f, 0.f, 0.f, 0.f};
  u32 cva = LB + L_VEC + 2048 - (l15 >> 3) * 1024 + g4 * 16;
  u32 x1p[4];
  {
    v4u of[8];
#pragma unroll
    for (int i = 0; i < 8; ++i) of[i] = ld128c(rs_ws, lane * 16, WS_O + ((8 * pnl + 4 * rh + tg) * 16 + 8 * kh + i) * 1024);
#define WO_FRAG(i, c) cat(trd(wo_a[c] + ((i) >> 1) * 16384 + ((i) & 1) * 4096), trd(wo_a[c] + ((i) >> 1) * 16384 + ((i) & 1) * 4096 + 512))
    const u32 va = LB + L_VEC + kh * 64 + g4 * 16;
    const v4u ga4 = lds128(va), cb4 = lds128(va + 2048 + cb * 128);
    v4u fa[4][4];
#pragma unroll
    for (int i = 0; i < 4; ++i)
#pragma unroll
      for (int c = 0; c < 4; ++c) fa[i][c] = WO_FRAG(i, c);
    PIN(xres);
    auto r0 = __builtin_amdgcn_permlane16_swap(xres.x, xres.z, false, false);
    auto r1 = __builtin_amdgcn_permlane16_swap(xres.y, xres.w, false, false);
    f32x2 xv[4] = {unp(r0[0]), unp(r1[0]), unp(r0[1]), unp(r1[1])};
    f32x2 gv[4] = {unp(ga4.x), unp(ga4.y), unp(ga4.z), unp(ga4.w)};
    f32x2 cv[4] = {unp(cb4.x), unp(cb4.y), unp(cb4.z), unp(cb4.w)};
#pragma unroll
    for (int e = 0; e < 4; ++e) { PIN(xv[e]); PIN(gv[e]); PIN(cv[e]); }
    u32 ea = LB + L_EX3 + tg * 4096 + lane * 16 + kh * 2048;
    PIN(ea);
    f32x4 acc[4];
    sfor<0, 8>([&](auto ic) AI {
      constexpr int i = ic.v;
      waitv<7 - i>();
      pin4(of[i]);
      SB();
#pragma unroll
      for (int c = 3; c >= 0; --c) {
        if constexpr (i == 0) mfma16_z(acc[c], fa[0][c], of[0]);
        else mfma16_ip(acc[c], fa[i & 3][c], of[i]);
        if constexpr (i + 4 < 8) fa[i & 3][c] = WO_FRAG(i + 4, c);
        SB();
      }
    });
    STAMP(10);
    asm volatile("s_nop 4" ::: "memory");
    SB();
    sts128(ea + 1024, __builtin_bit_cast(v4u, acc[3]));
    sts128(ea, __builtin_bit_cast(v4u, acc[2]));
    WF_GET(0, 8, 12);
    BAR();
    const f32x4 t0 = acc[0] + __builtin_bit_cast(f32x4, lds128(ea ^ 2048));
    const f32x4 t1 = acc[1] + __builtin_bit_cast(f32x4, lds128((ea ^ 2048) + 1024));
    f32x2 sum = {0.f, 0.f}, sq = {0.f, 0.f};
    v4u y;
#pragma unroll
    for (int e = 0; e < 4; ++e) {
      const f32x2 at = unp(e < 2 ? pk2(t0[2 * e], t0[2 * e + 1]) : pk2(t1[2 * e - 4], t1[2 * e - 3]));
      const f32x2 gp = gv[e] * at, xs = xv[e] + unp(pk2(gp[0], gp[1]));
      x1p[e] = pk2(xs[0], xs[1]);
      const f32x2 x1 = unp(x1p[e]), yy = x1 * cv[e];
      y[e] = pk2(yy[0], yy[1]);
      sum += x1; sq = fma2(x1, x1, sq);
    }
    st128c(y, rs_ws, lane * 16, WS_Y1 + ((8 * pnl + 4 * rh + tg) * 16 + 2 * cb + kh) * 1024);
    const float s1 = sum32(sum16(sum[0] + sum[1])), q1 = sum32(sum16(sq[0] + sq[1]));
    st64c(v2u{asu(s1), asu(q1)}, rs_ws, lane < 16 ? lane * 8 : WS_BYTES, WS_STAT + ((8 * pnl + 4 * rh + tg) * 16 + 2 * cb + kh) * 128);
    WF_GET(0, 12, 16);
    WAITV(4);
  }
  BAR();
  SIGNAL(4, 2 * 16 + pnl);
  STAMP(11);
  const v4u rs_wd = mk_srd(wd, F_ * D_ * 2);
  const u32 dso = tg * 8192 + cb * 128, dld = LB + tg * 1024;
#define WD_ISSUE(i, h) gldsi<(22 * (h) + (i)) * 32768, (2 * wd_sg(i) + (h)) * 4096>(rs_wd, dvo, dso, dld)
#define WD_ISSUE_K(i) gldsi<(i) * 32768, 2 * wd_sg(i) * 4096>(rs_wd, dvo, dso + kh * (22 * 32768), dld + kh * 4096)
  if (p4) {
    auto cs_part = [&](auto lo, auto hi) AI {
      v4u cf[hi.v - lo.v];
#pragma unroll
      for (int ks = lo.v; ks < hi.v; ++ks) { pin4(wf[0][ks]); cf[ks - lo.v] = lds128(cva + ks * 64); }
      SB();
#pragma unroll
      for (int ks = lo.v; ks < hi.v; ++ks) cs0 = mfma16(wf[0][ks], cf[ks - lo.v], cs0);
      pinf(cs0);
    };
    PIN(cva);
    if (kh == 0) { WF_GET(1, 4, 10); }
    gu32* const e4 = ctl + (WS_CNT + (2 * 16 + m2) * CNT_STRIDE + (bi & 3) * 128) / 4;
    if (w == 7) poll_kick<0>(e4);
    cs_part(IC<0>{}, IC<6>{});
    if (w == 7) poll_kick<1>(e4);
    cs_part(IC<6>{}, IC<12>{});
    if (w == 7) { poll_kick<2>(e4); poll_wait(e4, 16u); }
    STAMP(21);
    RBAR();
    STAMP(12);
    u32 ya0 = LB + lane * 16, ya1 = ya0 + 65536;
    PIN(ya0); PIN(ya1);
#define Y_READ(i) lds128(((i) < 64 ? ya0 : ya1) + ((i) & 63) * 1024)
    constexpr int PD = 6, RING = 7;
    const u32 hvo = l15 * 16, hso = WS_HH + 8 * m2 * (43 * 1024);
    if (w >= 4) {
      f32x4 acc0[8];
      v4u st[4], yr[RING];
      const u32 yso = WS_Y1 + (8 * m2 + 2 * tg) * 16384, yld = LB + tg * 2048;
#define Y_ISSUE(n) gldsic<((n) & 1) * 16384 + ((n) >> 1) * 1024, (((n) >> 1) * 8 + ((n) & 1)) * 1024>(rs_ws, lane * 16, yso, yld)
      PRIO(2);
      sfor<0, 12>([&](auto ic) AI { Y_ISSUE(ic.v); });
      WAITV(12);
      cs_part(IC<12>{}, IC<16>{});
      WAITV(8);
      RBAR();
      STAMP(19);
#pragma unroll
      for (int i = 0; i < PD; ++i) yr[i] = Y_READ(i);
      sfor<0, 128>([&](auto ic) AI {
        constexpr int i = ic.v, rd = i + PD;
        if constexpr (rd < 128) {
          if constexpr ((rd & 15) == 0) { waitv<ops_before(i) - (rd == 112 ? 46 : 4 * (rd >> 4) + 4)>(); RBAR(); }
          if constexpr (rd == 64) STAMP(20);
          yr[rd % RING] = Y_READ(rd);
        }
        if constexpr (i < 20) Y_ISSUE(12 + i);
        if constexpr (i >= 20 && i < 24) st[i - 20] = ld128c(rs_ws, lane * 16, WS_STAT + (8 * m2 + 2 * tg) * 2048 + (i - 20) * 1024);
        if constexpr (wd_slot(i) >= 0) {
          static_assert((wd_slot(i) >> 1) - 3 <= 2 * (rd >> 4) - 2, "slot still holds activations");
          WD_ISSUE(wd_slot(i) >> 1, wd_slot(i) & 1);
        }
        if constexpr (i == 76) {
          waitv<ops_before(i) - 36>();
          pin4(st[0]); pin4(st[1]); pin4(st[2]); pin4(st[3]);
          const f32x4 t0 = __builtin_bit_cast(f32x4, st[0]) + __builtin_bit_cast(f32x4, st[1]);
          const f32x4 t1 = __builtin_bit_cast(f32x4, st[2]) + __builtin_bit_cast(f32x4, st[3]);
          float t[4], q[2];
#pragma unroll
          for (int e = 0; e < 4; ++e) {
            auto r = __builtin_amdgcn_permlane32_swap(asu(t0[e]), asu(t1[e]), false, false);
            t[e] = asf(r[0]) + asf(r[1]);
          }
#pragma unroll
          for (int e = 0; e < 2; ++e) {
            auto r = __builtin_amdgcn_permlane16_swap(asu(t[e]), asu(t[e + 2]), false, false);
            q[e] = sum8(asf(r[0]) + asf(r[1]));
          }
          float m0, d0;
          ln_stats(q[0], q[1], m0, d0);
          sts64(LB + L_STAT2 + (2 * tg + hh) * 128 + (lane & 7) * 16 + (g4 & 1) * 8, v2u{asu(-m0), asu(__builtin_amdgcn_rcpf(d0))});
        }
        if constexpr ((i & 1) == 0) pin4(yr[(i + 1) % RING]);
        SB();
        if constexpr (i < 8) mfma16_z(acc0[i], wf[0][0], yr[i % RING]);
        else mfma16_ip(acc0[i & 7], wf[0][i >> 3], yr[i % RING]);
        SB();
      });
      SB();
      asm volatile("s_nop 7" ::: "memory");
      SB();
      PRIO(0);
      STAMP(13);
      {
        v2u sv[8];
#pragma unroll
        for (int t = 0; t < 8; ++t) sv[t] = lds64(LB + L_STAT2 + t * 128 + l15 * 8);
        const f32x2 cwa = {bcast<0>(cs0[0]), bcast<0>(cs0[1])}, cwb = {bcast<0>(cs0[2]), bcast<0>(cs0[3])};
        const f32x2 swa = {bcast<8>(cs0[0]), bcast<8>(cs0[1])}, swb = {bcast<8>(cs0[2]), bcast<8>(cs0[3])};
        f32x2 va[8], vb[8];
#pragma unroll
        for (int t = 0; t < 8; ++t) {
          const f32x2 m = {asf(sv[t].x), asf(sv[t].x)}, d = {asf(sv[t].y), asf(sv[t].y)};
          va[t] = fma2(fma2(m, cwa, f32x2{acc0[t][0], acc0[t][1]}), d, swa);
          vb[t] = fma2(fma2(m, cwb, f32x2{acc0[t][2], acc0[t][3]}), d, swb);
        }
        u32 hp[4][2];
#pragma unroll
        for (int t2 = 0; t2 < 4; ++t2) {
          auto r0 = __builtin_amdgcn_permlane32_swap(asu(va[2 * t2][0]), asu(va[2 * t2 + 1][0]), false, false);
          auto r1 = __builtin_amdgcn_permlane32_swap(asu(va[2 * t2][1]), asu(va[2 * t2 + 1][1]), false, false);
          auto r2 = __builtin_amdgcn_permlane32_swap(asu(vb[2 * t2][0]), asu(vb[2 * t2 + 1][0]), false, false);
          auto r3 = __builtin_amdgcn_permlane32_swap(asu(vb[2 * t2][1]), asu(vb[2 * t2 + 1][1]), false, false);
          hp[t2][0] = pk2(asf(r0[0]) * asf(r0[1]), asf(r1[0]) * asf(r1[1]));
          hp[t2][1] = pk2(asf(r2[0]) * asf(r2[1]), asf(r3[0]) * asf(r3[1]));
        }
        const int ug = 12 * j2 + un0;
        const u32 so = ug < 172 ? hso + ug * 256 : WS_BYTES;
#pragma unroll
        for (int qd = 0; qd < 2; ++qd) {
          auto r0 = __builtin_amdgcn_permlane16_swap(hp[2 * qd][0], hp[2 * qd + 1][0], false, false);
          auto r1 = __builtin_amdgcn_permlane16_swap(hp[2 * qd][1], hp[2 * qd + 1][1], false, false);
          st128c(v4u{r0[0], r1[0], r0[1], r1[1]}, rs_ws, hvo + (2 * (g4 & 1) + (g4 >> 1)) * (43 * 1024), so + qd * (4 * 43 * 1024));
        }
      }
#pragma unroll
      for (int ks = 0; ks < 16; ++ks) { asm volatile("" : "=v"(wf[0][ks])); asm volatile("" : "=v"(wf[1][ks])); }
      asm volatile("" : "=v"(cs0), "=v"(cs1));
    }
    int w2 = w;
    asm volatile("" : "+s"(w2));
    if (w2 < 4) {
      f32x4 acc0[8], acc1[8];
      v4u yr[RING], cfr;
      WAITV(6);
      cs_part(IC<12>{}, IC<16>{});
      WAITV(0);
#pragma unroll
      for (int ks = 0; ks < 10; ++ks) PIN(wf[1][ks]);
      RBAR();
      STAMP(19);
#pragma unroll
      for (int i = 0; i < PD; ++i) yr[i] = Y_READ(i);
      sfor<0, 128>([&](auto ic) AI {
        constexpr int i = ic.v, rd = i + PD;
        if constexpr (i == 80) { WAITV(0); _Pragma("unroll") for (int ks = 10; ks < 16; ++ks) pin4(wf[1][ks]); }
        if constexpr ((i & 1) == 0) pin4(yr[(i + 1) % RING]);
        SB();
        if constexpr (i < 8) mfma16_z(acc0[i], wf[0][0], yr[i % RING]);
        else mfma16_ip(acc0[i & 7], wf[0][i >> 3], yr[i % RING]);
        SB();
        if constexpr (rd < 128) {
          if constexpr ((rd & 15) == 0) RBAR();
          if constexpr (rd == 64) STAMP(20);
          yr[rd % RING] = Y_READ(rd);
        }
        SB();
        if constexpr (i < 8) mfma16_z(acc1[i], wf[1][0], yr[i % RING]);
        else mfma16_ip(acc1[i & 7], wf[1][i >> 3], yr[i % RING]);
        SB();
        if constexpr ((i & 7) == 0) cfr = lds128(cva + (i >> 3) * 64);
        if constexpr ((i & 7) == 6) mfma16_ip(cs1, wf[1][i >> 3], cfr);
        if constexpr (i >= 36 && i < 60 && (i & 3) == 2) { WF_GET(1, 10 + ((i - 36) >> 2), 11 + ((i - 36) >> 2)); }
        SB();
      });
      SB();
      asm volatile("s_nop 7" ::: "memory");
      SB();
      STAMP(13);
      {
        v2u sv[8];
#pragma unroll
        for (int t = 0; t < 8; ++t) sv[t] = lds64(LB + L_STAT2 + t * 128 + l15 * 8);
        const f32x2 cga = {bcast<0>(cs0[0]), bcast<0>(cs0[1])}, cgb = {bcast<0>(cs0[2]), bcast<0>(cs0[3])};
        const f32x2 sga = {bcast<8>(cs0[0]), bcast<8>(cs0[1])}, sgb = {bcast<8>(cs0[2]), bcast<8>(cs0[3])};
        const f32x2 cua = {bcast<0>(cs1[0]), bcast<0>(cs1[1])}, cub = {bcast<0>(cs1[2]), bcast<0>(cs1[3])};
        const f32x2 sua = {bcast<8>(cs1[0]), bcast<8>(cs1[1])}, sub = {bcast<8>(cs1[2]), bcast<8>(cs1[3])};
        const int ug = 12 * j2 + un0;
        const u32 so = ug < 172 ? hso + ug * 256 : WS_BYTES;
        const u32 vo = hvo + (g4 & 1) * (43 * 1024) + (g4 >> 1) * 256;
#pragma unroll
        for (int t2 = 0; t2 < 4; ++t2) {
          u32 hp[2][2];
#pragma unroll
          for (int k = 0; k < 2; ++k) {
            const int t = 2 * t2 + k;
            const f32x2 m = {asf(sv[t].x), asf(sv[t].x)}, d = {asf(sv[t].y), asf(sv[t].y)};
            const f32x2 pa = fma2(fma2(m, cga, f32x2{acc0[t][0], acc0[t][1]}), d, sga) * fma2(fma2(m, cua, f32x2{acc1[t][0], acc1[t][1]}), d, sua);
            const f32x2 pb = fma2(fma2(m, cgb, f32x2{acc0[t][2], acc0[t][3]}), d, sgb) * fma2(fma2(m, cub, f32x2{acc1[t][2], acc1[t][3]}), d, sub);
            hp[k][0] = pk2(pa[0], pa[1]); hp[k][1] = pk2(pb[0], pb[1]);
          }
          auto r0 = __builtin_amdgcn_permlane16_swap(hp[0][0], hp[1][0], false, false);
          auto r1 = __builtin_amdgcn_permlane16_swap(hp[0][1], hp[1][1], false, false);
          st128c(v4u{r0[0], r1[0], r0[1], r1[1]}, rs_ws, vo, so + t2 * (2 * 43 * 1024));
        }
      }
    }
  } else {
    sfor<0, 16>([&](auto ic) AI { WD_ISSUE_K(ic.v); });
    WAITV(0);
    RBAR();
  }
  {
    u32 da0[4], da1[4], da2[4];
#pragma unroll
    for (int c = 0; c < 4; ++c) { da0[c] = wra[c] + kh * 4096; da1[c] = da0[c] + 65536; da2[c] = da0[c] + 131072; PIN(da0[c]); PIN(da1[c]); PIN(da2[c]); }
#define D_ADDR(i, c) (wd_sg(i) < 8 ? da0[c] + wd_sg(i) * 8192 : (wd_sg(i) < 16 ? da1[c] + (wd_sg(i) - 8) * 8192 : da2[c] + (wd_sg(i) - 16) * 8192))
#define D_READ(i, c) cat(trd(D_ADDR(i, c)), trd(D_ADDR(i, c) + 512))
    v4u fa[5][4];
#pragma unroll
    for (int i = 0; i < 5; ++i)
#pragma unroll
      for (int c = 0; c < 4; ++c) fa[i][c] = D_READ(i, c);
    const v4u gb4 = lds128(LB + L_VEC + 128 + kh * 64 + g4 * 16);
    WAITV(0);
    BAR();
    if (p4) SIGNAL(1, 3 * 16 + m2);
    STAMP(14);
    if (w == 0) POLL(3 * 16 + pnl, bi & 3, 15u);
    sfor<16, 22>([&](auto ic) AI { WD_ISSUE_K(ic.v); });
    RBAR();
    STAMP(15);
    constexpr int HA = 8;
    v4u hf2[HA + 1];
    const u32 hso = WS_HH + (8 * pnl + 4 * rh + tg) * (43 * 1024) + kh * (22 * 1024);
#define H_GET(i) hf2[(i) % (HA + 1)] = ld128c(rs_ws, lane * 16, (i) < 21 ? hso + (i) * 1024 : hso + (21 - kh) * 1024)
#pragma unroll
    for (int i = 0; i < HA; ++i) H_GET(i);
    f32x2 gv[4] = {unp(gb4.x), unp(gb4.y), unp(gb4.z), unp(gb4.w)}, xv[4] = {unp(x1p[0]), unp(x1p[1]), unp(x1p[2]), unp(x1p[3])};
#pragma unroll
    for (int e = 0; e < 4; ++e) { PIN(gv[e]); PIN(xv[e]); }
    u32 ea = LB + tg * 4096 + lane * 16 + kh * 2048;
    PIN(ea);
    f32x4 acc[4];
    sfor<0, 22>([&](auto ic) AI {
      constexpr int i = ic.v;
      if constexpr (i == 3 || i == 8 || i == 13) RBAR();
      if constexpr (i + HA < 22) H_GET(i + HA);
      waitv<(i + HA < 22 ? HA : 21 - i)>();
      pin4(hf2[i % (HA + 1)]);
      SB();
#pragma unroll
      for (int c = 3; c >= 0; --c) {
        if constexpr (i == 0) mfma16_z(acc[c], fa[0][c], hf2[0]);
        else mfma16_ip(acc[c], fa[i % 5][c], hf2[i % (HA + 1)]);
        if constexpr (i + 5 < 22) fa[i % 5][c] = D_READ(i + 5, c);
        SB();
      }
    });
    STAMP(16);
    asm volatile("s_nop 4" ::: "memory");
    SB();
    sts128(ea + 1024, __builtin_bit_cast(v4u, acc[3]));
    sts128(ea, __builtin_bit_cast(v4u, acc[2]));
    BAR();
    const f32x4 t0 = acc[0] + __builtin_bit_cast(f32x4, lds128(ea ^ 2048));
    const f32x4 t1 = acc[1] + __builtin_bit_cast(f32x4, lds128((ea ^ 2048) + 1024));
    u32 op[4];
#pragma unroll
    for (int e = 0; e < 4; ++e) {
      const f32x2 gp = gv[e] * unp(e < 2 ? pk2(t0[2 * e], t0[2 * e + 1]) : pk2(t1[2 * e - 4], t1[2 * e - 3]));
      const f32x2 o2 = xv[e] + unp(pk2(gp[0], gp[1]));
      op[e] = pk2(o2[0], o2[1]);
    }
    auto r0 = __builtin_amdgcn_permlane16_swap(op[0], op[2], false, false);
    auto r1 = __builtin_amdgcn_permlane16_swap(op[1], op[3], false, false);
    *(GAS v4u*)((GAS char*)out + orow) = v4u{r0[0], r1[0], r0[1], r1[1]};
  }
  if (b == 0 && tid == 0) __hip_atomic_store(ctl + WS_EPOCH / 4, tag, RLX_AGENT);
}

extern "C" void kernel_launch(void* const* d_in, const int* in_sizes, int n_in, void* d_out, int out_size, void* d_ws, size_t ws_size,
                              hipStream_t stream) {
  (void)in_sizes; (void)n_in; (void)out_size; (void)ws_size;
  (void)hipFuncSetAttribute((const void*)fused_block, hipFuncAttributeMaxDynamicSharedMemorySize, L_TOTAL);
  fused_block<<<256, 512, L_TOTAL, stream>>>((const u16*)d_in[0], (const u16*)d_in[1], (const u16*)d_in[2], (const u16*)d_in[3],
                                             (const u16*)d_in[5], (const u16*)d_in[6], (const u16*)d_in[7], (const u16*)d_in[8],
                                             (const u16*)d_in[9], (u16*)d_out, (char*)d_ws);
}
```

```cpp
#include <hip/hip_runtime.h>
#define LEAK 2

typedef unsigned short u16;
typedef unsigned int u32;
typedef u32 v4u __attribute__((ext_vector_type(4)));
typedef u32 v2u __attribute__((ext_vector_type(2)));
typedef float f32x2 __attribute__((ext_vector_type(2)));
typedef float f32x4 __attribute__((ext_vector_type(4)));
typedef float f32x16 __attribute__((ext_vector_type(16)));
typedef short bf16x8 __attribute__((ext_vector_type(8)));
typedef short s16x4 __attribute__((ext_vector_type(4)));
typedef __bf16 bf16x2 __attribute__((ext_vector_type(2)));
#define GAS __attribute__((address_space(1)))
#define LAS __attribute__((address_space(3)))
#define DI __device__ __forceinline__
#define RLX_AGENT __ATOMIC_RELAXED, __HIP_MEMORY_SCOPE_AGENT
typedef GAS u32 gu32;

constexpr int S_ = 2048, D_ = 512, F_ = 1376;
constexpr int WS_EPOCH = 0, WS_CNT = 983040, CNT_STRIDE = 512;
constexpr int WS_GRAN = 32768, WS_ROWST = 98304, WS_MFLAG = 20480, WS_SLAB = 131072, WS_Z = 655360, WS_STAT = 720896;
constexpr int WS_O = 1 << 20, WS_Y1 = 3 << 20, WS_HH = 5 << 20, WS_GT = 11 << 20, WS_DBG = 15 << 20, WS_BYTES = 16 << 20;
constexpr int L_B = 65536, L_T = 131072, L_MISC = 155648, L_TOTAL = 163840;
constexpr int L_CS = L_MISC, L_VEC = L_MISC, L_RED = L_MISC + 4608, L_STATX = L_MISC + 4096, L_STAT2 = L_MISC + 4096, L_CWSW = L_MISC + 5120;
constexpr int L_ARR = L_MISC + 6912, L_WPHI = L_MISC + 7168;
constexpr int L_KVT = 139264, L_QEX = 147456, L_EX3 = 139264;
constexpr unsigned CODE_SLICES = 20;
constexpr unsigned SPIN_CAP = 1u << 18;

DI float asf(u32 v) { return __builtin_bit_cast(float, v); }
DI u32 asu(float v) { return __builtin_bit_cast(u32, v); }
DI u32 asu(bf16x2 v) { return __builtin_bit_cast(u32, v); }
DI float bf2f(u32 b) { return asf(b << 16); }
DI float bflo(u32 p) { return asf(p << 16); }
DI float bfhi(u32 p) { return asf(p & 0xffff0000u); }
DI u32 pk2(float a, float b) { f32x2 v = {a, b}; return asu(__builtin_convertvector(v, bf16x2)); }
DI float rb(float a) { return bflo(pk2(a, a) & 0xffffu); }
DI v4u mk_srd(const void* p, u32 bytes) {
  unsigned long long a = (unsigned long long)p;
  return v4u{(u32)a, (u32)(a >> 32) & 0xffffu, bytes, 0x00020000u};
}
DI void glds(v4u rs, u32 voff, u32 soff, u32 ldsaddr) {
  asm volatile("s_mov_b32 m0, %3\n\ts_nop 0\n\tbuffer_load_dwordx4 %0, %1, %2 offen lds" ::"v"(voff), "s"(rs), "s"(soff), "s"(ldsaddr) : "memory");
}
template <int SO, int LO> DI void gldsi(v4u rs, u32 voff, u32 soff, u32 ldsaddr) {
  u32 t;
  asm volatile("s_add_u32 m0, %4, %6\n\ts_add_u32 %0, %3, %5\n\tbuffer_load_dwordx4 %1, %2, %0 offen lds"
               : "=&s"(t) : "v"(voff), "s"(rs), "s"(soff), "s"(ldsaddr), "i"(SO), "i"(LO) : "memory", "scc");
}
template <int SO, int LO> DI void gldsic(v4u rs, u32 voff, u32 soff, u32 ldsaddr) {
  u32 t;
  asm volatile("s_add_u32 m0, %4, %6\n\ts_add_u32 %0, %3, %5\n\tbuffer_load_dwordx4 %1, %2, %0 offen sc1 lds"
               : "=&s"(t) : "v"(voff), "s"(rs), "s"(soff), "s"(ldsaddr), "i"(SO), "i"(LO) : "memory", "scc");
}
DI v2u ld64(v4u rs, u32 voff, u32 soff) {
  v2u r;
  asm volatile("buffer_load_dwordx2 %0, %1, %2, %3 offen" : "=v"(r) : "v"(voff), "s"(rs), "s"(soff) : "memory");
  return r;
}
DI v4u ld128(v4u rs, u32 voff, u32 soff) {
  v4u r;
  asm volatile("buffer_load_dwordx4 %0, %1, %2, %3 offen" : "=v"(r) : "v"(voff), "s"(rs), "s"(soff) : "memory");
  return r;
}
DI v4u ld128c(v4u rs, u32 voff, u32 soff) {
  v4u r;
  asm volatile("buffer_load_dwordx4 %0, %1, %2, %3 offen sc1" : "=v"(r) : "v"(voff), "s"(rs), "s"(soff) : "memory");
  return r;
}
DI v2u ld64c(v4u rs, u32 voff, u32 soff) {
  v2u r;
  asm volatile("buffer_load_dwordx2 %0, %1, %2, %3 offen sc1" : "=v"(r) : "v"(voff), "s"(rs), "s"(soff) : "memory");
  return r;
}
DI void st128c(v4u v, v4u rs, u32 voff, u32 soff) {
  asm volatile("buffer_store_dwordx4 %0, %1, %2, %3 offen sc1\n\ts_nop 1" ::"v"(v), "v"(voff), "s"(rs), "s"(soff) : "memory");
}
DI void st64c(v2u v, v4u rs, u32 voff, u32 soff) {
  asm volatile("buffer_store_dwordx2 %0, %1, %2, %3 offen sc1" ::"v"(v), "v"(voff), "s"(rs), "s"(soff) : "memory");
}
#define WAITV(n) asm volatile("s_waitcnt vmcnt(" #n ")" ::: "memory")
#define BAR() asm volatile("s_waitcnt lgkmcnt(0)\n\ts_barrier" ::: "memory")
#define PIN(x) asm volatile("" : "+v"(x))
#define SB() __builtin_amdgcn_sched_barrier(0)
#define RBAR() asm volatile("s_barrier" ::: "memory")
template <int N> DI void waitv() { asm volatile("s_waitcnt vmcnt(%0)" ::"i"(N) : "memory"); }

DI v4u lds128(u32 a) { return *(const LAS v4u*)(size_t)a; }
DI v2u lds64(u32 a) { return *(const LAS v2u*)(size_t)a; }
DI void sts128(u32 a, v4u v) { *(LAS v4u*)(size_t)a = v; }
DI void sts64(u32 a, v2u v) { *(LAS v2u*)(size_t)a = v; }
DI v2u trd(u32 a) { return __builtin_bit_cast(v2u, __builtin_amdgcn_ds_read_tr16_b64_v4i16((LAS s16x4*)(size_t)a)); }
DI v4u cat(v2u a, v2u b) { return v4u{a.x, a.y, b.x, b.y}; }
DI f32x16 mfma32(v4u a, v4u b, f32x16 c) {
  return __builtin_amdgcn_mfma_f32_32x32x16_bf16(__builtin_bit_cast(bf16x8, a), __builtin_bit_cast(bf16x8, b), c, 0, 0, 0);
}
DI f32x4 mfma16(v4u a, v4u b, f32x4 c) {
  return __builtin_amdgcn_mfma_f32_16x16x32_bf16(__builtin_bit_cast(bf16x8, a), __builtin_bit_cast(bf16x8, b), c, 0, 0, 0);
}
DI float sum32(float v) {
  u32 a = asu(v), c = a;
  auto r = __builtin_amdgcn_permlane32_swap(a, c, false, false);
  return asf(r[0]) + asf(r[1]);
}
DI float sum16(float v) {
  u32 a = asu(v), c = a;
  auto r = __builtin_amdgcn_permlane16_swap(a, c, false, false);
  return asf(r[0]) + asf(r[1]);
}
DI float sum8(float v) {
  return v + asf(__builtin_amdgcn_update_dpp(0, __builtin_bit_cast(int, v), 0x128, 0xf, 0xf, false));
}
DI float sum4(float v) {
  return v + asf(__builtin_amdgcn_update_dpp(0, __builtin_bit_cast(int, v), 0x124, 0xf, 0xf, false));
}
DI void ln_stats(float s, float q, float& mean, float& sd) {
  const float m = s * (1.0f / D_);
  float var = q * (1.0f / D_) - m * m;
  var = fmaxf(var, 0.0f);
  mean = rb(m);
  sd = rb(__builtin_amdgcn_sqrtf(rb(rb(var) + 1e-5f)));
}
template <int I> DI void poll_kick(gu32* cnt) {
  unsigned long long ex;
  asm volatile("v_mov_b32 v255, 0\n\ts_mov_b64 %0, exec\n\ts_mov_b64 exec, 1\n\tglobal_atomic_add v%c2, v255, v255, %1 sc0\n\ts_mov_b64 exec, %0"
               : "=&s"(ex) : "s"(cnt), "i"(252 + I) : "memory", "v252", "v253", "v254", "v255");
}
template <unsigned CAP = SPIN_CAP> DI void poll_wait(gu32* cnt, u32 target) {
  unsigned long long ex;
  u32 v, n;
  asm volatile(
      "s_mov_b64 %0, exec\n\ts_mov_b64 exec, 1\n\ts_mov_b32 %2, %5\n"
      "1:\n\t"
      "s_waitcnt vmcnt(2)\n\tv_readfirstlane_b32 %1, v252\n\ts_nop 1\n\ts_cmp_eq_u32 %1, %4\n\ts_cbranch_scc1 2f\n\t"
      "global_atomic_add v252, v255, v255, %3 sc0\n\t"
      "s_waitcnt vmcnt(2)\n\tv_readfirstlane_b32 %1, v253\n\ts_nop 1\n\ts_cmp_eq_u32 %1, %4\n\ts_cbranch_scc1 2f\n\t"
      "global_atomic_add v253, v255, v255, %3 sc0\n\t"
      "s_waitcnt vmcnt(2)\n\tv_readfirstlane_b32 %1, v254\n\ts_nop 1\n\ts_cmp_eq_u32 %1, %4\n\ts_cbranch_scc1 2f\n\t"
      "global_atomic_add v254, v255, v255, %3 sc0\n\t"
      "s_sub_u32 %2, %2, 1\n\ts_cmp_lg_u32 %2, 0\n\ts_cbranch_scc1 1b\n"
      "2:\n\t"
      "s_mov_b64 exec, %0"
      : "=&s"(ex), "=&s"(v), "=&s"(n)
      : "s"(cnt), "s"(target), "i"(CAP)
      : "memory", "scc", "v252", "v253", "v254", "v255");
}
template <unsigned CAP = SPIN_CAP> DI void poll_cnt(gu32* cnt, u32 target) {
  poll_kick<0>(cnt); __builtin_amdgcn_s_sleep(2);
  poll_kick<1>(cnt); __builtin_amdgcn_s_sleep(2);
  poll_kick<2>(cnt);
  poll_wait<CAP>(cnt, target);
}
DI float max16(const f32x16& p) {
  const float a = fmaxf(fmaxf(p[0], p[1]), p[2]), b = fmaxf(fmaxf(p[3], p[4]), p[5]), c = fmaxf(fmaxf(p[6], p[7]), p[8]);
  const float d = fmaxf(fmaxf(p[9], p[10]), p[11]), e = fmaxf(fmaxf(p[12], p[13]), p[14]);
  return fmaxf(fmaxf(fmaxf(a, b), c), fmaxf(fmaxf(d, e), p[15]));
}
DI float min16(const f32x16& p) {
  const float a = fminf(fminf(p[0], p[1]), p[2]), b = fminf(fminf(p[3], p[4]), p[5]), c = fminf(fminf(p[6], p[7]), p[8]);
  const float d = fminf(fminf(p[9], p[10]), p[11]), e = fminf(fminf(p[12], p[13]), p[14]);
  return fminf(fminf(fminf(a, b), c), fminf(fminf(d, e), p[15]));
}
DI void expnorm(const f32x16& p, float l, float a, u32* ph) {
  f32x2 e[8];
#pragma unroll
  for (int i = 0; i < 8; ++i) {
    const f32x2 t = __builtin_elementwise_fma(f32x2{p[2 * i], p[2 * i + 1]}, f32x2{l, l}, f32x2{a, a});
    e[i] = f32x2{__builtin_amdgcn_exp2f(t[0]), __builtin_amdgcn_exp2f(t[1])};
  }
  const f32x2 s = ((e[0] + e[1]) + (e[2] + e[3])) + ((e[4] + e[5]) + (e[6] + e[7]));
  const float r = __builtin_amdgcn_rcpf(s[0] + s[1]);
#pragma unroll
  for (int i = 0; i < 8; ++i) { const f32x2 t = e[i] * f32x2{r, r}; ph[i] = pk2(t[0], t[1]); }
}
constexpr float LOG2E = 1.44269504088896341f;
DI void softmax16(const f32x16& p, u32 (&ph)[8]) { expnorm(p, LOG2E, -max16(p) * LOG2E, ph); }
DI void hedgehog(const f32x16& p, u32 (&ph)[16]) {
  expnorm(p, LOG2E, -max16(p) * LOG2E, ph);
  expnorm(p, -LOG2E, min16(p) * LOG2E, ph + 8);
}
DI v4u pack8(const f32x16& x, int s) {
  return v4u{pk2(x[8 * s], x[8 * s + 1]), pk2(x[8 * s + 2], x[8 * s + 3]), pk2(x[8 * s + 4], x[8 * s + 5]), pk2(x[8 * s + 6], x[8 * s + 7])};
}
DI void ex_put(u32 a, const f32x16& x) {
#pragma unroll
  for (int i = 0; i < 4; ++i) sts128(a + i * 1024, __builtin_bit_cast(v4u, f32x4{x[4 * i], x[4 * i + 1], x[4 * i + 2], x[4 * i + 3]}));
}
DI void ex_add(u32 a, f32x16& x) {
#pragma unroll
  for (int i = 0; i < 4; ++i) {
    const f32x4 t = __builtin_bit_cast(f32x4, lds128(a + i * 1024));
    x[4 * i] += t[0]; x[4 * i + 1] += t[1]; x[4 * i + 2] += t[2]; x[4 * i + 3] += t[3];
  }
}

#define STAMP(i) do { if ((i) == 0) lk0 = (u32)__builtin_amdgcn_s_memrealtime(); if ((i) == LEAK) lk1 = (u32)__builtin_amdgcn_s_memrealtime(); } while (0)

template <int I> struct IC { static constexpr int v = I; };
template <int I, int N, class Fn> DI void sfor(Fn&& f) {
  if constexpr (I < N) { f(IC<I>{}); sfor<I + 1, N>(f); }
}
#define AI __attribute__((always_inline))
#define PRIO(n) __builtin_amdgcn_s_setprio(n)
constexpr int wd_slot(int i) { return (i >= 24 && i < 120 && (i - 24) % 3 == 0) ? (i - 24) / 3 : -1; }
constexpr int ops_before(int i) { return i <= 24 ? 12 + i : (i >= 118 ? 68 : 36 + (i - 24 + 2) / 3); }
constexpr int wd_sg(int i) { return i < 3 ? 16 + i : i - 3; }
template <int N> DI float bcast(float v) { return asf(__builtin_amdgcn_mov_dpp((int)asu(v), 0x150 + N, 0xf, 0xf, true)); }
DI f32x2 unp(u32 p) { return f32x2{bflo(p), bfhi(p)}; }
DI f32x2 fma2(f32x2 a, f32x2 b, f32x2 c) { return __builtin_elementwise_fma(a, b, c); }
DI void mfma16_ip(f32x4& c, v4u a, v4u b) { asm volatile("v_mfma_f32_16x16x32_bf16 %0, %1, %2, %0" : "+v"(c) : "v"(a), "v"(b)); }
DI void mfma16_z(f32x4& c, v4u a, v4u b) { asm volatile("v_mfma_f32_16x16x32_bf16 %0, %1, %2, 0" : "=&v"(c) : "v"(a), "v"(b)); }
DI void pinf(f32x4& v) { PIN(v); }
DI void pin4(v4u& v) { PIN(v); }
DI void fma2_ip(f32x2& x, f32x2 a, f32x2 c) { asm volatile("v_pk_fma_f32 %0, %0, %1, %2" : "+v"(x) : "v"(a), "v"(c)); }
DI void mfma32_ip(f32x16& c, v4u a, v4u b) { asm volatile("v_mfma_f32_32x32x16_bf16 %0, %1, %2, %0" : "+v"(c) : "v"(a), "v"(b)); }
template <int N> DI void fmac_bc(float& h, float c, float x) {
  asm("v_fmac_f32_dpp %0, %1, %2 row_newbcast:%3 row_mask:0xf bank_mask:0xf" : "+v"(h) : "v"(c), "v"(x), "i"(N));
}

__global__ void __launch_bounds__(512, 2) __attribute__((amdgpu_num_vgpr(248)))
fused_block(const u16* __restrict__ x, const u16* __restrict__ wqkv, const u16* __restrict__ wphi, const u16* __restrict__ wo,
            const u16* __restrict__ wg, const u16* __restrict__ wu, const u16* __restrict__ wd, const u16* __restrict__ cvec,
            const u16* __restrict__ wmod, u16* __restrict__ out, char* __restrict__ ws) {
  extern __shared__ __attribute__((aligned(16))) char smem[];
  const u32 LB = (u32)(size_t)(LAS char*)smem;
  u32 lk0 = 0, lk1 = 0;
  STAMP(0);
  u32 cpf;
  {
    unsigned long long pc;
    u32 sl = ((blockIdx.x >> 3) % CODE_SLICES) * 2048u, off;
    asm volatile(
        "s_getpc_b64 %2\n\tv_lshlrev_b32 %1, 7, %3\n\tv_cmp_lt_u32 vcc, 63, %3\n\tv_mov_b32 %0, %4\n\ts_nop 0\n\tv_cndmask_b32 %0, 0, %0, vcc\n\t"
        "v_add_u32 %1, %1, %0\n\tv_cmp_gt_u32 vcc, 0x50, %3\n\ts_nop 1\n\tv_cndmask_b32 %1, 0, %1, vcc\n\tglobal_load_dword %0, %1, %2"
        : "=&v"(cpf), "=&v"(off), "=&s"(pc) : "v"(threadIdx.x), "s"(sl) : "memory", "vcc");
    SB();
  }
  asm volatile("" ::"s"(x), "s"(wqkv), "s"(wphi), "s"(wo), "s"(wg), "s"(wu), "s"(wd), "s"(cvec), "s"(wmod), "s"(out), "s"(ws));
  const int tid = threadIdx.x, lane = tid & 63;
  int w = __builtin_amdgcn_readfirstlane(tid >> 6);
  asm volatile("s_nop 4" : "+s"(w));
  const int b = blockIdx.x, g = b & 7, bi = b >> 3;
  const int m2 = 4 * (g >> 1) + (bi >> 3), j2 = 8 * (g & 1) + (bi & 7);
  const int pnl = 2 * g + (bi >> 4), rh = (bi >> 3) & 1, cb = bi & 7;
  const v4u rs_x = mk_srd(x, S_ * D_ * 2), rs_ws = mk_srd(ws, WS_BYTES);
  gu32* const ctl = (gu32*)ws;
  const int tg = w & 3, kh = w >> 2;
  const int c32 = lane & 31, hh = lane >> 5;
  const int l15 = lane & 15, g4 = lane >> 4, tq = (lane & 15) >> 2, tp = lane & 3;

  const bool prod = b < 96, sprod = b >= 96 && b < 224;
  const int cg = b >> 1, kp = b & 1;
  const u32 xld = LB + w * 16384;
  v4u xr[4];
  u32 tagv;
  if (w == 0 && b < 4) __hip_atomic_store(ctl + (WS_CNT + (b * 64 + lane) * 128) / 4, 0u, RLX_AGENT);
  asm volatile("buffer_load_dword %0, off, %1, 0 sc1" : "=v"(tagv) : "s"(rs_ws) : "memory");
  v2u mc2;
  if (prod && w == 0) {
    const v4u rs_m = mk_srd(wmod, D_ * 6 * D_ * 2), rs_c = mk_srd(cvec, D_ * 2);
    mc2 = ld64(rs_c, lane * 8, kp * 512);
    const u32 vo = (lane >> 3) * 6144 + (((lane & 7) ^ (((lane >> 4) & 1) << 1)) << 4);
    const u32 so = 256 * kp * 6144 + cg * 128;
    sfor<0, 32>([&](auto ic) AI { gldsi<ic.v * 8 * 6144, (ic.v < 24 ? L_T + ic.v * 1024 : (ic.v - 24) * 1024)>(rs_m, (ic.v & 1) ? vo ^ 64 : vo, so, LB); });
  }
  if (kh == 0 && sprod) {
#pragma unroll
    for (int r = 0; r < 4; ++r) xr[r] = ld128(rs_x, lane * 16, (16 * (b - 96) + 4 * w + r) * 1024);
  }
  if (w == 5) glds(mk_srd(wphi, 32 * 256 * 2), lane * 16, j2 * 1024, LB + L_WPHI);
  if (prod) {
    if (w == 0) {
      asm volatile("s_waitcnt vmcnt(32)" : "+v"(mc2), "+v"(tagv)::"memory");
      float sv[4] = {bflo(mc2.x), bfhi(mc2.x), bflo(mc2.y), bfhi(mc2.y)};
#pragma unroll
      for (int e = 0; e < 4; ++e) sv[e] = sv[e] * __builtin_amdgcn_rcpf(1.0f + __expf(-sv[e]));
      sts64(LB + 8192 + lane * 8, v2u{pk2(sv[0], sv[1]), pk2(sv[2], sv[3])});
    }
    RBAR();
  }
  const u32 xvo0 = (lane >> 3) * 1024 + (((lane & 7) ^ (lane >> 4)) << 4), xvo1 = xvo0 ^ 64;
  const int rot = bi & 3;
  const u32 xso = (128 * m2 + 32 * tg) * 1024 + kh * 512 + rot * 128;
#define X_ISSUE(q4)                                                                       \
  do {                                                                                    \
    gldsi<0, (q4) * 4096>(rs_x, xvo0, xso ^ ((q4) * 128), xld);                           \
    gldsi<8192, (q4) * 4096 + 1024>(rs_x, xvo1, xso ^ ((q4) * 128), xld);                 \
    gldsi<16384, (q4) * 4096 + 2048>(rs_x, xvo0, xso ^ ((q4) * 128), xld);                \
    gldsi<24576, (q4) * 4096 + 3072>(rs_x, xvo1, xso ^ ((q4) * 128), xld);                \
  } while (0)
  if (kh == 0 && sprod) {
    asm volatile("s_waitcnt vmcnt(0)" : "+v"(xr[0]), "+v"(xr[1]), "+v"(xr[2]), "+v"(xr[3]), "+v"(tagv)::"memory");
    float s[4], q[4], t[2];
#pragma unroll
    for (int r = 0; r < 4; ++r) {
      s[r] = 0.f; q[r] = 0.f;
#pragma unroll
      for (int e = 0; e < 4; ++e) {
        const float lo = bflo(xr[r][e]), hi = bfhi(xr[r][e]);
        s[r] += lo + hi; q[r] = __builtin_fmaf(lo, lo, __builtin_fmaf(hi, hi, q[r]));
      }
    }
#pragma unroll
    for (int r = 0; r < 2; ++r) {
      auto rs = __builtin_amdgcn_permlane32_swap(asu(s[r]), asu(s[r + 2]), false, false);
      auto rq = __builtin_amdgcn_permlane32_swap(asu(q[r]), asu(q[r + 2]), false, false);
      s[r] = asf(rs[0]) + asf(rs[1]); q[r] = asf(rq[0]) + asf(rq[1]);
    }
    {
      auto rs = __builtin_amdgcn_permlane16_swap(asu(s[0]), asu(s[1]), false, false);
      auto rq = __builtin_amdgcn_permlane16_swap(asu(q[0]), asu(q[1]), false, false);
      t[0] = asf(rs[0]) + asf(rs[1]); t[1] = asf(rq[0]) + asf(rq[1]);
    }
#pragma unroll
    for (int r = 0; r < 2; ++r) {
      t[r] = sum4(sum8(t[r]));
      t[r] += asf(__builtin_amdgcn_update_dpp(0, (int)asu(t[r]), 0x122, 0xf, 0xf, false));
      t[r] += asf(__builtin_amdgcn_update_dpp(0, (int)asu(t[r]), 0x121, 0xf, 0xf, false));
    }
    float mean, sd;
    ln_stats(t[0], t[1], mean, sd);
    if (l15 == 0) st64c(v2u{pk2(mean, sd), tagv + 1u}, rs_ws, g4 * 8, WS_ROWST + (16 * (b - 96) + 4 * w) * 8);
  }
  if (prod && w == 0) {
    PRIO(3);
    v4u sb[8];
#pragma unroll
    for (int ks = 0; ks < 8; ++ks) sb[ks] = lds128(LB + 8192 + ks * 64 + g4 * 16);
    u32 ma[4], mb[4];
#pragma unroll
    for (int c = 0; c < 4; ++c) {
      mb[c] = LB + (8 * g4 + tq) * 128 + tp * 8 + ((c ^ (((tq >> 1) & 1) | ((g4 & 1) << 1))) << 5); ma[c] = mb[c] + L_T;
      PIN(ma[c]); PIN(mb[c]);
    }
    f32x4 acc[4];
#pragma unroll
    for (int c = 0; c < 4; ++c) acc[c] = f32x4{0.f, 0.f, 0.f, 0.f};
    v4u fm[2][2][4];
#define M_READ(h2) _Pragma("unroll") for (int k2 = 0; k2 < 2; ++k2) _Pragma("unroll") for (int c = 0; c < 4; ++c) {               \
      const u32 ad = (h2) < 3 ? ma[c] + (2 * (h2) + k2) * 4096 : mb[c] + k2 * 4096;                                                \
      fm[(h2) & 1][k2][c] = cat(trd(ad), trd(ad + 512)); }
    WAITV(24);
    M_READ(0);
    sfor<0, 4>([&](auto ic) AI {
      constexpr int h2 = ic.v;
      if constexpr (h2 < 3) { waitv<16 - 8 * h2>(); M_READ(h2 + 1); }
      SB();
#pragma unroll
      for (int k2 = 0; k2 < 2; ++k2)
#pragma unroll
        for (int c = 0; c < 4; ++c) acc[c] = mfma16(sb[2 * h2 + k2], fm[h2 & 1][k2][c], acc[c]);
      SB();
    });
    const float lo = (g4 & 1) ? acc[1][0] : acc[0][0], hi = (g4 & 1) ? acc[3][0] : acc[2][0];
    st64c(v2u{asu(hh ? hi : lo), tagv + 1u}, rs_ws, lane * 8, WS_GRAN + kp * 24576 + cg * 512);
    PRIO(0);
  }
  STAMP(1);
  X_ISSUE(0); X_ISSUE(1); X_ISSUE(2);
  const v4u rs_w = mk_srd(wqkv, D_ * 3 * D_ * 2);
  const u32 wvo = ((lane >> 2) * 1536 + 8 * (lane & 3)) * 2;
  const u32 wso = ((256 * (tg & 1) + 16 * (tg >> 1)) * 1536 + 32 * j2) * 2;
#define W_ISSUE(part, n) gldsi<((n) & 1) * 32 * 3072 + (part) * 1024, (n) * 1024 + ((part) == 0 ? 8192 : 0)>(rs_w, wvo, wso + (((n) >> 1) ^ rot) * (64 * 3072), xld)
  const bool p4 = j2 < 15;
  const int un0 = w < 4 ? 2 * w : 4 + w;
  f32x2 xf[16][4];
  u32 tagl;
  {
    u32 xa[4];
#pragma unroll
    for (int sp = 0; sp < 4; ++sp) { xa[sp] = LB + w * 16384 + c32 * 128 + (((2 * sp + hh) ^ ((c32 >> 1) & 7)) << 4); PIN(xa[sp]); }
    v4u gr[4];
    v2u gs;
#pragma unroll
    for (int q4 = 0; q4 < 4; ++q4) {
      if (q4 == 0) { X_ISSUE(3); WAITV(12); }
      if (q4 == 1) WAITV(8);
      if (q4 == 2) WAITV(4);
      if (q4 == 3) {
        WAITV(0);
        if (kh == 1) {
          PRIO(3);
          gs = ld64c(rs_ws, c32 * 8, WS_ROWST + (128 * m2 + 32 * tg) * 8);
#pragma unroll
          for (int n = 0; n < 4; ++n) gr[n] = ld128c(rs_ws, lane * 16, WS_GRAN + (n & 1) * 24576 + (tg * 2 + (n >> 1)) * 1024);
          PRIO(0);
        }
      }
      v4u t[4];
#pragma unroll
      for (int sp = 0; sp < 4; ++sp) t[sp] = lds128(xa[sp] + q4 * 4096);
#pragma unroll
      for (int sp = 0; sp < 4; ++sp) {
#pragma unroll
        for (int e = 0; e < 4; ++e) xf[4 * q4 + sp][e] = f32x2{bflo(t[sp][e]), bfhi(t[sp][e])};
        f32x2(&f)[4] = xf[4 * q4 + sp];
        asm volatile("" : "+v"(f[0]), "+v"(f[1]), "+v"(f[2]), "+v"(f[3]));
      }
      SB();
    }
    if (kh == 0) {
      if (w == 1) asm volatile("buffer_load_dword v251, %0, %1, %2 offen" ::"v"(lane * 128), "s"(mk_srd(wd, F_ * D_ * 2)), "s"(b * 5504) : "memory", "v251");
      if (w == 2) asm volatile("buffer_load_dword v251, %0, %1, %2 offen" ::"v"(l15 * 128), "s"(mk_srd(wo, D_ * D_ * 2)), "s"(b * 2048) : "memory", "v251");
    }
    PIN(tagv);
    PIN(cpf);
    tagl = tagv + 1u;
    STAMP(22);
    if (kh == 0) sfor<0, 8>([&](auto ic) AI { W_ISSUE(1, ic.v); });
    if (kh == 1) {
      asm volatile("s_waitcnt vmcnt(0)" : "+v"(gs), "+v"(gr[0]), "+v"(gr[1]), "+v"(gr[2]), "+v"(gr[3])::"memory");
      for (u32 spins = 0;; ++spins) {
        bool ok = gs.y == tagl;
#pragma unroll
        for (int n = 0; n < 4; ++n) ok = ok && gr[n].y == tagl && gr[n].w == tagl;
        if (__all(ok) || spins >= SPIN_CAP) break;
        if (spins) __builtin_amdgcn_s_sleep(4);
        gs = ld64c(rs_ws, c32 * 8, WS_ROWST + (128 * m2 + 32 * tg) * 8);
#pragma unroll
        for (int n = 0; n < 4; ++n) gr[n] = ld128c(rs_ws, lane * 16, WS_GRAN + (n & 1) * 24576 + (tg * 2 + (n >> 1)) * 1024);
        asm volatile("s_waitcnt vmcnt(0)" : "+v"(gs), "+v"(gr[0]), "+v"(gr[1]), "+v"(gr[2]), "+v"(gr[3])::"memory");
      }
      *(LAS u32*)(size_t)(LB + L_STATX + (tg * 32 + c32) * 4) = gs.x;
      const float one = tg >= 2 ? 1.0f : 0.0f;
#pragma unroll
      for (int n = 0; n < 2; ++n) {
        const u32 p = pk2(asf(gr[2 * n].x) + asf(gr[2 * n + 1].x), asf(gr[2 * n].z) + asf(gr[2 * n + 1].z));
        const u32 q = pk2(bflo(p) + one, bfhi(p) + one);
        sts64(LB + L_CS + tg * 1024 + (((2 * n + hh) ^ rot) << 8) + c32 * 8, v2u{q << 16, q & 0xffff0000u});
      }
    }
    BAR();
    STAMP(2);
    const u32 st = *(const LAS u32*)(size_t)(LB + L_STATX + (tg * 32 + c32) * 4);
    const float r = __builtin_amdgcn_rcpf(bfhi(st)), mr = -bflo(st) * r;
    const f32x2 r2 = {r, r}, mr2 = {mr, mr};
    if (kh == 1) sfor<0, 4>([&](auto ic) AI { W_ISSUE(2, ic.v); });
#pragma unroll
    for (int s = 0; s < 16; ++s)
#pragma unroll
      for (int e = 0; e < 4; ++e) fma2_ip(xf[s][e], r2, mr2);
  }
  const u32 tag = __builtin_amdgcn_readfirstlane(tagl);
  f32x16 zero;
#pragma unroll
  for (int i = 0; i < 16; ++i) zero[i] = 0.f;
  v4u hf[16];
  u32 wk = LB + kh * 16384 + (8 * hh + tq) * 64 + (g4 & 1) * 32 + tp * 8, wv = wk + L_B;
#define W_FRAG(base, s) cat(trd((base) + ((s) & 1) * 32768 + ((s) >> 1) * 1024), trd((base) + ((s) & 1) * 32768 + ((s) >> 1) * 1024 + 256))
  f32x16 ak = zero;
  {
    u32 ca = LB + L_CS + (256 * kh + 8 * hh) * 4, cda = ca + 2048 + (l15 >> 3) * 64 + (l15 & 7) * 4;
    PIN(wk); PIN(wv); PIN(ca); PIN(cda);
    float c1[8];
#pragma unroll
    for (int r = 0; r < 8; ++r) c1[r] = *(const LAS float*)(size_t)(cda + r * 128);
    f32x4 hs[2][2];
    v4u fw[2];
#define H_READ(s) do { hs[(s) & 1][0] = __builtin_bit_cast(f32x4, lds128(ca + (s) * 64)); hs[(s) & 1][1] = __builtin_bit_cast(f32x4, lds128(ca + (s) * 64 + 16)); } while (0)
#define H_FMA(s, hf_) do { float t0_ = hs[(s) & 1][hf_][0], t1_ = hs[(s) & 1][hf_][1], t2_ = hs[(s) & 1][hf_][2], t3_ = hs[(s) & 1][hf_][3]; \
    fmac_bc<8 * ((s) & 1) + 4 * (hf_) + 0>(t0_, c1[(s) >> 1], xf[s][2 * (hf_) + 0][0]); fmac_bc<8 * ((s) & 1) + 4 * (hf_) + 1>(t1_, c1[(s) >> 1], xf[s][2 * (hf_) + 0][1]); \
    fmac_bc<8 * ((s) & 1) + 4 * (hf_) + 2>(t2_, c1[(s) >> 1], xf[s][2 * (hf_) + 1][0]); fmac_bc<8 * ((s) & 1) + 4 * (hf_) + 3>(t3_, c1[(s) >> 1], xf[s][2 * (hf_) + 1][1]); \
    u32 p0_ = pk2(t0_, t1_), p1_ = pk2(t2_, t3_); PIN(p0_); PIN(p1_); \
    hf[s][2 * (hf_)] = p0_; hf[s][2 * (hf_) + 1] = p1_; } while (0)
    H_READ(0); H_READ(1);
    SB();
    H_FMA(0, 0); H_FMA(0, 1);
    if (kh == 0) WAITV(4);
    else sfor<4, 8>([&](auto ic) AI { W_ISSUE(2, ic.v); });
    RBAR();
    STAMP(3);
    fw[0] = W_FRAG(wk, 0);
    SB();
    sfor<0, 16>([&](auto ic) AI {
      constexpr int s = ic.v;
      if constexpr (s == 7) {
        if (kh == 0) {
          WAITV(0);
          if (p4 && tg != 0) {
            const v4u rs_g = mk_srd(wg, D_ * F_ * 2), rs_u = mk_srd(wu, D_ * F_ * 2);
            const u32 P = (tg - 1) * 64 + lane, gvo = (P / 12) * (F_ * 2) + (((P % 12) ^ (((P / 12) >> 2) & 1)) << 4);
            const u32 gso = 32 * m2 * (F_ * 2) + j2 * 192, gld = LB + L_T + tg * 1024 - 1024;
            gldsi<0, 0>(rs_g, gvo, gso, gld); gldsi<16 * F_ * 2, 3072>(rs_g, gvo, gso, gld);
            gldsi<0, 6144>(rs_u, gvo ^ 32, gso, gld); gldsi<16 * F_ * 2, 9216>(rs_u, gvo ^ 32, gso, gld);
          }
        }
        RBAR();
      }
      if constexpr (s < 15) fw[(s + 1) & 1] = W_FRAG(wk, s + 1);
      if constexpr (s < 14) H_READ((s + 2) & 15);
      SB();
      if constexpr (s < 15) H_FMA((s + 1) & 15, 0);
      SB();
      ak = mfma32(fw[s & 1], hf[s], ak);
      SB();
      if constexpr (s < 15) H_FMA((s + 1) & 15, 1);
      SB();
    });
  }
  STAMP(4);
  const v4u rs_wo = mk_srd(wo, D_ * D_ * 2);
  u32 dvo;
  {
    const u32 r8 = lane >> 3, pc = lane & 7;
    const u32 f = ((r8 >> 1) & 1) | ((w & 1) << 1);
    dvo = r8 * 1024 + ((((pc >> 1) ^ f) * 2 + (pc & 1)) << 4);
  }
#define SIGNAL(wv, cnt) do { if (tid >= (wv) * 64 && tid < (wv) * 64 + 4) __hip_atomic_fetch_add(ctl + (WS_CNT + (cnt) * CNT_STRIDE + lane * 128) / 4, 1u, RLX_AGENT); } while (0)
#define POLL(cnt, rep, target) do { PRIO(3); poll_cnt(ctl + (WS_CNT + (cnt) * CNT_STRIDE + (rep) * 128) / 4, target); PRIO(0); } while (0)
  v4u wf[2][16];
  const u32 wfo = WS_GT + (j2 * 12 + un0) * 16384;
#define WF_GET(n, lo, hi) _Pragma("unroll") for (int k_ = (lo); k_ < (hi); ++k_) wf[n][k_] = ld128c(rs_ws, lane * 16, wfo + (n) * 16384 + k_ * 1024)
#define WO_ISSUE(n) gldsi<(n) * 32768, ((n) >> 1) * 16384 + ((n) & 1) * 4096>(rs_wo, dvo, tg * 8192 + cb * 128, LB + tg * 1024)
  auto proj = [&](u32 base, auto hook) AI {
    f32x16 acc = zero;
    v4u fq[4];
#pragma unroll
    for (int s = 0; s < 3; ++s) fq[s] = W_FRAG(base, s);
    sfor<0, 16>([&](auto ic) AI {
      constexpr int s = ic.v;
      if constexpr (s + 3 < 16) fq[(s + 3) & 3] = W_FRAG(base, s + 3);
      hook(ic);
      SB();
      acc = mfma32(fq[s & 3], hf[s], acc);
      SB();
    });
    return acc;
  };
  v4u pf0, pf1;
  {
    const u32 sc = LB + tg * 16384 + 8192;
    const u32 pa = LB + L_WPHI + (tp & 1) * 512 + (4 * hh + tq) * 32 + (8 * (g4 & 1) + 4 * (tp >> 1)) * 2;
    const v4u pf = cat(trd(pa), trd(pa + 256));
    u32 m1 = 0u - ((lane >> 2) & 1);
    PIN(m1);
    pf1 = pf & m1; pf0 = pf ^ pf1;
    const u32 sg = kh ? 0x80008000u : 0u;
    const v4u pg0 = pf0 ^ v4u{sg, sg, sg, sg}, pg1 = pf1 ^ v4u{sg, sg, sg, sg};
    const v4u k0 = pack8(ak, 0), k1 = pack8(ak, 1);
    sts128(sc + (kh ^ 1) * 2048 + lane * 16, k0);
    sts128(sc + (kh ^ 1) * 2048 + 1024 + lane * 16, k1);
    f32x16 p = mfma32(pg0, k0, zero);
    p = mfma32(pg1, k1, p);
    if (kh == 1) WAITV(0);
    BAR();
    STAMP(23);
    {
      const v4u o0 = lds128(sc + kh * 2048 + lane * 16), o1 = lds128(sc + kh * 2048 + 1024 + lane * 16);
      p = mfma32(pg0, o0, p);
      p = mfma32(pg1, o1, p);
      SB();
    }
    auto feat = [&]() AI {
      u32 ph[8];
      softmax16(p, ph);
      const u32 row = sc + kh * 2048 + c32 * 64;
      sts128(row + (((2 * hh) ^ ((c32 >> 1) & 3)) << 4), v4u{ph[0], ph[1], ph[2], ph[3]});
      sts128(row + (((2 * hh + 1) ^ ((c32 >> 1) & 3)) << 4), v4u{ph[4], ph[5], ph[6], ph[7]});
    };
    if (kh == 0) feat();
    else sfor<0, 8>([&](auto ic) AI { W_ISSUE(0, ic.v); });
    {
      const f32x16 av = proj(wv, [](auto) {});
      const u32 row = sc + 4096 + kh * 2048 + c32 * 64 + 8 * hh;
#pragma unroll
      for (int i = 0; i < 4; ++i)
        sts64(row + ((i ^ ((c32 >> 1) & 3)) << 4), v2u{pk2(av[4 * i], av[4 * i + 1]), pk2(av[4 * i + 2], av[4 * i + 3])});
    }
    if (kh == 1) feat();
    u32 aa[2], ba[2];
#pragma unroll
    for (int t = 0; t < 2; ++t) {
      const u32 tok = 8 * g4 + 4 * t + tq;
      const u32 o = LB + 8192 + tok * 64 + (((2 * (tg >> 1) + (tp >> 1)) ^ ((tok >> 1) & 3)) << 4) + 8 * (tp & 1);
      aa[t] = o + (tg & 1) * 2048; ba[t] = o + 4096;
      PIN(aa[t]); PIN(ba[t]);
    }
    if (kh == 0) WAITV(0);
    BAR();
    STAMP(18);
    if (kh == 0) {
      PRIO(3);
      f32x4 kv0 = {0.f, 0.f, 0.f, 0.f}, kv1 = kv0, zz = kv0;
      const v4u ones = v4u{0x3f803f80u, 0x3f803f80u, 0x3f803f80u, 0x3f803f80u};
      v4u a[4], b0[4], b1[4];
#pragma unroll
      for (int ks = 0; ks < 4; ++ks) {
        a[ks] = cat(trd(aa[0] + ks * 16384), trd(aa[1] + ks * 16384));
        b0[ks] = cat(trd(ba[0] + ks * 16384), trd(ba[1] + ks * 16384));
        b1[ks] = cat(trd(ba[0] + ks * 16384 + 2048), trd(ba[1] + ks * 16384 + 2048));
      }
      SB();
#pragma unroll
      for (int ks = 0; ks < 4; ++ks) {
        kv0 = mfma16(a[ks], b0[ks], kv0); kv1 = mfma16(a[ks], b1[ks], kv1); zz = mfma16(a[ks], ones, zz);
      }
      kv0 += kv1;
      st64c(v2u{pk2(kv0[0], kv0[1]), pk2(kv0[2], kv0[3])}, rs_ws, lane * 8, WS_SLAB + ((j2 * 4 + w) * 16 + m2) * 512);
      if (l15 == 0) st64c(v2u{pk2(zz[0], zz[1]), pk2(zz[2], zz[3])}, rs_ws, g4 * 8, WS_Z + ((j2 * 4 + w) * 16 + m2) * 32);
      sfor<0, 8>([&](auto ic) AI { WO_ISSUE(ic.v); });
      WAITV(8);
      PRIO(0);
    } else {
      const u32 gb = LB + L_T + (4 * g4 + tq) * 192 + 8 * (tp & 1);
      const u32 ga = gb + (((2 * tg + (tp >> 1)) ^ (g4 & 1)) << 4), gc = gb + (tp >> 1) * 6144 + (((8 + tg) ^ (g4 & 1) ^ (tp & 2)) << 4);
      const v4u gt0 = cat(trd(ga), trd(ga + 3072)), gt1 = cat(trd((ga ^ 32) + 6144), trd((ga ^ 32) + 9216)), gt2 = cat(trd(gc), trd(gc + 3072));
      const u32 gto = p4 ? WS_GT + j2 * (12 * 16384) + m2 * 1024 : WS_BYTES;
      st128c(gt0, rs_ws, lane * 16, gto + tg * 32768);
      st128c(gt1, rs_ws, lane * 16, gto + tg * 32768 + 16384);
      st128c(gt2, rs_ws, lane * 16, gto + (8 + tg) * 16384);
      WAITV(0);
    }
    RBAR();
    SIGNAL(1, 0 * 16 + j2);
  }
  STAMP(5);
  const u32 orow = (128 * pnl + 64 * rh + 16 * tg + l15) * 1024 + (64 * cb + 32 * kh + 16 * (g4 & 1) + 8 * (g4 >> 1)) * 2;
  v4u xres;
  u32 phq[16];
  {
    if (kh == 1) sfor<8, 16>([&](auto ic) AI { WO_ISSUE(ic.v); });
    xres = ld128(rs_x, orow, 0);
    gu32* const e2 = ctl + (WS_CNT + (0 * 16 + j2) * CNT_STRIDE + (m2 & 3) * 128) / 4;
    const f32x16 aq = proj(wv + 8192, [&](auto ic) AI {
      if constexpr (ic.v == 7 || ic.v == 11 || ic.v == 15) { if (w == 0) poll_kick<((ic.v - 7) >> 2) & 3>(e2); }
    });
    const v4u q0 = pack8(aq, 0), q1 = pack8(aq, 1);
    const u32 ea = LB + L_QEX + tg * 2048 + lane * 16;
    f32x16 p;
    if (kh == 0) { sts128(ea, q0); sts128(ea + 1024, q1); }
    else {
      p = mfma32(pf0, q0, zero);
      p = mfma32(pf1, q1, p);
      if (lane < 16) *(LAS u32*)(size_t)(LB + L_KVT + 34 * 64 + lane * 4) = 0u;
    }
    if (w == 0) poll_wait(e2, 16u);
    BAR();
    STAMP(6);
    if (kh == 1) {
      const v4u o0 = lds128(ea), o1 = lds128(ea + 1024);
      p = mfma32(pf0, o0, p);
      p = mfma32(pf1, o1, p);
      hedgehog(p, phq);
    }
  }
  if (kh == 0) {
    PRIO(3);
    v4u sl[8];
    v2u zl = ld64c(rs_ws, lane * 8, WS_Z + (j2 * 4 + w) * 512);
#pragma unroll
    for (int m = 0; m < 8; ++m) sl[m] = ld128c(rs_ws, lane * 16, WS_SLAB + ((j2 * 4 + w) * 8 + m) * 1024);
    WF_GET(0, 0, 4); WF_GET(1, 0, 4);
    asm volatile("s_waitcnt vmcnt(16)" : "+v"(zl)::"memory");
    const int hd = w >> 1, fh = w & 1;
    {
      auto r = __builtin_amdgcn_permlane32_swap(zl.x, zl.y, false, false);
      float z0 = bflo(r[0]) + bflo(r[1]), z1 = bfhi(r[0]) + bfhi(r[1]);
      z0 = sum16(sum8(sum4(z0))); z1 = sum16(sum8(sum4(z1)));
      *(LAS u32*)(size_t)(LB + L_KVT + (hd * 17 + 16) * 64 + (16 * fh + 4 * tp + 2 * hh) * 2) = pk2(z0, z1);
    }
    float sa[8] = {0.f, 0.f, 0.f, 0.f, 0.f, 0.f, 0.f, 0.f};
    sfor<0, 8>([&](auto ic) AI {
      constexpr int m = ic.v;
      asm volatile("s_waitcnt vmcnt(%1)" : "+v"(sl[m]) : "i"(15 - m) : "memory");
#pragma unroll
      for (int e = 0; e < 4; ++e) { sa[2 * e] += bflo(sl[m][e]); sa[2 * e + 1] += bfhi(sl[m][e]); }
    });
#pragma unroll
    for (int e = 0; e < 4; ++e) {
      auto r = __builtin_amdgcn_permlane32_swap(asu(sa[e]), asu(sa[e + 4]), false, false);
      sa[e] = asf(r[0]) + asf(r[1]);
    }
    sts64(LB + L_KVT + (hd * 17 + 2 * (lane & 7) + hh) * 64 + (16 * fh + 4 * (c32 >> 3)) * 2, v2u{pk2(sa[0], sa[1]), pk2(sa[2], sa[3])});
    PRIO(0);
  }
  u32 ka, za;
  {
    const bool own = ((lane >> 2) & 1) == hh;
    const u32 d = (c32 & 3) + 4 * (c32 >> 3);
    ka = LB + L_KVT + (own ? hh * 17 + d : 34) * 64; za = LB + L_KVT + (own ? hh * 17 + 16 : 34) * 64;
    PIN(ka); PIN(za);
  }
  BAR();
  STAMP(7);
  if (kh == 1) {
    PRIO(3);
    f32x16 num = zero, den = zero;
    v4u a[4], z[4];
#pragma unroll
    for (int s = 0; s < 4; ++s) z[s] = lds128(za + s * 16);
#pragma unroll
    for (int s = 0; s < 4; ++s) a[s] = lds128(ka + s * 16);
    SB();
#pragma unroll
    for (int s = 0; s < 4; ++s) den = mfma32(z[s], v4u{phq[4 * s], phq[4 * s + 1], phq[4 * s + 2], phq[4 * s + 3]}, den);
    SB();
#pragma unroll
    for (int s = 0; s < 4; ++s) num = mfma32(a[s], v4u{phq[4 * s], phq[4 * s + 1], phq[4 * s + 2], phq[4 * s + 3]}, num);
    const float r = __builtin_amdgcn_rcpf(den[0] + 1e-6f);
    f32x16 o;
#pragma unroll
    for (int i = 0; i < 16; ++i) o[i] = num[i] * r;
    const u32 vo = (c32 >> 4) * 16384 + hh * 512 + l15 * 16;
    const u32 so = WS_O + ((8 * m2 + 2 * tg) * 16 + j2) * 1024;
    st128c(pack8(o, 0), rs_ws, vo, so);
    st128c(pack8(o, 1), rs_ws, vo + 256, so);
    WF_GET(0, 0, 8);
    WAITV(8);
    PRIO(0);
  }
  RBAR();
  SIGNAL(1, 1 * 16 + m2);
  STAMP(8);
  if (kh == 1) {
    v4u gr[5];
    for (u32 spins = 0;; ++spins) {
#pragma unroll
      for (int n = 0; n < 4; ++n) gr[n] = ld128c(rs_ws, lane * 16, WS_GRAN + (n & 1) * 24576 + (12 + tg * 2 + (n >> 1)) * 1024);
      gr[4] = ld128c(rs_ws, c32 * 16 + hh * 24576, WS_GRAN + ((tg & 1) ? 40 + cb : 16 + cb) * 512);
      asm volatile("s_waitcnt vmcnt(0)" : "+v"(gr[0]), "+v"(gr[1]), "+v"(gr[2]), "+v"(gr[3]), "+v"(gr[4])::"memory");
      bool ok = true;
#pragma unroll
      for (int n = 0; n < 5; ++n) ok = ok && gr[n].y == tag && gr[n].w == tag;
      if (__all(ok) || spins >= SPIN_CAP) break;
    }
    const float one = tg >= 2 ? 1.0f : 0.0f;
    const u32 vperm = g4 * 64 + ((lane >> 1) & 3) * 16 + ((lane >> 3) & 1) * 8 + (lane & 1) * 4;
#pragma unroll
    for (int n = 0; n < 2; ++n) {
      const u32 p = pk2(asf(gr[2 * n].x) + asf(gr[2 * n + 1].x), asf(gr[2 * n].z) + asf(gr[2 * n + 1].z));
      *(LAS u32*)(size_t)(LB + L_VEC + 1024 + (tg * 2 + n) * 256 + vperm) = pk2(bflo(p) + one, bfhi(p) + one);
    }
    *(LAS u32*)(size_t)(LB + L_VEC + (tg & 1) * 128 + (vperm & 127)) = pk2(sum32(asf(gr[4].x)), sum32(asf(gr[4].z)));
  } else {
    WF_GET(0, 4, 8);
    WAITV(4);
    if (w == 0) POLL(1 * 16 + pnl, bi & 3, 16u);
  }
  u32 wra[4], wo_a[4];
#pragma unroll
  for (int c = 0; c < 4; ++c) {
    wra[c] = LB + (8 * g4 + tq) * 128 + tp * 8 + ((c ^ (2 * kh) ^ (((tq >> 1) & 1) | ((g4 & 1) << 1))) << 5);
    wo_a[c] = wra[c] + kh * 65536;
    PIN(wo_a[c]);
  }
  BAR();
  STAMP(9);
  f32x4 cs0 = {0.f, 0.f, 0.f, 0.f}, cs1 = {0.f, 0.f, 0.f, 0.f};
  u32 cva = LB + L_VEC + 2048 - (l15 >> 3) * 1024 + g4 * 16;
  u32 x1p[4];
  {
    v4u of[8];
#pragma unroll
    for (int i = 0; i < 8; ++i) of[i] = ld128c(rs_ws, lane * 16, WS_O + ((8 * pnl + 4 * rh + tg) * 16 + 8 * kh + i) * 1024);
#define WO_FRAG(i, c) cat(trd(wo_a[c] + ((i) >> 1) * 16384 + ((i) & 1) * 4096), trd(wo_a[c] + ((i) >> 1) * 16384 + ((i) & 1) * 4096 + 512))
    const u32 va = LB + L_VEC + kh * 64 + g4 * 16;
    const v4u ga4 = lds128(va), cb4 = lds128(va + 2048 + cb * 128);
    v4u fa[4][4];
#pragma unroll
    for (int i = 0; i < 4; ++i)
#pragma unroll
      for (int c = 0; c < 4; ++c) fa[i][c] = WO_FRAG(i, c);
    PIN(xres);
    auto r0 = __builtin_amdgcn_permlane16_swap(xres.x, xres.z, false, false);
    auto r1 = __builtin_amdgcn_permlane16_swap(xres.y, xres.w, false, false);
    f32x2 xv[4] = {unp(r0[0]), unp(r1[0]), unp(r0[1]), unp(r1[1])};
    f32x2 gv[4] = {unp(ga4.x), unp(ga4.y), unp(ga4.z), unp(ga4.w)};
    f32x2 cv[4] = {unp(cb4.x), unp(cb4.y), unp(cb4.z), unp(cb4.w)};
#pragma unroll
    for (int e = 0; e < 4; ++e) { PIN(xv[e]); PIN(gv[e]); PIN(cv[e]); }
    u32 ea = LB + L_EX3 + tg * 4096 + lane * 16 + kh * 2048;
    PIN(ea);
    f32x4 acc[4];
    sfor<0, 8>([&](auto ic) AI {
      constexpr int i = ic.v;
      waitv<7 - i>();
      pin4(of[i]);
      SB();
#pragma unroll
      for (int c = 3; c >= 0; --c) {
        if constexpr (i == 0) mfma16_z(acc[c], fa[0][c], of[0]);
        else mfma16_ip(acc[c], fa[i & 3][c], of[i]);
        if constexpr (i + 4 < 8) fa[i & 3][c] = WO_FRAG(i + 4, c);
        SB();
      }
    });
    STAMP(10);
    asm volatile("s_nop 4" ::: "memory");
    SB();
    sts128(ea + 1024, __builtin_bit_cast(v4u, acc[3]));
    sts128(ea, __builtin_bit_cast(v4u, acc[2]));
    WF_GET(0, 8, 12);
    BAR();
    const f32x4 t0 = acc[0] + __builtin_bit_cast(f32x4, lds128(ea ^ 2048));
    const f32x4 t1 = acc[1] + __builtin_bit_cast(f32x4, lds128((ea ^ 2048) + 1024));
    f32x2 sum = {0.f, 0.f}, sq = {0.f, 0.f};
    v4u y;
#pragma unroll
    for (int e = 0; e < 4; ++e) {
      const f32x2 at = unp(e < 2 ? pk2(t0[2 * e], t0[2 * e + 1]) : pk2(t1[2 * e - 4], t1[2 * e - 3]));
      const f32x2 gp = gv[e] * at, xs = xv[e] + unp(pk2(gp[0], gp[1]));
      x1p[e] = pk2(xs[0], xs[1]);
      const f32x2 x1 = unp(x1p[e]), yy = x1 * cv[e];
      y[e] = pk2(yy[0], yy[1]);
      sum += x1; sq = fma2(x1, x1, sq);
    }
    st128c(y, rs_ws, lane * 16, WS_Y1 + ((8 * pnl + 4 * rh + tg) * 16 + 2 * cb + kh) * 1024);
    const float s1 = sum32(sum16(sum[0] + sum[1])), q1 = sum32(sum16(sq[0] + sq[1]));
    st64c(v2u{asu(s1), asu(q1)}, rs_ws, lane < 16 ? lane * 8 : WS_BYTES, WS_STAT + ((8 * pnl + 4 * rh + tg) * 16 + 2 * cb + kh) * 128);
    WF_GET(0, 12, 16);
    WAITV(4);
  }
  BAR();
  SIGNAL(4, 2 * 16 + pnl);
  STAMP(11);
  const v4u rs_wd = mk_srd(wd, F_ * D_ * 2);
  const u32 dso = tg * 8192 + cb * 128, dld = LB + tg * 1024;
#define WD_ISSUE(i, h) gldsi<(22 * (h) + (i)) * 32768, (2 * wd_sg(i) + (h)) * 4096>(rs_wd, dvo, dso, dld)
#define WD_ISSUE_K(i) gldsi<(i) * 32768, 2 * wd_sg(i) * 4096>(rs_wd, dvo, dso + kh * (22 * 32768), dld + kh * 4096)
  if (p4) {
    auto cs_part = [&](auto lo, auto hi) AI {
      v4u cf[hi.v - lo.v];
#pragma unroll
      for (int ks = lo.v; ks < hi.v; ++ks) { pin4(wf[0][ks]); cf[ks - lo.v] = lds128(cva + ks * 64); }
      SB();
#pragma unroll
      for (int ks = lo.v; ks < hi.v; ++ks) cs0 = mfma16(wf[0][ks], cf[ks - lo.v], cs0);
      pinf(cs0);
    };
    PIN(cva);
    if (kh == 0) { WF_GET(1, 4, 10); }
    gu32* const e4 = ctl + (WS_CNT + (2 * 16 + m2) * CNT_STRIDE + (bi & 3) * 128) / 4;
    if (w == 7) poll_kick<0>(e4);
    cs_part(IC<0>{}, IC<6>{});
    if (w == 7) poll_kick<1>(e4);
    cs_part(IC<6>{}, IC<12>{});
    if (w == 7) { poll_kick<2>(e4); poll_wait(e4, 16u); }
    STAMP(21);
    RBAR();
    STAMP(12);
    u32 ya0 = LB + lane * 16, ya1 = ya0 + 65536;
    PIN(ya0); PIN(ya1);
#define Y_READ(i) lds128(((i) < 64 ? ya0 : ya1) + ((i) & 63) * 1024)
    constexpr int PD = 6, RING = 7;
    const u32 hvo = l15 * 16, hso = WS_HH + 8 * m2 * (43 * 1024);
    if (w >= 4) {
      f32x4 acc0[8];
      v4u st[4], yr[RING];
      const u32 yso = WS_Y1 + (8 * m2 + 2 * tg) * 16384, yld = LB + tg * 2048;
#define Y_ISSUE(n) gldsic<((n) & 1) * 16384 + ((n) >> 1) * 1024, (((n) >> 1) * 8 + ((n) & 1)) * 1024>(rs_ws, lane * 16, yso, yld)
      PRIO(2);
      sfor<0, 12>([&](auto ic) AI { Y_ISSUE(ic.v); });
      WAITV(12);
      cs_part(IC<12>{}, IC<16>{});
      WAITV(8);
      RBAR();
      STAMP(19);
#pragma unroll
      for (int i = 0; i < PD; ++i) yr[i] = Y_READ(i);
      sfor<0, 128>([&](auto ic) AI {
        constexpr int i = ic.v, rd = i + PD;
        if constexpr (rd < 128) {
          if constexpr ((rd & 15) == 0) { waitv<ops_before(i) - (rd == 112 ? 46 : 4 * (rd >> 4) + 4)>(); RBAR(); }
          if constexpr (rd == 64) STAMP(20);
          yr[rd % RING] = Y_READ(rd);
        }
        if constexpr (i < 20) Y_ISSUE(12 + i);
        if constexpr (i >= 20 && i < 24) st[i - 20] = ld128c(rs_ws, lane * 16, WS_STAT + (8 * m2 + 2 * tg) * 2048 + (i - 20) * 1024);
        if constexpr (wd_slot(i) >= 0) {
          static_assert((wd_slot(i) >> 1) - 3 <= 2 * (rd >> 4) - 2, "slot still holds activations");
          WD_ISSUE(wd_slot(i) >> 1, wd_slot(i) & 1);
        }
        if constexpr (i == 76) {
          waitv<ops_before(i) - 36>();
          pin4(st[0]); pin4(st[1]); pin4(st[2]); pin4(st[3]);
          const f32x4 t0 = __builtin_bit_cast(f32x4, st[0]) + __builtin_bit_cast(f32x4, st[1]);
          const f32x4 t1 = __builtin_bit_cast(f32x4, st[2]) + __builtin_bit_cast(f32x4, st[3]);
          float t[4], q[2];
#pragma unroll
          for (int e = 0; e < 4; ++e) {
            auto r = __builtin_amdgcn_permlane32_swap(asu(t0[e]), asu(t1[e]), false, false);
            t[e] = asf(r[0]) + asf(r[1]);
          }
#pragma unroll
          for (int e = 0; e < 2; ++e) {
            auto r = __builtin_amdgcn_permlane16_swap(asu(t[e]), asu(t[e + 2]), false, false);
            q[e] = sum8(asf(r[0]) + asf(r[1]));
          }
          float m0, d0;
          ln_stats(q[0], q[1], m0, d0);
          sts64(LB + L_STAT2 + (2 * tg + hh) * 128 + (lane & 7) * 16 + (g4 & 1) * 8, v2u{asu(-m0), asu(__builtin_amdgcn_rcpf(d0))});
        }
        if constexpr ((i & 1) == 0) pin4(yr[(i + 1) % RING]);
        SB();
        if constexpr (i < 8) mfma16_z(acc0[i], wf[0][0], yr[i % RING]);
        else mfma16_ip(acc0[i & 7], wf[0][i >> 3], yr[i % RING]);
        SB();
      });
      SB();
      asm volatile("s_nop 7" ::: "memory");
      SB();
      PRIO(0);
      STAMP(13);
      {
        v2u sv[8];
#pragma unroll
        for (int t = 0; t < 8; ++t) sv[t] = lds64(LB + L_STAT2 + t * 128 + l15 * 8);
        const f32x2 cwa = {bcast<0>(cs0[0]), bcast<0>(cs0[1])}, cwb = {bcast<0>(cs0[2]), bcast<0>(cs0[3])};
        const f32x2 swa = {bcast<8>(cs0[0]), bcast<8>(cs0[1])}, swb = {bcast<8>(cs0[2]), bcast<8>(cs0[3])};
        f32x2 va[8], vb[8];
#pragma unroll
        for (int t = 0; t < 8; ++t) {
          const f32x2 m = {asf(sv[t].x), asf(sv[t].x)}, d = {asf(sv[t].y), asf(sv[t].y)};
          va[t] = fma2(fma2(m, cwa, f32x2{acc0[t][0], acc0[t][1]}), d, swa);
          vb[t] = fma2(fma2(m, cwb, f32x2{acc0[t][2], acc0[t][3]}), d, swb);
        }
        u32 hp[4][2];
#pragma unroll
        for (int t2 = 0; t2 < 4; ++t2) {
          auto r0 = __builtin_amdgcn_permlane32_swap(asu(va[2 * t2][0]), asu(va[2 * t2 + 1][0]), false, false);
          auto r1 = __builtin_amdgcn_permlane32_swap(asu(va[2 * t2][1]), asu(va[2 * t2 + 1][1]), false, false);
          auto r2 = __builtin_amdgcn_permlane32_swap(asu(vb[2 * t2][0]), asu(vb[2 * t2 + 1][0]), false, false);
          auto r3 = __builtin_amdgcn_permlane32_swap(asu(vb[2 * t2][1]), asu(vb[2 * t2 + 1][1]), false, false);
          hp[t2][0] = pk2(asf(r0[0]) * asf(r0[1]), asf(r1[0]) * asf(r1[1]));
          hp[t2][1] = pk2(asf(r2[0]) * asf(r2[1]), asf(r3[0]) * asf(r3[1]));
        }
        const int ug = 12 * j2 + un0;
        const u32 so = ug < 172 ? hso + ug * 256 : WS_BYTES;
#pragma unroll
        for (int qd = 0; qd < 2; ++qd) {
          auto r0 = __builtin_amdgcn_permlane16_swap(hp[2 * qd][0], hp[2 * qd + 1][0], false, false);
          auto r1 = __builtin_amdgcn_permlane16_swap(hp[2 * qd][1], hp[2 * qd + 1][1], false, false);
          st128c(v4u{r0[0], r1[0], r0[1], r1[1]}, rs_ws, hvo + (2 * (g4 & 1) + (g4 >> 1)) * (43 * 1024), so + qd * (4 * 43 * 1024));
        }
      }
#pragma unroll
      for (int ks = 0; ks < 16; ++ks) { asm volatile("" : "=v"(wf[0][ks])); asm volatile("" : "=v"(wf[1][ks])); }
      asm volatile("" : "=v"(cs0), "=v"(cs1));
    }
    int w2 = w;
    asm volatile("" : "+s"(w2));
    if (w2 < 4) {
      f32x4 acc0[8], acc1[8];
      v4u yr[RING], cfr;
      WAITV(6);
      cs_part(IC<12>{}, IC<16>{});
      WAITV(0);
#pragma unroll
      for (int ks = 0; ks < 10; ++ks) PIN(wf[1][ks]);
      RBAR();
      STAMP(19);
#pragma unroll
      for (int i = 0; i < PD; ++i) yr[i] = Y_READ(i);
      sfor<0, 128>([&](auto ic) AI {
        constexpr int i = ic.v, rd = i + PD;
        if constexpr (i == 80) { WAITV(0); _Pragma("unroll") for (int ks = 10; ks < 16; ++ks) pin4(wf[1][ks]); }
        if constexpr ((i & 1) == 0) pin4(yr[(i + 1) % RING]);
        SB();
        if constexpr (i < 8) mfma16_z(acc0[i], wf[0][0], yr[i % RING]);
        else mfma16_ip(acc0[i & 7], wf[0][i >> 3], yr[i % RING]);
        SB();
        if constexpr (rd < 128) {
          if constexpr ((rd & 15) == 0) RBAR();
          if constexpr (rd == 64) STAMP(20);
          yr[rd % RING] = Y_READ(rd);
        }
        SB();
        if constexpr (i < 8) mfma16_z(acc1[i], wf[1][0], yr[i % RING]);
        else mfma16_ip(acc1[i & 7], wf[1][i >> 3], yr[i % RING]);
        SB();
        if constexpr ((i & 7) == 0) cfr = lds128(cva + (i >> 3) * 64);
        if constexpr ((i & 7) == 6) mfma16_ip(cs1, wf[1][i >> 3], cfr);
        if constexpr (i >= 36 && i < 60 && (i & 3) == 2) { WF_GET(1, 10 + ((i - 36) >> 2), 11 + ((i - 36) >> 2)); }
        SB();
      });
      SB();
      asm volatile("s_nop 7" ::: "memory");
      SB();
      STAMP(13);
      {
        v2u sv[8];
#pragma unroll
        for (int t = 0; t < 8; ++t) sv[t] = lds64(LB + L_STAT2 + t * 128 + l15 * 8);
        const f32x2 cga = {bcast<0>(cs0[0]), bcast<0>(cs0[1])}, cgb = {bcast<0>(cs0[2]), bcast<0>(cs0[3])};
        const f32x2 sga = {bcast<8>(cs0[0]), bcast<8>(cs0[1])}, sgb = {bcast<8>(cs0[2]), bcast<8>(cs0[3])};
        const f32x2 cua = {bcast<0>(cs1[0]), bcast<0>(cs1[1])}, cub = {bcast<0>(cs1[2]), bcast<0>(cs1[3])};
        const f32x2 sua = {bcast<8>(cs1[0]), bcast<8>(cs1[1])}, sub = {bcast<8>(cs1[2]), bcast<8>(cs1[3])};
        const int ug = 12 * j2 + un0;
        const u32 so = ug < 172 ? hso + ug * 256 : WS_BYTES;
        const u32 vo = hvo + (g4 & 1) * (43 * 1024) + (g4 >> 1) * 256;
#pragma unroll
        for (int t2 = 0; t2 < 4; ++t2) {
          u32 hp[2][2];
#pragma unroll
          for (int k = 0; k < 2; ++k) {
            const int t = 2 * t2 + k;
            const f32x2 m = {asf(sv[t].x), asf(sv[t].x)}, d = {asf(sv[t].y), asf(sv[t].y)};
            const f32x2 pa = fma2(fma2(m, cga, f32x2{acc0[t][0], acc0[t][1]}), d, sga) * fma2(fma2(m, cua, f32x2{acc1[t][0], acc1[t][1]}), d, sua);
            const f32x2 pb = fma2(fma2(m, cgb, f32x2{acc0[t][2], acc0[t][3]}), d, sgb) * fma2(fma2(m, cub, f32x2{acc1[t][2], acc1[t][3]}), d, sub);
            hp[k][0] = pk2(pa[0], pa[1]); hp[k][1] = pk2(pb[0], pb[1]);
          }
          auto r0 = __builtin_amdgcn_permlane16_swap(hp[0][0], hp[1][0], false, false);
          auto r1 = __builtin_amdgcn_permlane16_swap(hp[0][1], hp[1][1], false, false);
          st128c(v4u{r0[0], r1[0], r0[1], r1[1]}, rs_ws, vo, so + t2 * (2 * 43 * 1024));
        }
      }
    }
  } else {
    sfor<0, 16>([&](auto ic) AI { WD_ISSUE_K(ic.v); });
    WAITV(0);
    RBAR();
  }
  {
    u32 da0[4], da1[4], da2[4];
#pragma unroll
    for (int c = 0; c < 4; ++c) { da0[c] = wra[c] + kh * 4096; da1[c] = da0[c] + 65536; da2[c] = da0[c] + 131072; PIN(da0[c]); PIN(da1[c]); PIN(da2[c]); }
#define D_ADDR(i, c) (wd_sg(i) < 8 ? da0[c] + wd_sg(i) * 8192 : (wd_sg(i) < 16 ? da1[c] + (wd_sg(i) - 8) * 8192 : da2[c] + (wd_sg(i) - 16) * 8192))
#define D_READ(i, c) cat(trd(D_ADDR(i, c)), trd(D_ADDR(i, c) + 512))
    v4u fa[5][4];
#pragma unroll
    for (int i = 0; i < 5; ++i)
#pragma unroll
      for (int c = 0; c < 4; ++c) fa[i][c] = D_READ(i, c);
    const v4u gb4 = lds128(LB + L_VEC + 128 + kh * 64 + g4 * 16);
    WAITV(0);
    BAR();
    if (p4) SIGNAL(1, 3 * 16 + m2);
    STAMP(14);
    if (w == 0) POLL(3 * 16 + pnl, bi & 3, 15u);
    sfor<16, 22>([&](auto ic) AI { WD_ISSUE_K(ic.v); });
    RBAR();
    STAMP(15);
    constexpr int HA = 8;
    v4u hf2[HA + 1];
    const u32 hso = WS_HH + (8 * pnl + 4 * rh + tg) * (43 * 1024) + kh * (22 * 1024);
#define H_GET(i) hf2[(i) % (HA + 1)] = ld128c(rs_ws, lane * 16, (i) < 21 ? hso + (i) * 1024 : hso + (21 - kh) * 1024)
#pragma unroll
    for (int i = 0; i < HA; ++i) H_GET(i);
    f32x2 gv[4] = {unp(gb4.x), unp(gb4.y), unp(gb4.z), unp(gb4.w)}, xv[4] = {unp(x1p[0]), unp(x1p[1]), unp(x1p[2]), unp(x1p[3])};
#pragma unroll
    for (int e = 0; e < 4; ++e) { PIN(gv[e]); PIN(xv[e]); }
    u32 ea = LB + tg * 4096 + lane * 16 + kh * 2048;
    PIN(ea);
    f32x4 acc[4];
    sfor<0, 22>([&](auto ic) AI {
      constexpr int i = ic.v;
      if constexpr (i == 3 || i == 8 || i == 13) RBAR();
      if constexpr (i + HA < 22) H_GET(i + HA);
      waitv<(i + HA < 22 ? HA : 21 - i)>();
      pin4(hf2[i % (HA + 1)]);
      SB();
#pragma unroll
      for (int c = 3; c >= 0; --c) {
        if constexpr (i == 0) mfma16_z(acc[c], fa[0][c], hf2[0]);
        else mfma16_ip(acc[c], fa[i % 5][c], hf2[i % (HA + 1)]);
        if constexpr (i + 5 < 22) fa[i % 5][c] = D_READ(i + 5, c);
        SB();
      }
    });
    STAMP(16);
    asm volatile("s_nop 4" ::: "memory");
    SB();
    sts128(ea + 1024, __builtin_bit_cast(v4u, acc[3]));
    sts128(ea, __builtin_bit_cast(v4u, acc[2]));
    BAR();
    const f32x4 t0 = acc[0] + __builtin_bit_cast(f32x4, lds128(ea ^ 2048));
    const f32x4 t1 = acc[1] + __builtin_bit_cast(f32x4, lds128((ea ^ 2048) + 1024));
    u32 op[4];
#pragma unroll
    for (int e = 0; e < 4; ++e) {
      const f32x2 gp = gv[e] * unp(e < 2 ? pk2(t0[2 * e], t0[2 * e + 1]) : pk2(t1[2 * e - 4], t1[2 * e - 3]));
      const f32x2 o2 = xv[e] + unp(pk2(gp[0], gp[1]));
      op[e] = pk2(o2[0], o2[1]);
    }
    auto r0 = __builtin_amdgcn_permlane16_swap(op[0], op[2], false, false);
    auto r1 = __builtin_amdgcn_permlane16_swap(op[1], op[3], false, false);
    *(GAS v4u*)((GAS char*)out + orow) = v4u{r0[0], r1[0], r0[1], r1[1]};
  }
  if (b == 0 && tid == 0) __hip_atomic_store(ctl + WS_EPOCH / 4, tag, RLX_AGENT);
  STAMP(17);
  { const u32 te = (u32)__builtin_amdgcn_s_memrealtime(); const u32 d = lk1 ? 2u * (lk1 - lk0) : 0u; while ((u32)__builtin_amdgcn_s_memrealtime() - te < d) __builtin_amdgcn_s_sleep(1); }
}

extern "C" void kernel_launch(void* const* d_in, const int* in_sizes, int n_in, void* d_out, int out_size, void* d_ws, size_t ws_size,
                              hipStream_t stream) {
  (void)in_sizes; (void)n_in; (void)out_size; (void)ws_size;
  (void)hipFuncSetAttribute((const void*)fused_block, hipFuncAttributeMaxDynamicSharedMemorySize, L_TOTAL);
  fused_block<<<256, 512, L_TOTAL, stream>>>((const u16*)d_in[0], (const u16*)d_in[1], (const u16*)d_in[2], (const u16*)d_in[3],
                                             (const u16*)d_in[5], (const u16*)d_in[6], (const u16*)d_in[7], (const u16*)d_in[8],
                                             (const u16*)d_in[9], (u16*)d_out, (char*)d_ws);
}
```

```cpp
#include <hip/hip_runtime.h>

typedef unsigned short u16;
typedef unsigned int u32;
typedef u32 v4u __attribute__((ext_vector_type(4)));
typedef u32 v2u __attribute__((ext_vector_type(2)));
typedef float f32x2 __attribute__((ext_vector_type(2)));
typedef float f32x4 __attribute__((ext_vector_type(4)));
typedef float f32x16 __attribute__((ext_vector_type(16)));
typedef short bf16x8 __attribute__((ext_vector_type(8)));
typedef short s16x4 __attribute__((ext_vector_type(4)));
typedef __bf16 bf16x2 __attribute__((ext_vector_type(2)));
#define GAS __attribute__((address_space(1)))
#define LAS __attribute__((address_space(3)))
#define DI __device__ __forceinline__
#define RLX_AGENT __ATOMIC_RELAXED, __HIP_MEMORY_SCOPE_AGENT
typedef GAS u32 gu32;

constexpr int S_ = 2048, D_ = 512, F_ = 1376;
constexpr int WS_EPOCH = 0, WS_CNT = 983040, CNT_STRIDE = 512;
constexpr int WS_GRAN = 32768, WS_ROWST = 98304, WS_MFLAG = 20480, WS_SLAB = 131072, WS_Z = 655360, WS_STAT = 720896;
constexpr int WS_O = 1 << 20, WS_Y1 = 3 << 20, WS_HH = 5 << 20, WS_GT = 11 << 20, WS_DBG = 15 << 20, WS_BYTES = 16 << 20;
constexpr int L_B = 65536, L_T = 131072, L_MISC = 155648, L_TOTAL = 163840;
constexpr int L_CS = L_MISC, L_VEC = L_MISC, L_RED = L_MISC + 4608, L_STATX = L_MISC + 4096, L_STAT2 = L_MISC + 4096, L_CWSW = L_MISC + 5120;
constexpr int L_ARR = L_MISC + 6912, L_WPHI = L_MISC + 7168;
constexpr int L_KVT = 139264, L_QEX = 147456, L_EX3 = 139264;
constexpr unsigned CODE_SLICES = 20;
constexpr unsigned SPIN_CAP = 1u << 18;

DI float asf(u32 v) { return __builtin_bit_cast(float, v); }
DI u32 asu(float v) { return __builtin_bit_cast(u32, v); }
DI u32 asu(bf16x2 v) { return __builtin_bit_cast(u32, v); }
DI float bf2f(u32 b) { return asf(b << 16); }
DI float bflo(u32 p) { return asf(p << 16); }
DI float bfhi(u32 p) { return asf(p & 0xffff0000u); }
DI u32 pk2(float a, float b) { f32x2 v = {a, b}; return asu(__builtin_convertvector(v, bf16x2)); }
DI float rb(float a) { return bflo(pk2(a, a) & 0xffffu); }
DI v4u mk_srd(const void* p, u32 bytes) {
  unsigned long long a = (unsigned long long)p;
  return v4u{(u32)a, (u32)(a >> 32) & 0xffffu, bytes, 0x00020000u};
}
DI void glds(v4u rs, u32 voff, u32 soff, u32 ldsaddr) {
  asm volatile("s_mov_b32 m0, %3\n\ts_nop 0\n\tbuffer_load_dwordx4 %0, %1, %2 offen lds" ::"v"(voff), "s"(rs), "s"(soff), "s"(ldsaddr) : "memory");
}
template <int SO, int LO> DI void gldsi(v4u rs, u32 voff, u32 soff, u32 ldsaddr) {
  u32 t;
  asm volatile("s_add_u32 m0, %4, %6\n\ts_add_u32 %0, %3, %5\n\tbuffer_load_dwordx4 %1, %2, %0 offen lds"
               : "=&s"(t) : "v"(voff), "s"(rs), "s"(soff), "s"(ldsaddr), "i"(SO), "i"(LO) : "memory", "scc");
}
template <int SO, int LO> DI void gldsic(v4u rs, u32 voff, u32 soff, u32 ldsaddr) {
  u32 t;
  asm volatile("s_add_u32 m0, %4, %6\n\ts_add_u32 %0, %3, %5\n\tbuffer_load_dwordx4 %1, %2, %0 offen sc1 lds"
               : "=&s"(t) : "v"(voff), "s"(rs), "s"(soff), "s"(ldsaddr), "i"(SO), "i"(LO) : "memory", "scc");
}
DI v2u ld64(v4u rs, u32 voff, u32 soff) {
  v2u r;
  asm volatile("buffer_load_dwordx2 %0, %1, %2, %3 offen" : "=v"(r) : "v"(voff), "s"(rs), "s"(soff) : "memory");
  return r;
}
DI v4u ld128(v4u rs, u32 voff, u32 soff) {
  v4u r;
  asm volatile("buffer_load_dwordx4 %0, %1, %2, %3 offen" : "=v"(r) : "v"(voff), "s"(rs), "s"(soff) : "memory");
  return r;
}
DI v4u ld128c(v4u rs, u32 voff, u32 soff) {
  v4u r;
  asm volatile("buffer_load_dwordx4 %0, %1, %2, %3 offen sc1" : "=v"(r) : "v"(voff), "s"(rs), "s"(soff) : "memory");
  return r;
}
DI v2u ld64c(v4u rs, u32 voff, u32 soff) {
  v2u r;
  asm volatile("buffer_load_dwordx2 %0, %1, %2, %3 offen sc1" : "=v"(r) : "v"(voff), "s"(rs), "s"(soff) : "memory");
  return r;
}
DI void st128c(v4u v, v4u rs, u32 voff, u32 soff) {
  asm volatile("buffer_store_dwordx4 %0, %1, %2, %3 offen sc1\n\ts_nop 1" ::"v"(v), "v"(voff), "s"(rs), "s"(soff) : "memory");
}
DI void st64c(v2u v, v4u rs, u32 voff, u32 soff) {
  asm volatile("buffer_store_dwordx2 %0, %1, %2, %3 offen sc1" ::"v"(v), "v"(voff), "s"(rs), "s"(soff) : "memory");
}
#define WAITV(n) asm volatile("s_waitcnt vmcnt(" #n ")" ::: "memory")
#define BAR() asm volatile("s_waitcnt lgkmcnt(0)\n\ts_barrier" ::: "memory")
#define PIN(x) asm volatile("" : "+v"(x))
#define SB() __builtin_amdgcn_sched_barrier(0)
#define RBAR() asm volatile("s_barrier" ::: "memory")
template <int N> DI void waitv() { asm volatile("s_waitcnt vmcnt(%0)" ::"i"(N) : "memory"); }

DI v4u lds128(u32 a) { return *(const LAS v4u*)(size_t)a; }
DI v2u lds64(u32 a) { return *(const LAS v2u*)(size_t)a; }
DI void sts128(u32 a, v4u v) { *(LAS v4u*)(size_t)a = v; }
DI void sts64(u32 a, v2u v) { *(LAS v2u*)(size_t)a = v; }
DI v2u trd(u32 a) { return __builtin_bit_cast(v2u, __builtin_amdgcn_ds_read_tr16_b64_v4i16((LAS s16x4*)(size_t)a)); }
DI v4u cat(v2u a, v2u b) { return v4u{a.x, a.y, b.x, b.y}; }
DI f32x16 mfma32(v4u a, v4u b, f32x16 c) {
  return __builtin_amdgcn_mfma_f32_32x32x16_bf16(__builtin_bit_cast(bf16x8, a), __builtin_bit_cast(bf16x8, b), c, 0, 0, 0);
}
DI f32x4 mfma16(v4u a, v4u b, f32x4 c) {
  return __builtin_amdgcn_mfma_f32_16x16x32_bf16(__builtin_bit_cast(bf16x8, a), __builtin_bit_cast(bf16x8, b), c, 0, 0, 0);
}
DI float sum32(float v) {
  u32 a = asu(v), c = a;
  auto r = __builtin_amdgcn_permlane32_swap(a, c, false, false);
  return asf(r[0]) + asf(r[1]);
}
DI float sum16(float v) {
  u32 a = asu(v), c = a;
  auto r = __builtin_amdgcn_permlane16_swap(a, c, false, false);
  return asf(r[0]) + asf(r[1]);
}
DI float sum8(float v) {
  return v + asf(__builtin_amdgcn_update_dpp(0, __builtin_bit_cast(int, v), 0x128, 0xf, 0xf, false));
}
DI float sum4(float v) {
  return v + asf(__builtin_amdgcn_update_dpp(0, __builtin_bit_cast(int, v), 0x124, 0xf, 0xf, false));
}
DI void ln_stats(float s, float q, float& mean, float& sd) {
  const float m = s * (1.0f / D_);
  float var = q * (1.0f / D_) - m * m;
  var = fmaxf(var, 0.0f);
  mean = rb(m);
  sd = rb(__builtin_amdgcn_sqrtf(rb(rb(var) + 1e-5f)));
}
template <int I> DI void poll_kick(gu32* cnt) {
  unsigned long long ex;
  asm volatile("v_mov_b32 v255, 0\n\ts_mov_b64 %0, exec\n\ts_mov_b64 exec, 1\n\tglobal_atomic_add v%c2, v255, v255, %1 sc0\n\ts_mov_b64 exec, %0"
               : "=&s"(ex) : "s"(cnt), "i"(252 + I) : "memory", "v252", "v253", "v254", "v255");
}
template <unsigned CAP = SPIN_CAP> DI void poll_wait(gu32* cnt, u32 target) {
  unsigned long long ex;
  u32 v, n;
  asm volatile(
      "s_mov_b64 %0, exec\n\ts_mov_b64 exec, 1\n\ts_mov_b32 %2, %5\n"
      "1:\n\t"
      "s_waitcnt vmcnt(2)\n\tv_readfirstlane_b32 %1, v252\n\ts_nop 1\n\ts_cmp_eq_u32 %1, %4\n\ts_cbranch_scc1 2f\n\t"
      "global_atomic_add v252, v255, v255, %3 sc0\n\t"
      "s_waitcnt vmcnt(2)\n\tv_readfirstlane_b32 %1, v253\n\ts_nop 1\n\ts_cmp_eq_u32 %1, %4\n\ts_cbranch_scc1 2f\n\t"
      "global_atomic_add v253, v255, v255, %3 sc0\n\t"
      "s_waitcnt vmcnt(2)\n\tv_readfirstlane_b32 %1, v254\n\ts_nop 1\n\ts_cmp_eq_u32 %1, %4\n\ts_cbranch_scc1 2f\n\t"
      "global_atomic_add v254, v255, v255, %3 sc0\n\t"
      "s_sub_u32 %2, %2, 1\n\ts_cmp_lg_u32 %2, 0\n\ts_cbranch_scc1 1b\n"
      "2:\n\t"
      "s_mov_b64 exec, %0"
      : "=&s"(ex), "=&s"(v), "=&s"(n)
      : "s"(cnt), "s"(target), "i"(CAP)
      : "memory", "scc", "v252", "v253", "v254", "v255");
}
template <unsigned CAP = SPIN_CAP> DI void poll_cnt(gu32* cnt, u32 target) {
  poll_kick<0>(cnt); __builtin_amdgcn_s_sleep(2);
  poll_kick<1>(cnt); __builtin_amdgcn_s_sleep(2);
  poll_kick<2>(cnt);
  poll_wait<CAP>(cnt, target);
}
DI float max16(const f32x16& p) {
  const float a = fmaxf(fmaxf(p[0], p[1]), p[2]), b = fmaxf(fmaxf(p[3], p[4]), p[5]), c = fmaxf(fmaxf(p[6], p[7]), p[8]);
  const float d = fmaxf(fmaxf(p[9], p[10]), p[11]), e = fmaxf(fmaxf(p[12], p[13]), p[14]);
  return fmaxf(fmaxf(fmaxf(a, b), c), fmaxf(fmaxf(d, e), p[15]));
}
DI float min16(const f32x16& p) {
  const float a = fminf(fminf(p[0], p[1]), p[2]), b = fminf(fminf(p[3], p[4]), p[5]), c = fminf(fminf(p[6], p[7]), p[8]);
  const float d = fminf(fminf(p[9], p[10]), p[11]), e = fminf(fminf(p[12], p[13]), p[14]);
  return fminf(fminf(fminf(a, b), c), fminf(fminf(d, e), p[15]));
}
DI void expnorm(const f32x16& p, float l, float a, u32* ph) {
  f32x2 e[8];
#pragma unroll
  for (int i = 0; i < 8; ++i) {
    const f32x2 t = __builtin_elementwise_fma(f32x2{p[2 * i], p[2 * i + 1]}, f32x2{l, l}, f32x2{a, a});
    e[i] = f32x2{__builtin_amdgcn_exp2f(t[0]), __builtin_amdgcn_exp2f(t[1])};
  }
  const f32x2 s = ((e[0] + e[1]) + (e[2] + e[3])) + ((e[4] + e[5]) + (e[6] + e[7]));
  const float r = __builtin_amdgcn_rcpf(s[0] + s[1]);
#pragma unroll
  for (int i = 0; i < 8; ++i) { const f32x2 t = e[i] * f32x2{r, r}; ph[i] = pk2(t[0], t[1]); }
}
constexpr float LOG2E = 1.44269504088896341f;
DI void softmax16(const f32x16& p, u32 (&ph)[8]) { expnorm(p, LOG2E, -max16(p) * LOG2E, ph); }
DI void hedgehog(const f32x16& p, u32 (&ph)[16]) {
  expnorm(p, LOG2E, -max16(p) * LOG2E, ph);
  expnorm(p, -LOG2E, min16(p) * LOG2E, ph + 8);
}
DI v4u pack8(const f32x16& x, int s) {
  return v4u{pk2(x[8 * s], x[8 * s + 1]), pk2(x[8 * s + 2], x[8 * s + 3]), pk2(x[8 * s + 4], x[8 * s + 5]), pk2(x[8 * s + 6], x[8 * s + 7])};
}
DI void ex_put(u32 a, const f32x16& x) {
#pragma unroll
  for (int i = 0; i < 4; ++i) sts128(a + i * 1024, __builtin_bit_cast(v4u, f32x4{x[4 * i], x[4 * i + 1], x[4 * i + 2], x[4 * i + 3]}));
}
DI void ex_add(u32 a, f32x16& x) {
#pragma unroll
  for (int i = 0; i < 4; ++i) {
    const f32x4 t = __builtin_bit_cast(f32x4, lds128(a + i * 1024));
    x[4 * i] += t[0]; x[4 * i + 1] += t[1]; x[4 * i + 2] += t[2]; x[4 * i + 3] += t[3];
  }
}

#define STAMP(i) do {} while (0)

template <int I> struct IC { static constexpr int v = I; };
template <int I, int N, class Fn> DI void sfor(Fn&& f) {
  if constexpr (I < N) { f(IC<I>{}); sfor<I + 1, N>(f); }
}
#define AI __attribute__((always_inline))
#define PRIO(n) __builtin_amdgcn_s_setprio(n)
constexpr int wd_slot(int i) { return (i >= 24 && i < 120 && (i - 24) % 3 == 0) ? (i - 24) / 3 : -1; }
constexpr int ops_before(int i) { return i <= 24 ? 12 + i : (i >= 118 ? 68 : 36 + (i - 24 + 2) / 3); }
constexpr int wd_sg(int i) { return i < 3 ? 16 + i : i - 3; }
template <int N> DI float bcast(float v) { return asf(__builtin_amdgcn_mov_dpp((int)asu(v), 0x150 + N, 0xf, 0xf, true)); }
DI f32x2 unp(u32 p) { return f32x2{bflo(p), bfhi(p)}; }
DI f32x2 fma2(f32x2 a, f32x2 b, f32x2 c) { return __builtin_elementwise_fma(a, b, c); }
DI void mfma16_ip(f32x4& c, v4u a, v4u b) { asm volatile("v_mfma_f32_16x16x32_bf16 %0, %1, %2, %0" : "+v"(c) : "v"(a), "v"(b)); }
DI void mfma16_z(f32x4& c, v4u a, v4u b) { asm volatile("v_mfma_f32_16x16x32_bf16 %0, %1, %2, 0" : "=&v"(c) : "v"(a), "v"(b)); }
DI void pinf(f32x4& v) { PIN(v); }
DI void pin4(v4u& v) { PIN(v); }
DI void fma2_ip(f32x2& x, f32x2 a, f32x2 c) { asm volatile("v_pk_fma_f32 %0, %0, %1, %2" : "+v"(x) : "v"(a), "v"(c)); }
DI void mfma32_ip(f32x16& c, v4u a, v4u b) { asm volatile("v_mfma_f32_32x32x16_bf16 %0, %1, %2, %0" : "+v"(c) : "v"(a), "v"(b)); }
template <int N> DI void fmac_bc(float& h, float c, float x) {
  asm("v_fmac_f32_dpp %0, %1, %2 row_newbcast:%3 row_mask:0xf bank_mask:0xf" : "+v"(h) : "v"(c), "v"(x), "i"(N));
}

__global__ void __launch_bounds__(512, 2) __attribute__((amdgpu_num_vgpr(248)))
fused_block(const u16* __restrict__ x, const u16* __restrict__ wqkv, const u16* __restrict__ wphi, const u16* __restrict__ wo,
            const u16* __restrict__ wg, const u16* __restrict__ wu, const u16* __restrict__ wd, const u16* __restrict__ cvec,
            const u16* __restrict__ wmod, u16* __restrict__ out, char* __restrict__ ws) {
  extern __shared__ __attribute__((aligned(16))) char smem[];
  const u32 LB = (u32)(size_t)(LAS char*)smem;
  STAMP(0);
  u32 cpf;
  {
    unsigned long long pc;
    u32 sl = ((blockIdx.x >> 3) % CODE_SLICES) * 2048u, off;
    asm volatile(
        "s_getpc_b64 %2\n\tv_lshlrev_b32 %1, 7, %3\n\tv_cmp_lt_u32 vcc, 63, %3\n\tv_mov_b32 %0, %4\n\ts_nop 0\n\tv_cndmask_b32 %0, 0, %0, vcc\n\t"
        "v_add_u32 %1, %1, %0\n\tv_cmp_gt_u32 vcc, 0x50, %3\n\ts_nop 1\n\tv_cndmask_b32 %1, 0, %1, vcc\n\tglobal_load_dword %0, %1, %2"
        : "=&v"(cpf), "=&v"(off), "=&s"(pc) : "v"(threadIdx.x), "s"(sl) : "memory", "vcc");
    SB();
  }
  asm volatile("" ::"s"(x), "s"(wqkv), "s"(wphi), "s"(wo), "s"(wg), "s"(wu), "s"(wd), "s"(cvec), "s"(wmod), "s"(out), "s"(ws));
  const int tid = threadIdx.x, lane = tid & 63;
  int w = __builtin_amdgcn_readfirstlane(tid >> 6);
  asm volatile("s_nop 4" : "+s"(w));
  const int b = blockIdx.x, g = b & 7, bi = b >> 3;
  const int m2 = 4 * (g >> 1) + (bi >> 3), j2 = 8 * (g & 1) + (bi & 7);
  const int pnl = 2 * g + (bi >> 4), rh = (bi >> 3) & 1, cb = bi & 7;
  const v4u rs_x = mk_srd(x, S_ * D_ * 2), rs_ws = mk_srd(ws, WS_BYTES);
  gu32* const ctl = (gu32*)ws;
  const int tg = w & 3, kh = w >> 2;
  const int c32 = lane & 31, hh = lane >> 5;
  const int l15 = lane & 15, g4 = lane >> 4, tq = (lane & 15) >> 2, tp = lane & 3;

  const bool prod = b < 96, sprod = b >= 96 && b < 224;
  const int cg = b >> 1, kp = b & 1;
  const u32 xld = LB + w * 16384;
  v4u xr[4];
  u32 tagv;
  if (w == 0 && b < 4) __hip_atomic_store(ctl + (WS_CNT + (b * 64 + lane) * 128) / 4, 0u, RLX_AGENT);
  asm volatile("buffer_load_dword %0, off, %1, 0 sc1" : "=v"(tagv) : "s"(rs_ws) : "memory");
  v2u mc2;
  if (prod && w == 0) {
    const v4u rs_m = mk_srd(wmod, D_ * 6 * D_ * 2), rs_c = mk_srd(cvec, D_ * 2);
    mc2 = ld64(rs_c, lane * 8, kp * 512);
    const u32 vo = (lane >> 3) * 6144 + (((lane & 7) ^ (((lane >> 4) & 1) << 1)) << 4);
    const u32 so = 256 * kp * 6144 + cg * 128;
    sfor<0, 32>([&](auto ic) AI { gldsi<ic.v * 8 * 6144, (ic.v < 24 ? L_T + ic.v * 1024 : (ic.v - 24) * 1024)>(rs_m, (ic.v & 1) ? vo ^ 64 : vo, so, LB); });
  }
  if (kh == 0 && sprod) {
#pragma unroll
    for (int r = 0; r < 4; ++r) xr[r] = ld128(rs_x, lane * 16, (16 * (b - 96) + 4 * w + r) * 1024);
  }
  if (w == 5) glds(mk_srd(wphi, 32 * 256 * 2), lane * 16, j2 * 1024, LB + L_WPHI);
  if (prod) {
    if (w == 0) {
      asm volatile("s_waitcnt vmcnt(32)" : "+v"(mc2), "+v"(tagv)::"memory");
      float sv[4] = {bflo(mc2.x), bfhi(mc2.x), bflo(mc2.y), bfhi(mc2.y)};
#pragma unroll
      for (int e = 0; e < 4; ++e) sv[e] = sv[e] * __builtin_amdgcn_rcpf(1.0f + __expf(-sv[e]));
      sts64(LB + 8192 + lane * 8, v2u{pk2(sv[0], sv[1]), pk2(sv[2], sv[3])});
    }
    RBAR();
  }
  const u32 xvo0 = (lane >> 3) * 1024 + (((lane & 7) ^ (lane >> 4)) << 4), xvo1 = xvo0 ^ 64;
  const int rot = bi & 3;
  const u32 xso = (128 * m2 + 32 * tg) * 1024 + kh * 512 + rot * 128;
#define X_ISSUE(q4)                                                                       \
  do {                                                                                    \
    gldsi<0, (q4) * 4096>(rs_x, xvo0, xso ^ ((q4) * 128), xld);                           \
    gldsi<8192, (q4) * 4096 + 1024>(rs_x, xvo1, xso ^ ((q4) * 128), xld);                 \
    gldsi<16384, (q4) * 4096 + 2048>(rs_x, xvo0, xso ^ ((q4) * 128), xld);                \
    gldsi<24576, (q4) * 4096 + 3072>(rs_x, xvo1, xso ^ ((q4) * 128), xld);                \
  } while (0)
  if (kh == 0 && sprod) {
    asm volatile("s_waitcnt vmcnt(0)" : "+v"(xr[0]), "+v"(xr[1]), "+v"(xr[2]), "+v"(xr[3]), "+v"(tagv)::"memory");
    float s[4], q[4], t[2];
#pragma unroll
    for (int r = 0; r < 4; ++r) {
      s[r] = 0.f; q[r] = 0.f;
#pragma unroll
      for (int e = 0; e < 4; ++e) {
        const float lo = bflo(xr[r][e]), hi = bfhi(xr[r][e]);
        s[r] += lo + hi; q[r] = __builtin_fmaf(lo, lo, __builtin_fmaf(hi, hi, q[r]));
      }
    }
#pragma unroll
    for (int r = 0; r < 2; ++r) {
      auto rs = __builtin_amdgcn_permlane32_swap(asu(s[r]), asu(s[r + 2]), false, false);
      auto rq = __builtin_amdgcn_permlane32_swap(asu(q[r]), asu(q[r + 2]), false, false);
      s[r] = asf(rs[0]) + asf(rs[1]); q[r] = asf(rq[0]) + asf(rq[1]);
    }
    {
      auto rs = __builtin_amdgcn_permlane16_swap(asu(s[0]), asu(s[1]), false, false);
      auto rq = __builtin_amdgcn_permlane16_swap(asu(q[0]), asu(q[1]), false, false);
      t[0] = asf(rs[0]) + asf(rs[1]); t[1] = asf(rq[0]) + asf(rq[1]);
    }
#pragma unroll
    for (int r = 0; r < 2; ++r) {
      t[r] = sum4(sum8(t[r]));
      t[r] += asf(__builtin_amdgcn_update_dpp(0, (int)asu(t[r]), 0x122, 0xf, 0xf, false));
      t[r] += asf(__builtin_amdgcn_update_dpp(0, (int)asu(t[r]), 0x121, 0xf, 0xf, false));
    }
    float mean, sd;
    ln_stats(t[0], t[1], mean, sd);
    if (l15 == 0) st64c(v2u{pk2(mean, sd), tagv + 1u}, rs_ws, g4 * 8, WS_ROWST + (16 * (b - 96) + 4 * w) * 8);
  }
  if (prod && w == 0) {
    PRIO(3);
    v4u sb[8];
#pragma unroll
    for (int ks = 0; ks < 8; ++ks) sb[ks] = lds128(LB + 8192 + ks * 64 + g4 * 16);
    u32 ma[4], mb[4];
#pragma unroll
    for (int c = 0; c < 4; ++c) {
      mb[c] = LB + (8 * g4 + tq) * 128 + tp * 8 + ((c ^ (((tq >> 1) & 1) | ((g4 & 1) << 1))) << 5); ma[c] = mb[c] + L_T;
      PIN(ma[c]); PIN(mb[c]);
    }
    f32x4 acc[4];
#pragma unroll
    for (int c = 0; c < 4; ++c) acc[c] = f32x4{0.f, 0.f, 0.f, 0.f};
    v4u fm[2][2][4];
#define M_READ(h2) _Pragma("unroll") for (int k2 = 0; k2 < 2; ++k2) _Pragma("unroll") for (int c = 0; c < 4; ++c) {               \
      const u32 ad = (h2) < 3 ? ma[c] + (2 * (h2) + k2) * 4096 : mb[c] + k2 * 4096;                                                \
      fm[(h2) & 1][k2][c] = cat(trd(ad), trd(ad + 512)); }
    WAITV(24);
    M_READ(0);
    sfor<0, 4>([&](auto ic) AI {
      constexpr int h2 = ic.v;
      if constexpr (h2 < 3) { waitv<16 - 8 * h2>(); M_READ(h2 + 1); }
      SB();
#pragma unroll
      for (int k2 = 0; k2 < 2; ++k2)
#pragma unroll
        for (int c = 0; c < 4; ++c) acc[c] = mfma16(sb[2 * h2 + k2], fm[h2 & 1][k2][c], acc[c]);
      SB();
    });
    const float lo = (g4 & 1) ? acc[1][0] : acc[0][0], hi = (g4 & 1) ? acc[3][0] : acc[2][0];
    st64c(v2u{asu(hh ? hi : lo), tagv + 1u}, rs_ws, lane * 8, WS_GRAN + kp * 24576 + cg * 512);
    PRIO(0);
  }
  STAMP(1);
  X_ISSUE(0); X_ISSUE(1); X_ISSUE(2);
  const v4u rs_w = mk_srd(wqkv, D_ * 3 * D_ * 2);
  const u32 wvo = ((lane >> 2) * 1536 + 8 * (lane & 3)) * 2;
  const u32 wso = ((256 * (tg & 1) + 16 * (tg >> 1)) * 1536 + 32 * j2) * 2;
#define W_ISSUE(part, n) gldsi<((n) & 1) * 32 * 3072 + (part) * 1024, (n) * 1024 + ((part) == 0 ? 8192 : 0)>(rs_w, wvo, wso + (((n) >> 1) ^ rot) * (64 * 3072), xld)
  const bool p4 = j2 < 15;
  const int un0 = w < 4 ? 2 * w : 4 + w;
  f32x2 xf[16][4];
  u32 tagl;
  {
    u32 xa[4];
#pragma unroll
    for (int sp = 0; sp < 4; ++sp) { xa[sp] = LB + w * 16384 + c32 * 128 + (((2 * sp + hh) ^ ((c32 >> 1) & 7)) << 4); PIN(xa[sp]); }
    v4u gr[4];
    v2u gs;
#pragma unroll
    for (int q4 = 0; q4 < 4; ++q4) {
      if (q4 == 0) { X_ISSUE(3); WAITV(12); }
      if (q4 == 1) WAITV(8);
      if (q4 == 2) WAITV(4);
      if (q4 == 3) {
        WAITV(0);
        if (kh == 1) {
          PRIO(3);
          gs = ld64c(rs_ws, c32 * 8, WS_ROWST + (128 * m2 + 32 * tg) * 8);
#pragma unroll
          for (int n = 0; n < 4; ++n) gr[n] = ld128c(rs_ws, lane * 16, WS_GRAN + (n & 1) * 24576 + (tg * 2 + (n >> 1)) * 1024);
          PRIO(0);
        }
      }
      v4u t[4];
#pragma unroll
      for (int sp = 0; sp < 4; ++sp) t[sp] = lds128(xa[sp] + q4 * 4096);
#pragma unroll
      for (int sp = 0; sp < 4; ++sp) {
#pragma unroll
        for (int e = 0; e < 4; ++e) xf[4 * q4 + sp][e] = f32x2{bflo(t[sp][e]), bfhi(t[sp][e])};
        f32x2(&f)[4] = xf[4 * q4 + sp];
        asm volatile("" : "+v"(f[0]), "+v"(f[1]), "+v"(f[2]), "+v"(f[3]));
      }
      SB();
    }
    if (kh == 0) {
      if (w == 1) asm volatile("buffer_load_dword v251, %0, %1, %2 offen" ::"v"(lane * 128), "s"(mk_srd(wd, F_ * D_ * 2)), "s"(b * 5504) : "memory", "v251");
      if (w == 2) asm volatile("buffer_load_dword v251, %0, %1, %2 offen" ::"v"(l15 * 128), "s"(mk_srd(wo, D_ * D_ * 2)), "s"(b * 2048) : "memory", "v251");
    }
    PIN(tagv);
    PIN(cpf);
    tagl = tagv + 1u;
    STAMP(22);
    if (kh == 0) sfor<0, 8>([&](auto ic) AI { W_ISSUE(1, ic.v); });
    if (kh == 1) {
      asm volatile("s_waitcnt vmcnt(0)" : "+v"(gs), "+v"(gr[0]), "+v"(gr[1]), "+v"(gr[2]), "+v"(gr[3])::"memory");
      for (u32 spins = 0;; ++spins) {
        bool ok = gs.y == tagl;
#pragma unroll
        for (int n = 0; n < 4; ++n) ok = ok && gr[n].y == tagl && gr[n].w == tagl;
        if (__all(ok) || spins >= SPIN_CAP) break;
        if (spins) __builtin_amdgcn_s_sleep(4);
        gs = ld64c(rs_ws, c32 * 8, WS_ROWST + (128 * m2 + 32 * tg) * 8);
#pragma unroll
        for (int n = 0; n < 4; ++n) gr[n] = ld128c(rs_ws, lane * 16, WS_GRAN + (n & 1) * 24576 + (tg * 2 + (n >> 1)) * 1024);
        asm volatile("s_waitcnt vmcnt(0)" : "+v"(gs), "+v"(gr[0]), "+v"(gr[1]), "+v"(gr[2]), "+v"(gr[3])::"memory");
      }
      *(LAS u32*)(size_t)(LB + L_STATX + (tg * 32 + c32) * 4) = gs.x;
      const float one = tg >= 2 ? 1.0f : 0.0f;
#pragma unroll
      for (int n = 0; n < 2; ++n) {
        const u32 p = pk2(asf(gr[2 * n].x) + asf(gr[2 * n + 1].x), asf(gr[2 * n].z) + asf(gr[2 * n + 1].z));
        const u32 q = pk2(bflo(p) + one, bfhi(p) + one);
        sts64(LB + L_CS + tg * 1024 + (((2 * n + hh) ^ rot) << 8) + c32 * 8, v2u{q << 16, q & 0xffff0000u});
      }
    }
    BAR();
    STAMP(2);
    const u32 st = *(const LAS u32*)(size_t)(LB + L_STATX + (tg * 32 + c32) * 4);
    const float r = __builtin_amdgcn_rcpf(bfhi(st)), mr = -bflo(st) * r;
    const f32x2 r2 = {r, r}, mr2 = {mr, mr};
    if (kh == 1) sfor<0, 4>([&](auto ic) AI { W_ISSUE(2, ic.v); });
#pragma unroll
    for (int s = 0; s < 16; ++s)
#pragma unroll
      for (int e = 0; e < 4; ++e) fma2_ip(xf[s][e], r2, mr2);
  }
  const u32 tag = __builtin_amdgcn_readfirstlane(tagl);
  f32x16 zero;
#pragma unroll
  for (int i = 0; i < 16; ++i) zero[i] = 0.f;
  v4u hf[16];
  u32 wk = LB + kh * 16384 + (8 * hh + tq) * 64 + (g4 & 1) * 32 + tp * 8, wv = wk + L_B;
#define W_FRAG(base, s) cat(trd((base) + ((s) & 1) * 32768 + ((s) >> 1) * 1024), trd((base) + ((s) & 1) * 32768 + ((s) >> 1) * 1024 + 256))
  f32x16 ak = zero;
  {
    u32 ca = LB + L_CS + (256 * kh + 8 * hh) * 4, cda = ca + 2048 + (l15 >> 3) * 64 + (l15 & 7) * 4;
    PIN(wk); PIN(wv); PIN(ca); PIN(cda);
    float c1[8];
#pragma unroll
    for (int r = 0; r < 8; ++r) c1[r] = *(const LAS float*)(size_t)(cda + r * 128);
    f32x4 hs[2][2];
    v4u fw[2];
#define H_READ(s) do { hs[(s) & 1][0] = __builtin_bit_cast(f32x4, lds128(ca + (s) * 64)); hs[(s) & 1][1] = __builtin_bit_cast(f32x4, lds128(ca + (s) * 64 + 16)); } while (0)
#define H_FMA(s, hf_) do { float t0_ = hs[(s) & 1][hf_][0], t1_ = hs[(s) & 1][hf_][1], t2_ = hs[(s) & 1][hf_][2], t3_ = hs[(s) & 1][hf_][3]; \
    fmac_bc<8 * ((s) & 1) + 4 * (hf_) + 0>(t0_, c1[(s) >> 1], xf[s][2 * (hf_) + 0][0]); fmac_bc<8 * ((s) & 1) + 4 * (hf_) + 1>(t1_, c1[(s) >> 1], xf[s][2 * (hf_) + 0][1]); \
    fmac_bc<8 * ((s) & 1) + 4 * (hf_) + 2>(t2_, c1[(s) >> 1], xf[s][2 * (hf_) + 1][0]); fmac_bc<8 * ((s) & 1) + 4 * (hf_) + 3>(t3_, c1[(s) >> 1], xf[s][2 * (hf_) + 1][1]); \
    u32 p0_ = pk2(t0_, t1_), p1_ = pk2(t2_, t3_); PIN(p0_); PIN(p1_); \
    hf[s][2 * (hf_)] = p0_; hf[s][2 * (hf_) + 1] = p1_; } while (0)
    H_READ(0); H_READ(1);
    SB();
    H_FMA(0, 0); H_FMA(0, 1);
    if (kh == 0) WAITV(4);
    else sfor<4, 8>([&](auto ic) AI { W_ISSUE(2, ic.v); });
    RBAR();
    STAMP(3);
    fw[0] = W_FRAG(wk, 0);
    SB();
    sfor<0, 16>([&](auto ic) AI {
      constexpr int s = ic.v;
      if constexpr (s == 7) {
        if (kh == 0) {
          WAITV(0);
          if (p4 && tg != 0) {
            const v4u rs_g = mk_srd(wg, D_ * F_ * 2), rs_u = mk_srd(wu, D_ * F_ * 2);
            const u32 P = (tg - 1) * 64 + lane, gvo = (P / 12) * (F_ * 2) + (((P % 12) ^ (((P / 12) >> 2) & 1)) << 4);
            const u32 gso = 32 * m2 * (F_ * 2) + j2 * 192, gld = LB + L_T + tg * 1024 - 1024;
            gldsi<0, 0>(rs_g, gvo, gso, gld); gldsi<16 * F_ * 2, 3072>(rs_g, gvo, gso, gld);
            gldsi<0, 6144>(rs_u, gvo ^ 32, gso, gld); gldsi<16 * F_ * 2, 9216>(rs_u, gvo ^ 32, gso, gld);
          }
        }
        RBAR();
      }
      if constexpr (s < 15) fw[(s + 1) & 1] = W_FRAG(wk, s + 1);
      if constexpr (s < 14) H_READ((s + 2) & 15);
      SB();
      if constexpr (s < 15) H_FMA((s + 1) & 15, 0);
      SB();
      ak = mfma32(fw[s & 1], hf[s], ak);
      SB();
      if constexpr (s < 15) H_FMA((s + 1) & 15, 1);
      SB();
    });
  }
  STAMP(4);
  const v4u rs_wo = mk_srd(wo, D_ * D_ * 2);
  u32 dvo;
  {
    const u32 r8 = lane >> 3, pc = lane & 7;
    const u32 f = ((r8 >> 1) & 1) | ((w & 1) << 1);
    dvo = r8 * 1024 + ((((pc >> 1) ^ f) * 2 + (pc & 1)) << 4);
  }
#define SIGNAL(wv, cnt) do { if (tid >= (wv) * 64 && tid < (wv) * 64 + 4) __hip_atomic_fetch_add(ctl + (WS_CNT + (cnt) * CNT_STRIDE + lane * 128) / 4, 1u, RLX_AGENT); } while (0)
#define POLL(cnt, rep, target) do { PRIO(3); poll_cnt(ctl + (WS_CNT + (cnt) * CNT_STRIDE + (rep) * 128) / 4, target); PRIO(0); } while (0)
  v4u wf[2][16];
  const u32 wfo = WS_GT + (j2 * 12 + un0) * 16384;
#define WF_GET(n, lo, hi) _Pragma("unroll") for (int k_ = (lo); k_ < (hi); ++k_) wf[n][k_] = ld128c(rs_ws, lane * 16, wfo + (n) * 16384 + k_ * 1024)
#define WO_ISSUE(n) gldsi<(n) * 32768, ((n) >> 1) * 16384 + ((n) & 1) * 4096>(rs_wo, dvo, tg * 8192 + cb * 128, LB + tg * 1024)
  auto proj = [&](u32 base, auto hook) AI {
    f32x16 acc = zero;
    v4u fq[4];
#pragma unroll
    for (int s = 0; s < 3; ++s) fq[s] = W_FRAG(base, s);
    sfor<0, 16>([&](auto ic) AI {
      constexpr int s = ic.v;
      if constexpr (s + 3 < 16) fq[(s + 3) & 3] = W_FRAG(base, s + 3);
      hook(ic);
      SB();
      acc = mfma32(fq[s & 3], hf[s], acc);
      SB();
    });
    return acc;
  };
  v4u pf0, pf1;
  {
    const u32 sc = LB + tg * 16384 + 8192;
    const u32 pa = LB + L_WPHI + (tp & 1) * 512 + (4 * hh + tq) * 32 + (8 * (g4 & 1) + 4 * (tp >> 1)) * 2;
    const v4u pf = cat(trd(pa), trd(pa + 256));
    u32 m1 = 0u - ((lane >> 2) & 1);
    PIN(m1);
    pf1 = pf & m1; pf0 = pf ^ pf1;
    const u32 sg = kh ? 0x80008000u : 0u;
    const v4u pg0 = pf0 ^ v4u{sg, sg, sg, sg}, pg1 = pf1 ^ v4u{sg, sg, sg, sg};
    const v4u k0 = pack8(ak, 0), k1 = pack8(ak, 1);
    sts128(sc + (kh ^ 1) * 2048 + lane * 16, k0);
    sts128(sc + (kh ^ 1) * 2048 + 1024 + lane * 16, k1);
    f32x16 p = mfma32(pg0, k0, zero);
    p = mfma32(pg1, k1, p);
    if (kh == 1) WAITV(0);
    BAR();
    STAMP(23);
    {
      const v4u o0 = lds128(sc + kh * 2048 + lane * 16), o1 = lds128(sc + kh * 2048 + 1024 + lane * 16);
      p = mfma32(pg0, o0, p);
      p = mfma32(pg1, o1, p);
      SB();
    }
    auto feat = [&]() AI {
      u32 ph[8];
      softmax16(p, ph);
      const u32 row = sc + kh * 2048 + c32 * 64;
      sts128(row + (((2 * hh) ^ ((c32 >> 1) & 3)) << 4), v4u{ph[0], ph[1], ph[2], ph[3]});
      sts128(row + (((2 * hh + 1) ^ ((c32 >> 1) & 3)) << 4), v4u{ph[4], ph[5], ph[6], ph[7]});
    };
    if (kh == 0) feat();
    else sfor<0, 8>([&](auto ic) AI { W_ISSUE(0, ic.v); });
    {
      const f32x16 av = proj(wv, [](auto) {});
      const u32 row = sc + 4096 + kh * 2048 + c32 * 64 + 8 * hh;
#pragma unroll
      for (int i = 0; i < 4; ++i)
        sts64(row + ((i ^ ((c32 >> 1) & 3)) << 4), v2u{pk2(av[4 * i], av[4 * i + 1]), pk2(av[4 * i + 2], av[4 * i + 3])});
    }
    if (kh == 1) feat();
    u32 aa[2], ba[2];
#pragma unroll
    for (int t = 0; t < 2; ++t) {
      const u32 tok = 8 * g4 + 4 * t + tq;
      const u32 o = LB + 8192 + tok * 64 + (((2 * (tg >> 1) + (tp >> 1)) ^ ((tok >> 1) & 3)) << 4) + 8 * (tp & 1);
      aa[t] = o + (tg & 1) * 2048; ba[t] = o + 4096;
      PIN(aa[t]); PIN(ba[t]);
    }
    if (kh == 0) WAITV(0);
    BAR();
    STAMP(18);
    if (kh == 0) {
      PRIO(3);
      f32x4 kv0 = {0.f, 0.f, 0.f, 0.f}, kv1 = kv0, zz = kv0;
      const v4u ones = v4u{0x3f803f80u, 0x3f803f80u, 0x3f803f80u, 0x3f803f80u};
      v4u a[4], b0[4], b1[4];
#pragma unroll
      for (int ks = 0; ks < 4; ++ks) {
        a[ks] = cat(trd(aa[0] + ks * 16384), trd(aa[1] + ks * 16384));
        b0[ks] = cat(trd(ba[0] + ks * 16384), trd(ba[1] + ks * 16384));
        b1[ks] = cat(trd(ba[0] + ks * 16384 + 2048), trd(ba[1] + ks * 16384 + 2048));
      }
      SB();
#pragma unroll
      for (int ks = 0; ks < 4; ++ks) {
        kv0 = mfma16(a[ks], b0[ks], kv0); kv1 = mfma16(a[ks], b1[ks], kv1); zz = mfma16(a[ks], ones, zz);
      }
      kv0 += kv1;
      st64c(v2u{pk2(kv0[0], kv0[1]), pk2(kv0[2], kv0[3])}, rs_ws, lane * 8, WS_SLAB + ((j2 * 4 + w) * 16 + m2) * 512);
      if (l15 == 0) st64c(v2u{pk2(zz[0], zz[1]), pk2(zz[2], zz[3])}, rs_ws, g4 * 8, WS_Z + ((j2 * 4 + w) * 16 + m2) * 32);
      sfor<0, 8>([&](auto ic) AI { WO_ISSUE(ic.v); });
      WAITV(8);
      PRIO(0);
    } else {
      const u32 gb = LB + L_T + (4 * g4 + tq) * 192 + 8 * (tp & 1);
      const u32 ga = gb + (((2 * tg + (tp >> 1)) ^ (g4 & 1)) << 4), gc = gb + (tp >> 1) * 6144 + (((8 + tg) ^ (g4 & 1) ^ (tp & 2)) << 4);
      const v4u gt0 = cat(trd(ga), trd(ga + 3072)), gt1 = cat(trd((ga ^ 32) + 6144), trd((ga ^ 32) + 9216)), gt2 = cat(trd(gc), trd(gc + 3072));
      const u32 gto = p4 ? WS_GT + j2 * (12 * 16384) + m2 * 1024 : WS_BYTES;
      st128c(gt0, rs_ws, lane * 16, gto + tg * 32768);
      st128c(gt1, rs_ws, lane * 16, gto + tg * 32768 + 16384);
      st128c(gt2, rs_ws, lane * 16, gto + (8 + tg) * 16384);
      WAITV(0);
    }
    RBAR();
    SIGNAL(1, 0 * 16 + j2);
  }
  STAMP(5);
  const u32 orow = (128 * pnl + 64 * rh + 16 * tg + l15) * 1024 + (64 * cb + 32 * kh + 16 * (g4 & 1) + 8 * (g4 >> 1)) * 2;
  v4u xres;
  u32 phq[16];
  {
    if (kh == 1) sfor<8, 16>([&](auto ic) AI { WO_ISSUE(ic.v); });
    xres = ld128(rs_x, orow, 0);
    gu32* const e2 = ctl + (WS_CNT + (0 * 16 + j2) * CNT_STRIDE + (m2 & 3) * 128) / 4;
    const f32x16 aq = proj(wv + 8192, [&](auto ic) AI {
      if constexpr (ic.v == 7 || ic.v == 11 || ic.v == 15) { if (w == 0) poll_kick<((ic.v - 7) >> 2) & 3>(e2); }
    });
    const v4u q0 = pack8(aq, 0), q1 = pack8(aq, 1);
    const u32 ea = LB + L_QEX + tg * 2048 + lane * 16;
    f32x16 p;
    if (kh == 0) { sts128(ea, q0); sts128(ea + 1024, q1); }
    else {
      p = mfma32(pf0, q0, zero);
      p = mfma32(pf1, q1, p);
      if (lane < 16) *(LAS u32*)(size_t)(LB + L_KVT + 34 * 64 + lane * 4) = 0u;
    }
    if (w == 0) poll_wait(e2, 16u);
    BAR();
    STAMP(6);
    if (kh == 1) {
      const v4u o0 = lds128(ea), o1 = lds128(ea + 1024);
      p = mfma32(pf0, o0, p);
      p = mfma32(pf1, o1, p);
      hedgehog(p, phq);
    }
  }
  if (kh == 0) {
    PRIO(3);
    v4u sl[8];
    v2u zl = ld64c(rs_ws, lane * 8, WS_Z + (j2 * 4 + w) * 512);
#pragma unroll
    for (int m = 0; m < 8; ++m) sl[m] = ld128c(rs_ws, lane * 16, WS_SLAB + ((j2 * 4 + w) * 8 + m) * 1024);
    WF_GET(0, 0, 4); WF_GET(1, 0, 4);
    asm volatile("s_waitcnt vmcnt(16)" : "+v"(zl)::"memory");
    const int hd = w >> 1, fh = w & 1;
    {
      auto r = __builtin_amdgcn_permlane32_swap(zl.x, zl.y, false, false);
      float z0 = bflo(r[0]) + bflo(r[1]), z1 = bfhi(r[0]) + bfhi(r[1]);
      z0 = sum16(sum8(sum4(z0))); z1 = sum16(sum8(sum4(z1)));
      *(LAS u32*)(size_t)(LB + L_KVT + (hd * 17 + 16) * 64 + (16 * fh + 4 * tp + 2 * hh) * 2) = pk2(z0, z1);
    }
    float sa[8] = {0.f, 0.f, 0.f, 0.f, 0.f, 0.f, 0.f, 0.f};
    sfor<0, 8>([&](auto ic) AI {
      constexpr int m = ic.v;
      asm volatile("s_waitcnt vmcnt(%1)" : "+v"(sl[m]) : "i"(15 - m) : "memory");
#pragma unroll
      for (int e = 0; e < 4; ++e) { sa[2 * e] += bflo(sl[m][e]); sa[2 * e + 1] += bfhi(sl[m][e]); }
    });
#pragma unroll
    for (int e = 0; e < 4; ++e) {
      auto r = __builtin_amdgcn_permlane32_swap(asu(sa[e]), asu(sa[e + 4]), false, false);
      sa[e] = asf(r[0]) + asf(r[1]);
    }
    sts64(LB + L_KVT + (hd * 17 + 2 * (lane & 7) + hh) * 64 + (16 * fh + 4 * (c32 >> 3)) * 2, v2u{pk2(sa[0], sa[1]), pk2(sa[2], sa[3])});
    PRIO(0);
  }
  u32 ka, za;
  {
    const bool own = ((lane >> 2) & 1) == hh;
    const u32 d = (c32 & 3) + 4 * (c32 >> 3);
    ka = LB + L_KVT + (own ? hh * 17 + d : 34) * 64; za = LB + L_KVT + (own ? hh * 17 + 16 : 34) * 64;
    PIN(ka); PIN(za);
  }
  BAR();
  STAMP(7);
  if (kh == 1) {
    PRIO(3);
    f32x16 num = zero, den = zero;
    v4u a[4], z[4];
#pragma unroll
    for (int s = 0; s < 4; ++s) z[s] = lds128(za + s * 16);
#pragma unroll
    for (int s = 0; s < 4; ++s) a[s] = lds128(ka + s * 16);
    SB();
#pragma unroll
    for (int s = 0; s < 4; ++s) den = mfma32(z[s], v4u{phq[4 * s], phq[4 * s + 1], phq[4 * s + 2], phq[4 * s + 3]}, den);
    SB();
#pragma unroll
    for (int s = 0; s < 4; ++s) num = mfma32(a[s], v4u{phq[4 * s], phq[4 * s + 1], phq[4 * s + 2], phq[4 * s + 3]}, num);
    const float r = __builtin_amdgcn_rcpf(den[0] + 1e-6f);
    f32x16 o;
#pragma unroll
    for (int i = 0; i < 16; ++i) o[i] = num[i] * r;
    const u32 vo = (c32 >> 4) * 16384 + hh * 512 + l15 * 16;
    const u32 so = WS_O + ((8 * m2 + 2 * tg) * 16 + j2) * 1024;
    st128c(pack8(o, 0), rs_ws, vo, so);
    st128c(pack8(o, 1), rs_ws, vo + 256, so);
    WF_GET(0, 0, 8);
    WAITV(8);
    PRIO(0);
  }
  RBAR();
  SIGNAL(1, 1 * 16 + m2);
  STAMP(8);
  if (kh == 1) {
    v4u gr[5];
    for (u32 spins = 0;; ++spins) {
#pragma unroll
      for (int n = 0; n < 4; ++n) gr[n] = ld128c(rs_ws, lane * 16, WS_GRAN + (n & 1) * 24576 + (12 + tg * 2 + (n >> 1)) * 1024);
      gr[4] = ld128c(rs_ws, c32 * 16 + hh * 24576, WS_GRAN + ((tg & 1) ? 40 + cb : 16 + cb) * 512);
      asm volatile("s_waitcnt vmcnt(0)" : "+v"(gr[0]), "+v"(gr[1]), "+v"(gr[2]), "+v"(gr[3]), "+v"(gr[4])::"memory");
      bool ok = true;
#pragma unroll
      for (int n = 0; n < 5; ++n) ok = ok && gr[n].y == tag && gr[n].w == tag;
      if (__all(ok) || spins >= SPIN_CAP) break;
    }
    const float one = tg >= 2 ? 1.0f : 0.0f;
    const u32 vperm = g4 * 64 + ((lane >> 1) & 3) * 16 + ((lane >> 3) & 1) * 8 + (lane & 1) * 4;
#pragma unroll
    for (int n = 0; n < 2; ++n) {
      const u32 p = pk2(asf(gr[2 * n].x) + asf(gr[2 * n + 1].x), asf(gr[2 * n].z) + asf(gr[2 * n + 1].z));
      *(LAS u32*)(size_t)(LB + L_VEC + 1024 + (tg * 2 + n) * 256 + vperm) = pk2(bflo(p) + one, bfhi(p) + one);
    }
    *(LAS u32*)(size_t)(LB + L_VEC + (tg & 1) * 128 + (vperm & 127)) = pk2(sum32(asf(gr[4].x)), sum32(asf(gr[4].z)));
  } else {
    WF_GET(0, 4, 8);
    WAITV(4);
    if (w == 0) POLL(1 * 16 + pnl, bi & 3, 16u);
  }
  u32 wra[4], wo_a[4];
#pragma unroll
  for (int c = 0; c < 4; ++c) {
    wra[c] = LB + (8 * g4 + tq) * 128 + tp * 8 + ((c ^ (2 * kh) ^ (((tq >> 1) & 1) | ((g4 & 1) << 1))) << 5);
    wo_a[c] = wra[c] + kh * 65536;
    PIN(wo_a[c]);
  }
  BAR();
  STAMP(9);
  f32x4 cs0 = {0.f, 0.f, 0.f, 0.f}, cs1 = {0.f, 0.f, 0.f, 0.f};
  u32 cva = LB + L_VEC + 2048 - (l15 >> 3) * 1024 + g4 * 16;
  u32 x1p[4];
  {
    v4u of[8];
#pragma unroll
    for (int i = 0; i < 8; ++i) of[i] = ld128c(rs_ws, lane * 16, WS_O + ((8 * pnl + 4 * rh + tg) * 16 + 8 * kh + i) * 1024);
#define WO_FRAG(i, c) cat(trd(wo_a[c] + ((i) >> 1) * 16384 + ((i) & 1) * 4096), trd(wo_a[c] + ((i) >> 1) * 16384 + ((i) & 1) * 4096 + 512))
    const u32 va = LB + L_VEC + kh * 64 + g4 * 16;
    const v4u ga4 = lds128(va), cb4 = lds128(va + 2048 + cb * 128);
    v4u fa[4][4];
#pragma unroll
    for (int i = 0; i < 4; ++i)
#pragma unroll
      for (int c = 0; c < 4; ++c) fa[i][c] = WO_FRAG(i, c);
    PIN(xres);
    auto r0 = __builtin_amdgcn_permlane16_swap(xres.x, xres.z, false, false);
    auto r1 = __builtin_amdgcn_permlane16_swap(xres.y, xres.w, false, false);
    f32x2 xv[4] = {unp(r0[0]), unp(r1[0]), unp(r0[1]), unp(r1[1])};
    f32x2 gv[4] = {unp(ga4.x), unp(ga4.y), unp(ga4.z), unp(ga4.w)};
    f32x2 cv[4] = {unp(cb4.x), unp(cb4.y), unp(cb4.z), unp(cb4.w)};
#pragma unroll
    for (int e = 0; e < 4; ++e) { PIN(xv[e]); PIN(gv[e]); PIN(cv[e]); }
    u32 ea = LB + L_EX3 + tg * 4096 + lane * 16 + kh * 2048;
    PIN(ea);
    f32x4 acc[4];
    sfor<0, 8>([&](auto ic) AI {
      constexpr int i = ic.v;
      waitv<7 - i>();
      pin4(of[i]);
      SB();
#pragma unroll
      for (int c = 3; c >= 0; --c) {
        if constexpr (i == 0) mfma16_z(acc[c], fa[0][c], of[0]);
        else mfma16_ip(acc[c], fa[i & 3][c], of[i]);
        if constexpr (i + 4 < 8) fa[i & 3][c] = WO_FRAG(i + 4, c);
        SB();
      }
    });
    STAMP(10);
    asm volatile("s_nop 4" ::: "memory");
    SB();
    sts128(ea + 1024, __builtin_bit_cast(v4u, acc[3]));
    sts128(ea, __builtin_bit_cast(v4u, acc[2]));
    WF_GET(0, 8, 12);
    BAR();
    const f32x4 t0 = acc[0] + __builtin_bit_cast(f32x4, lds128(ea ^ 2048));
    const f32x4 t1 = acc[1] + __builtin_bit_cast(f32x4, lds128((ea ^ 2048) + 1024));
    f32x2 sum = {0.f, 0.f}, sq = {0.f, 0.f};
    v4u y;
#pragma unroll
    for (int e = 0; e < 4; ++e) {
      const f32x2 at = unp(e < 2 ? pk2(t0[2 * e], t0[2 * e + 1]) : pk2(t1[2 * e - 4], t1[2 * e - 3]));
      const f32x2 gp = gv[e] * at, xs = xv[e] + unp(pk2(gp[0], gp[1]));
      x1p[e] = pk2(xs[0], xs[1]);
      const f32x2 x1 = unp(x1p[e]), yy = x1 * cv[e];
      y[e] = pk2(yy[0], yy[1]);
      sum += x1; sq = fma2(x1, x1, sq);
    }
    st128c(y, rs_ws, lane * 16, WS_Y1 + ((8 * pnl + 4 * rh + tg) * 16 + 2 * cb + kh) * 1024);
    const float s1 = sum32(sum16(sum[0] + sum[1])), q1 = sum32(sum16(sq[0] + sq[1]));
    st64c(v2u{asu(s1), asu(q1)}, rs_ws, lane < 16 ? lane * 8 : WS_BYTES, WS_STAT + ((8 * pnl + 4 * rh + tg) * 16 + 2 * cb + kh) * 128);
    WF_GET(0, 12, 16);
    WAITV(4);
  }
  BAR();
  SIGNAL(4, 2 * 16 + pnl);
  STAMP(11);
  const v4u rs_wd = mk_srd(wd, F_ * D_ * 2);
  const u32 dso = tg * 8192 + cb * 128, dld = LB + tg * 1024;
#define WD_ISSUE(i, h) gldsi<(22 * (h) + (i)) * 32768, (2 * wd_sg(i) + (h)) * 4096>(rs_wd, dvo, dso, dld)
#define WD_ISSUE_K(i) gldsi<(i) * 32768, 2 * wd_sg(i) * 4096>(rs_wd, dvo, dso + kh * (22 * 32768), dld + kh * 4096)
  if (p4) {
    auto cs_part = [&](auto lo, auto hi) AI {
      v4u cf[hi.v - lo.v];
#pragma unroll
      for (int ks = lo.v; ks < hi.v; ++ks) { pin4(wf[0][ks]); cf[ks - lo.v] = lds128(cva + ks * 64); }
      SB();
#pragma unroll
      for (int ks = lo.v; ks < hi.v; ++ks) cs0 = mfma16(wf[0][ks], cf[ks - lo.v], cs0);
      pinf(cs0);
    };
    PIN(cva);
    if (kh == 0) { WF_GET(1, 4, 10); }
    gu32* const e4 = ctl + (WS_CNT + (2 * 16 + m2) * CNT_STRIDE + (bi & 3) * 128) / 4;
    if (w == 7) poll_kick<0>(e4);
    cs_part(IC<0>{}, IC<6>{});
    if (w == 7) poll_kick<1>(e4);
    cs_part(IC<6>{}, IC<12>{});
    if (w == 7) { poll_kick<2>(e4); poll_wait(e4, 16u); }
    STAMP(21);
    RBAR();
    STAMP(12);
    u32 ya0 = LB + lane * 16, ya1 = ya0 + 65536;
    PIN(ya0); PIN(ya1);
#define Y_READ(i) lds128(((i) < 64 ? ya0 : ya1) + ((i) & 63) * 1024)
    constexpr int PD = 6, RING = 7;
    const u32 hvo = l15 * 16, hso = WS_HH + 8 * m2 * (43 * 1024);
    if (w >= 4) {
      f32x4 acc0[8];
      v4u st[4], yr[RING];
      const u32 yso = WS_Y1 + (8 * m2 + 2 * tg) * 16384, yld = LB + tg * 2048;
#define Y_ISSUE(n) gldsic<((n) & 1) * 16384 + ((n) >> 1) * 1024, (((n) >> 1) * 8 + ((n) & 1)) * 1024>(rs_ws, lane * 16, yso, yld)
      PRIO(2);
      sfor<0, 12>([&](auto ic) AI { Y_ISSUE(ic.v); });
      WAITV(12);
      cs_part(IC<12>{}, IC<16>{});
      WAITV(8);
      RBAR();
      STAMP(19);
#pragma unroll
      for (int i = 0; i < PD; ++i) yr[i] = Y_READ(i);
      sfor<0, 128>([&](auto ic) AI {
        constexpr int i = ic.v, rd = i + PD;
        if constexpr (rd < 128) {
          if constexpr ((rd & 15) == 0) { waitv<ops_before(i) - (rd == 112 ? 46 : 4 * (rd >> 4) + 4)>(); RBAR(); }
          if constexpr (rd == 64) STAMP(20);
          yr[rd % RING] = Y_READ(rd);
        }
        if constexpr (i < 20) Y_ISSUE(12 + i);
        if constexpr (i >= 20 && i < 24) st[i - 20] = ld128c(rs_ws, lane * 16, WS_STAT + (8 * m2 + 2 * tg) * 2048 + (i - 20) * 1024);
        if constexpr (wd_slot(i) >= 0) {
          static_assert((wd_slot(i) >> 1) - 3 <= 2 * (rd >> 4) - 2, "slot still holds activations");
          WD_ISSUE(wd_slot(i) >> 1, wd_slot(i) & 1);
        }
        if constexpr (i == 76) {
          waitv<ops_before(i) - 36>();
          pin4(st[0]); pin4(st[1]); pin4(st[2]); pin4(st[3]);
          const f32x4 t0 = __builtin_bit_cast(f32x4, st[0]) + __builtin_bit_cast(f32x4, st[1]);
          const f32x4 t1 = __builtin_bit_cast(f32x4, st[2]) + __builtin_bit_cast(f32x4, st[3]);
          float t[4], q[2];
#pragma unroll
          for (int e = 0; e < 4; ++e) {
            auto r = __builtin_amdgcn_permlane32_swap(asu(t0[e]), asu(t1[e]), false, false);
            t[e] = asf(r[0]) + asf(r[1]);
          }
#pragma unroll
          for (int e = 0; e < 2; ++e) {
            auto r = __builtin_amdgcn_permlane16_swap(asu(t[e]), asu(t[e + 2]), false, false);
            q[e] = sum8(asf(r[0]) + asf(r[1]));
          }
          float m0, d0;
          ln_stats(q[0], q[1], m0, d0);
          sts64(LB + L_STAT2 + (2 * tg + hh) * 128 + (lane & 7) * 16 + (g4 & 1) * 8, v2u{asu(-m0), asu(__builtin_amdgcn_rcpf(d0))});
        }
        if constexpr ((i & 1) == 0) pin4(yr[(i + 1) % RING]);
        SB();
        if constexpr (i < 8) mfma16_z(acc0[i], wf[0][0], yr[i % RING]);
        else mfma16_ip(acc0[i & 7], wf[0][i >> 3], yr[i % RING]);
        SB();
      });
      SB();
      asm volatile("s_nop 7" ::: "memory");
      SB();
      PRIO(0);
      STAMP(13);
      {
        v2u sv[8];
#pragma unroll
        for (int t = 0; t < 8; ++t) sv[t] = lds64(LB + L_STAT2 + t * 128 + l15 * 8);
        const f32x2 cwa = {bcast<0>(cs0[0]), bcast<0>(cs0[1])}, cwb = {bcast<0>(cs0[2]), bcast<0>(cs0[3])};
        const f32x2 swa = {bcast<8>(cs0[0]), bcast<8>(cs0[1])}, swb = {bcast<8>(cs0[2]), bcast<8>(cs0[3])};
        f32x2 va[8], vb[8];
#pragma unroll
        for (int t = 0; t < 8; ++t) {
          const f32x2 m = {asf(sv[t].x), asf(sv[t].x)}, d = {asf(sv[t].y), asf(sv[t].y)};
          va[t] = fma2(fma2(m, cwa, f32x2{acc0[t][0], acc0[t][1]}), d, swa);
          vb[t] = fma2(fma2(m, cwb, f32x2{acc0[t][2], acc0[t][3]}), d, swb);
        }
        u32 hp[4][2];
#pragma unroll
        for (int t2 = 0; t2 < 4; ++t2) {
          auto r0 = __builtin_amdgcn_permlane32_swap(asu(va[2 * t2][0]), asu(va[2 * t2 + 1][0]), false, false);
          auto r1 = __builtin_amdgcn_permlane32_swap(asu(va[2 * t2][1]), asu(va[2 * t2 + 1][1]), false, false);
          auto r2 = __builtin_amdgcn_permlane32_swap(asu(vb[2 * t2][0]), asu(vb[2 * t2 + 1][0]), false, false);
          auto r3 = __builtin_amdgcn_permlane32_swap(asu(vb[2 * t2][1]), asu(vb[2 * t2 + 1][1]), false, false);
          hp[t2][0] = pk2(asf(r0[0]) * asf(r0[1]), asf(r1[0]) * asf(r1[1]));
          hp[t2][1] = pk2(asf(r2[0]) * asf(r2[1]), asf(r3[0]) * asf(r3[1]));
        }
        const int ug = 12 * j2 + un0;
        const u32 so = ug < 172 ? hso + ug * 256 : WS_BYTES;
#pragma unroll
        for (int qd = 0; qd < 2; ++qd) {
          auto r0 = __builtin_amdgcn_permlane16_swap(hp[2 * qd][0], hp[2 * qd + 1][0], false, false);
          auto r1 = __builtin_amdgcn_permlane16_swap(hp[2 * qd][1], hp[2 * qd + 1][1], false, false);
          st128c(v4u{r0[0], r1[0], r0[1], r1[1]}, rs_ws, hvo + (2 * (g4 & 1) + (g4 >> 1)) * (43 * 1024), so + qd * (4 * 43 * 1024));
        }
      }
#pragma unroll
      for (int ks = 0; ks < 16; ++ks) { asm volatile("" : "=v"(wf[0][ks])); asm volatile("" : "=v"(wf[1][ks])); }
      asm volatile("" : "=v"(cs0), "=v"(cs1));
    }
    int w2 = w;
    asm volatile("" : "+s"(w2));
    if (w2 < 4) {
      f32x4 acc0[8], acc1[8];
      v4u yr[RING], cfr;
      WAITV(6);
      cs_part(IC<12>{}, IC<16>{});
      WAITV(0);
#pragma unroll
      for (int ks = 0; ks < 10; ++ks) PIN(wf[1][ks]);
      RBAR();
      STAMP(19);
#pragma unroll
      for (int i = 0; i < PD; ++i) yr[i] = Y_READ(i);
      sfor<0, 128>([&](auto ic) AI {
        constexpr int i = ic.v, rd = i + PD;
        if constexpr (i == 80) { WAITV(0); _Pragma("unroll") for (int ks = 10; ks < 16; ++ks) pin4(wf[1][ks]); }
        if constexpr ((i & 1) == 0) pin4(yr[(i + 1) % RING]);
        SB();
        if constexpr (i < 8) mfma16_z(acc0[i], wf[0][0], yr[i % RING]);
        else mfma16_ip(acc0[i & 7], wf[0][i >> 3], yr[i % RING]);
        SB();
        if constexpr (rd < 128) {
          if constexpr ((rd & 15) == 0) RBAR();
          if constexpr (rd == 64) STAMP(20);
          yr[rd % RING] = Y_READ(rd);
        }
        SB();
        if constexpr (i < 8) mfma16_z(acc1[i], wf[1][0], yr[i % RING]);
        else mfma16_ip(acc1[i & 7], wf[1][i >> 3], yr[i % RING]);
        SB();
        if constexpr ((i & 7) == 0) cfr = lds128(cva + (i >> 3) * 64);
        if constexpr ((i & 7) == 6) mfma16_ip(cs1, wf[1][i >> 3], cfr);
        if constexpr (i >= 36 && i < 60 && (i & 3) == 2) { WF_GET(1, 10 + ((i - 36) >> 2), 11 + ((i - 36) >> 2)); }
        SB();
      });
      SB();
      asm volatile("s_nop 7" ::: "memory");
      SB();
      STAMP(13);
      {
        v2u sv[8];
#pragma unroll
        for (int t = 0; t < 8; ++t) sv[t] = lds64(LB + L_STAT2 + t * 128 + l15 * 8);
        const f32x2 cga = {bcast<0>(cs0[0]), bcast<0>(cs0[1])}, cgb = {bcast<0>(cs0[2]), bcast<0>(cs0[3])};
        const f32x2 sga = {bcast<8>(cs0[0]), bcast<8>(cs0[1])}, sgb = {bcast<8>(cs0[2]), bcast<8>(cs0[3])};
        const f32x2 cua = {bcast<0>(cs1[0]), bcast<0>(cs1[1])}, cub = {bcast<0>(cs1[2]), bcast<0>(cs1[3])};
        const f32x2 sua = {bcast<8>(cs1[0]), bcast<8>(cs1[1])}, sub = {bcast<8>(cs1[2]), bcast<8>(cs1[3])};
        const int ug = 12 * j2 + un0;
        const u32 so = ug < 172 ? hso + ug * 256 : WS_BYTES;
        const u32 vo = hvo + (g4 & 1) * (43 * 1024) + (g4 >> 1) * 256;
#pragma unroll
        for (int t2 = 0; t2 < 4; ++t2) {
          u32 hp[2][2];
#pragma unroll
          for (int k = 0; k < 2; ++k) {
            const int t = 2 * t2 + k;
            const f32x2 m = {asf(sv[t].x), asf(sv[t].x)}, d = {asf(sv[t].y), asf(sv[t].y)};
            const f32x2 pa = fma2(fma2(m, cga, f32x2{acc0[t][0], acc0[t][1]}), d, sga) * fma2(fma2(m, cua, f32x2{acc1[t][0], acc1[t][1]}), d, sua);
            const f32x2 pb = fma2(fma2(m, cgb, f32x2{acc0[t][2], acc0[t][3]}), d, sgb) * fma2(fma2(m, cub, f32x2{acc1[t][2], acc1[t][3]}), d, sub);
            hp[k][0] = pk2(pa[0], pa[1]); hp[k][1] = pk2(pb[0], pb[1]);
          }
          auto r0 = __builtin_amdgcn_permlane16_swap(hp[0][0], hp[1][0], false, false);
          auto r1 = __builtin_amdgcn_permlane16_swap(hp[0][1], hp[1][1], false, false);
          st128c(v4u{r0[0], r1[0], r0[1], r1[1]}, rs_ws, vo, so + t2 * (2 * 43 * 1024));
        }
      }
    }
  } else {
    sfor<0, 16>([&](auto ic) AI { WD_ISSUE_K(ic.v); });
    WAITV(0);
    RBAR();
  }
  {
    u32 da0[4], da1[4], da2[4];
#pragma unroll
    for (int c = 0; c < 4; ++c) { da0[c] = wra[c] + kh * 4096; da1[c] = da0[c] + 65536; da2[c] = da0[c] + 131072; PIN(da0[c]); PIN(da1[c]); PIN(da2[c]); }
#define D_ADDR(i, c) (wd_sg(i) < 8 ? da0[c] + wd_sg(i) * 8192 : (wd_sg(i) < 16 ? da1[c] + (wd_sg(i) - 8) * 8192 : da2[c] + (wd_sg(i) - 16) * 8192))
#define D_READ(i, c) cat(trd(D_ADDR(i, c)), trd(D_ADDR(i, c) + 512))
    v4u fa[5][4];
#pragma unroll
    for (int i = 0; i < 5; ++i)
#pragma unroll
      for (int c = 0; c < 4; ++c) fa[i][c] = D_READ(i, c);
    const v4u gb4 = lds128(LB + L_VEC + 128 + kh * 64 + g4 * 16);
    WAITV(0);
    BAR();
    if (p4) SIGNAL(1, 3 * 16 + m2);
    STAMP(14);
    if (w == 0) POLL(3 * 16 + pnl, bi & 3, 15u);
    sfor<16, 22>([&](auto ic) AI { WD_ISSUE_K(ic.v); });
    RBAR();
    STAMP(15);
    constexpr int HA = 8;
    v4u hf2[HA + 1];
    const u32 hso = WS_HH + (8 * pnl + 4 * rh + tg) * (43 * 1024) + kh * (22 * 1024);
#define H_GET(i) hf2[(i) % (HA + 1)] = ld128c(rs_ws, lane * 16, (i) < 21 ? hso + (i) * 1024 : hso + (21 - kh) * 1024)
#pragma unroll
    for (int i = 0; i < HA; ++i) H_GET(i);
    f32x2 gv[4] = {unp(gb4.x), unp(gb4.y), unp(gb4.z), unp(gb4.w)}, xv[4] = {unp(x1p[0]), unp(x1p[1]), unp(x1p[2]), unp(x1p[3])};
#pragma unroll
    for (int e = 0; e < 4; ++e) { PIN(gv[e]); PIN(xv[e]); }
    u32 ea = LB + tg * 4096 + lane * 16 + kh * 2048;
    PIN(ea);
    f32x4 acc[4];
    sfor<0, 22>([&](auto ic) AI {
      constexpr int i = ic.v;
      if constexpr (i == 3 || i == 8 || i == 13) RBAR();
      if constexpr (i + HA < 22) H_GET(i + HA);
      waitv<(i + HA < 22 ? HA : 21 - i)>();
      pin4(hf2[i % (HA + 1)]);
      SB();
#pragma unroll
      for (int c = 3; c >= 0; --c) {
        if constexpr (i == 0) mfma16_z(acc[c], fa[0][c], hf2[0]);
        else mfma16_ip(acc[c], fa[i % 5][c], hf2[i % (HA + 1)]);
        if constexpr (i + 5 < 22) fa[i % 5][c] = D_READ(i + 5, c);
        SB();
      }
    });
    STAMP(16);
    asm volatile("s_nop 4" ::: "memory");
    SB();
    sts128(ea + 1024, __builtin_bit_cast(v4u, acc[3]));
    sts128(ea, __builtin_bit_cast(v4u, acc[2]));
    BAR();
    const f32x4 t0 = acc[0] + __builtin_bit_cast(f32x4, lds128(ea ^ 2048));
    const f32x4 t1 = acc[1] + __builtin_bit_cast(f32x4, lds128((ea ^ 2048) + 1024));
    u32 op[4];
#pragma unroll
    for (int e = 0; e < 4; ++e) {
      const f32x2 gp = gv[e] * unp(e < 2 ? pk2(t0[2 * e], t0[2 * e + 1]) : pk2(t1[2 * e - 4], t1[2 * e - 3]));
      const f32x2 o2 = xv[e] + unp(pk2(gp[0], gp[1]));
      op[e] = pk2(o2[0], o2[1]);
    }
    auto r0 = __builtin_amdgcn_permlane16_swap(op[0], op[2], false, false);
    auto r1 = __builtin_amdgcn_permlane16_swap(op[1], op[3], false, false);
    st128c(v4u{r0[0], r1[0], r0[1], r1[1]}, mk_srd(out, S_ * D_ * 2), orow, 0);
  }
  if (b == 0 && tid == 0) __hip_atomic_store(ctl + WS_EPOCH / 4, tag, RLX_AGENT);
}

extern "C" void kernel_launch(void* const* d_in, const int* in_sizes, int n_in, void* d_out, int out_size, void* d_ws, size_t ws_size,
                              hipStream_t stream) {
  (void)in_sizes; (void)n_in; (void)out_size; (void)ws_size;
  (void)hipFuncSetAttribute((const void*)fused_block, hipFuncAttributeMaxDynamicSharedMemorySize, L_TOTAL);
  fused_block<<<256, 512, L_TOTAL, stream>>>((const u16*)d_in[0], (const u16*)d_in[1], (const u16*)d_in[2], (const u16*)d_in[3],
                                             (const u16*)d_in[5], (const u16*)d_in[6], (const u16*)d_in[7], (const u16*)d_in[8],
                                             (const u16*)d_in[9], (u16*)d_out, (char*)d_ws);
}
```

```cpp
#include <hip/hip_runtime.h>

typedef unsigned short u16;
typedef unsigned int u32;
typedef u32 v4u __attribute__((ext_vector_type(4)));
typedef u32 v2u __attribute__((ext_vector_type(2)));
typedef float f32x2 __attribute__((ext_vector_type(2)));
typedef float f32x4 __attribute__((ext_vector_type(4)));
typedef float f32x16 __attribute__((ext_vector_type(16)));
typedef short bf16x8 __attribute__((ext_vector_type(8)));
typedef short s16x4 __attribute__((ext_vector_type(4)));
typedef __bf16 bf16x2 __attribute__((ext_vector_type(2)));
#define GAS __attribute__((address_space(1)))
#define LAS __attribute__((address_space(3)))
#define DI __device__ __forceinline__
#define RLX_AGENT __ATOMIC_RELAXED, __HIP_MEMORY_SCOPE_AGENT
typedef GAS u32 gu32;

constexpr int S_ = 2048, D_ = 512, F_ = 1376;
constexpr int WS_EPOCH = 0, WS_CNT = 983040, CNT_STRIDE = 512;
constexpr int WS_GRAN = 32768, WS_ROWST = 98304, WS_MFLAG = 20480, WS_SLAB = 131072, WS_Z = 655360, WS_STAT = 720896;
constexpr int WS_O = 1 << 20, WS_Y1 = 3 << 20, WS_HH = 5 << 20, WS_GT = 11 << 20, WS_DBG = 15 << 20, WS_BYTES = 16 << 20;
constexpr int L_B = 65536, L_T = 131072, L_MISC = 155648, L_TOTAL = 163840;
constexpr int L_CS = L_MISC, L_VEC = L_MISC, L_RED = L_MISC + 4608, L_STATX = L_MISC + 4096, L_STAT2 = L_MISC + 4096, L_CWSW = L_MISC + 5120;
constexpr int L_ARR = L_MISC + 6912, L_WPHI = L_MISC + 7168;
constexpr int L_KVT = 139264, L_QEX = 147456, L_EX3 = 139264;
constexpr unsigned CODE_SLICES = 20;
constexpr unsigned SPIN_CAP = 1u << 18;

DI float asf(u32 v) { return __builtin_bit_cast(float, v); }
DI u32 asu(float v) { return __builtin_bit_cast(u32, v); }
DI u32 asu(bf16x2 v) { return __builtin_bit_cast(u32, v); }
DI float bf2f(u32 b) { return asf(b << 16); }
DI float bflo(u32 p) { return asf(p << 16); }
DI float bfhi(u32 p) { return asf(p & 0xffff0000u); }
DI u32 pk2(float a, float b) { f32x2 v = {a, b}; return asu(__builtin_convertvector(v, bf16x2)); }
DI float rb(float a) { return bflo(pk2(a, a) & 0xffffu); }
DI v4u mk_srd(const void* p, u32 bytes) {
  unsigned long long a = (unsigned long long)p;
  return v4u{(u32)a, (u32)(a >> 32) & 0xffffu, bytes, 0x00020000u};
}
DI void glds(v4u rs, u32 voff, u32 soff, u32 ldsaddr) {
  asm volatile("s_mov_b32 m0, %3\n\ts_nop 0\n\tbuffer_load_dwordx4 %0, %1, %2 offen lds" ::"v"(voff), "s"(rs), "s"(soff), "s"(ldsaddr) : "memory");
}
template <int SO, int LO> DI void gldsi(v4u rs, u32 voff, u32 soff, u32 ldsaddr) {
  u32 t;
  asm volatile("s_add_u32 m0, %4, %6\n\ts_add_u32 %0, %3, %5\n\tbuffer_load_dwordx4 %1, %2, %0 offen lds"
               : "=&s"(t) : "v"(voff), "s"(rs), "s"(soff), "s"(ldsaddr), "i"(SO), "i"(LO) : "memory", "scc");
}
template <int SO, int LO> DI void gldsic(v4u rs, u32 voff, u32 soff, u32 ldsaddr) {
  u32 t;
  asm volatile("s_add_u32 m0, %4, %6\n\ts_add_u32 %0, %3, %5\n\tbuffer_load_dwordx4 %1, %2, %0 offen sc1 lds"
               : "=&s"(t) : "v"(voff), "s"(rs), "s"(soff), "s"(ldsaddr), "i"(SO), "i"(LO) : "memory", "scc");
}
DI v2u ld64(v4u rs, u32 voff, u32 soff) {
  v2u r;
  asm volatile("buffer_load_dwordx2 %0, %1, %2, %3 offen" : "=v"(r) : "v"(voff), "s"(rs), "s"(soff) : "memory");
  return r;
}
DI v4u ld128(v4u rs, u32 voff, u32 soff) {
  v4u r;
  asm volatile("buffer_load_dwordx4 %0, %1, %2, %3 offen" : "=v"(r) : "v"(voff), "s"(rs), "s"(soff) : "memory");
  return r;
}
DI v4u ld128c(v4u rs, u32 voff, u32 soff) {
  v4u r;
  asm volatile("buffer_load_dwordx4 %0, %1, %2, %3 offen sc1" : "=v"(r) : "v"(voff), "s"(rs), "s"(soff) : "memory");
  return r;
}
DI v2u ld64c(v4u rs, u32 voff, u32 soff) {
  v2u r;
  asm volatile("buffer_load_dwordx2 %0, %1, %2, %3 offen sc1" : "=v"(r) : "v"(voff), "s"(rs), "s"(soff) : "memory");
  return r;
}
DI void st128c(v4u v, v4u rs, u32 voff, u32 soff) {
  asm volatile("buffer_store_dwordx4 %0, %1, %2, %3 offen sc1\n\ts_nop 1" ::"v"(v), "v"(voff), "s"(rs), "s"(soff) : "memory");
}
DI void st64c(v2u v, v4u rs, u32 voff, u32 soff) {
  asm volatile("buffer_store_dwordx2 %0, %1, %2, %3 offen sc1" ::"v"(v), "v"(voff), "s"(rs), "s"(soff) : "memory");
}
#define WAITV(n) asm volatile("s_waitcnt vmcnt(" #n ")" ::: "memory")
#define BAR() asm volatile("s_waitcnt lgkmcnt(0)\n\ts_barrier" ::: "memory")
#define PIN(x) asm volatile("" : "+v"(x))
#define SB() __builtin_amdgcn_sched_barrier(0)
#define RBAR() asm volatile("s_barrier" ::: "memory")
template <int N> DI void waitv() { asm volatile("s_waitcnt vmcnt(%0)" ::"i"(N) : "memory"); }

DI v4u lds128(u32 a) { return *(const LAS v4u*)(size_t)a; }
DI v2u lds64(u32 a) { return *(const LAS v2u*)(size_t)a; }
DI void sts128(u32 a, v4u v) { *(LAS v4u*)(size_t)a = v; }
DI void sts64(u32 a, v2u v) { *(LAS v2u*)(size_t)a = v; }
DI v2u trd(u32 a) { return __builtin_bit_cast(v2u, __builtin_amdgcn_ds_read_tr16_b64_v4i16((LAS s16x4*)(size_t)a)); }
DI v4u cat(v2u a, v2u b) { return v4u{a.x, a.y, b.x, b.y}; }
DI f32x16 mfma32(v4u a, v4u b, f32x16 c) {
  return __builtin_amdgcn_mfma_f32_32x32x16_bf16(__builtin_bit_cast(bf16x8, a), __builtin_bit_cast(bf16x8, b), c, 0, 0, 0);
}
DI f32x4 mfma16(v4u a, v4u b, f32x4 c) {
  return __builtin_amdgcn_mfma_f32_16x16x32_bf16(__builtin_bit_cast(bf16x8, a), __builtin_bit_cast(bf16x8, b), c, 0, 0, 0);
}
DI float sum32(float v) {
  u32 a = asu(v), c = a;
  auto r = __builtin_amdgcn_permlane32_swap(a, c, false, false);
  return asf(r[0]) + asf(r[1]);
}
DI float sum16(float v) {
  u32 a = asu(v), c = a;
  auto r = __builtin_amdgcn_permlane16_swap(a, c, false, false);
  return asf(r[0]) + asf(r[1]);
}
DI float sum8(float v) {
  return v + asf(__builtin_amdgcn_update_dpp(0, __builtin_bit_cast(int, v), 0x128, 0xf, 0xf, false));
}
DI float sum4(float v) {
  return v + asf(__builtin_amdgcn_update_dpp(0, __builtin_bit_cast(int, v), 0x124, 0xf, 0xf, false));
}
DI void ln_stats(float s, float q, float& mean, float& sd) {
  const float m = s * (1.0f / D_);
  float var = q * (1.0f / D_) - m * m;
  var = fmaxf(var, 0.0f);
  mean = rb(m);
  sd = rb(__builtin_amdgcn_sqrtf(rb(rb(var) + 1e-5f)));
}
template <int I> DI void poll_kick(gu32* cnt) {
  unsigned long long ex;
  asm volatile("v_mov_b32 v255, 0\n\ts_mov_b64 %0, exec\n\ts_mov_b64 exec, 1\n\tglobal_atomic_add v%c2, v255, v255, %1 sc0\n\ts_mov_b64 exec, %0"
               : "=&s"(ex) : "s"(cnt), "i"(252 + I) : "memory", "v252", "v253", "v254", "v255");
}
template <unsigned CAP = SPIN_CAP> DI void poll_wait(gu32* cnt, u32 target) {
  unsigned long long ex;
  u32 v, n;
  asm volatile(
      "s_mov_b64 %0, exec\n\ts_mov_b64 exec, 1\n\ts_mov_b32 %2, %5\n"
      "1:\n\t"
      "s_waitcnt vmcnt(2)\n\tv_readfirstlane_b32 %1, v252\n\ts_nop 1\n\ts_cmp_eq_u32 %1, %4\n\ts_cbranch_scc1 2f\n\t"
      "global_atomic_add v252, v255, v255, %3 sc0\n\t"
      "s_waitcnt vmcnt(2)\n\tv_readfirstlane_b32 %1, v253\n\ts_nop 1\n\ts_cmp_eq_u32 %1, %4\n\ts_cbranch_scc1 2f\n\t"
      "global_atomic_add v253, v255, v255, %3 sc0\n\t"
      "s_waitcnt vmcnt(2)\n\tv_readfirstlane_b32 %1, v254\n\ts_nop 1\n\ts_cmp_eq_u32 %1, %4\n\ts_cbranch_scc1 2f\n\t"
      "global_atomic_add v254, v255, v255, %3 sc0\n\t"
      "s_sub_u32 %2, %2, 1\n\ts_cmp_lg_u32 %2, 0\n\ts_cbranch_scc1 1b\n"
      "2:\n\t"
      "s_mov_b64 exec, %0"
      : "=&s"(ex), "=&s"(v), "=&s"(n)
      : "s"(cnt), "s"(target), "i"(CAP)
      : "memory", "scc", "v252", "v253", "v254", "v255");
}
template <unsigned CAP = SPIN_CAP> DI void poll_cnt(gu32* cnt, u32 target) {
  poll_kick<0>(cnt); __builtin_amdgcn_s_sleep(2);
  poll_kick<1>(cnt); __builtin_amdgcn_s_sleep(2);
  poll_kick<2>(cnt);
  poll_wait<CAP>(cnt, target);
}
DI float max16(const f32x16& p) {
  const float a = fmaxf(fmaxf(p[0], p[1]), p[2]), b = fmaxf(fmaxf(p[3], p[4]), p[5]), c = fmaxf(fmaxf(p[6], p[7]), p[8]);
  const float d = fmaxf(fmaxf(p[9], p[10]), p[11]), e = fmaxf(fmaxf(p[12], p[13]), p[14]);
  return fmaxf(fmaxf(fmaxf(a, b), c), fmaxf(fmaxf(d, e), p[15]));
}
DI float min16(const f32x16& p) {
  const float a = fminf(fminf(p[0], p[1]), p[2]), b = fminf(fminf(p[3], p[4]), p[5]), c = fminf(fminf(p[6], p[7]), p[8]);
  const float d = fminf(fminf(p[9], p[10]), p[11]), e = fminf(fminf(p[12], p[13]), p[14]);
  return fminf(fminf(fminf(a, b), c), fminf(fminf(d, e), p[15]));
}
DI void expnorm(const f32x16& p, float l, float a, u32* ph) {
  f32x2 e[8];
#pragma unroll
  for (int i = 0; i < 8; ++i) {
    const f32x2 t = __builtin_elementwise_fma(f32x2{p[2 * i], p[2 * i + 1]}, f32x2{l, l}, f32x2{a, a});
    e[i] = f32x2{__builtin_amdgcn_exp2f(t[0]), __builtin_amdgcn_exp2f(t[1])};
  }
  const f32x2 s = ((e[0] + e[1]) + (e[2] + e[3])) + ((e[4] + e[5]) + (e[6] + e[7]));
  const float r = __builtin_amdgcn_rcpf(s[0] + s[1]);
#pragma unroll
  for (int i = 0; i < 8; ++i) { const f32x2 t = e[i] * f32x2{r, r}; ph[i] = pk2(t[0], t[1]); }
}
constexpr float LOG2E = 1.44269504088896341f;
DI void softmax16(const f32x16& p, u32 (&ph)[8]) { expnorm(p, LOG2E, -max16(p) * LOG2E, ph); }
DI void hedgehog(const f32x16& p, u32 (&ph)[16]) {
  expnorm(p, LOG2E, -max16(p) * LOG2E, ph);
  expnorm(p, -LOG2E, min16(p) * LOG2E, ph + 8);
}
DI v4u pack8(const f32x16& x, int s) {
  return v4u{pk2(x[8 * s], x[8 * s + 1]), pk2(x[8 * s + 2], x[8 * s + 3]), pk2(x[8 * s + 4], x[8 * s + 5]), pk2(x[8 * s + 6], x[8 * s + 7])};
}
DI void ex_put(u32 a, const f32x16& x) {
#pragma unroll
  for (int i = 0; i < 4; ++i) sts128(a + i * 1024, __builtin_bit_cast(v4u, f32x4{x[4 * i], x[4 * i + 1], x[4 * i + 2], x[4 * i + 3]}));
}
DI void ex_add(u32 a, f32x16& x) {
#pragma unroll
  for (int i = 0; i < 4; ++i) {
    const f32x4 t = __builtin_bit_cast(f32x4, lds128(a + i * 1024));
    x[4 * i] += t[0]; x[4 * i + 1] += t[1]; x[4 * i + 2] += t[2]; x[4 * i + 3] += t[3];
  }
}

#define STAMP(i) do {} while (0)

template <int I> struct IC { static constexpr int v = I; };
template <int I, int N, class Fn> DI void sfor(Fn&& f) {
  if constexpr (I < N) { f(IC<I>{}); sfor<I + 1, N>(f); }
}
#define AI __attribute__((always_inline))
#define PRIO(n) __builtin_amdgcn_s_setprio(n)
constexpr int wd_slot(int i) { return (i >= 24 && i < 120 && (i - 24) % 3 == 0) ? (i - 24) / 3 : -1; }
constexpr int ops_before(int i) { return i <= 24 ? 12 + i : (i >= 118 ? 68 : 36 + (i - 24 + 2) / 3); }
constexpr int wd_sg(int i) { return i < 3 ? 16 + i : i - 3; }
template <int N> DI float bcast(float v) { return asf(__builtin_amdgcn_mov_dpp((int)asu(v), 0x150 + N, 0xf, 0xf, true)); }
DI f32x2 unp(u32 p) { return f32x2{bflo(p), bfhi(p)}; }
DI f32x2 fma2(f32x2 a, f32x2 b, f32x2 c) { return __builtin_elementwise_fma(a, b, c); }
DI void mfma16_ip(f32x4& c, v4u a, v4u b) { asm volatile("v_mfma_f32_16x16x32_bf16 %0, %1, %2, %0" : "+v"(c) : "v"(a), "v"(b)); }
DI void mfma16_z(f32x4& c, v4u a, v4u b) { asm volatile("v_mfma_f32_16x16x32_bf16 %0, %1, %2, 0" : "=&v"(c) : "v"(a), "v"(b)); }
DI void pinf(f32x4& v) { PIN(v); }
DI void pin4(v4u& v) { PIN(v); }
DI void fma2_ip(f32x2& x, f32x2 a, f32x2 c) { asm volatile("v_pk_fma_f32 %0, %0, %1, %2" : "+v"(x) : "v"(a), "v"(c)); }
DI void mfma32_ip(f32x16& c, v4u a, v4u b) { asm volatile("v_mfma_f32_32x32x16_bf16 %0, %1, %2, %0" : "+v"(c) : "v"(a), "v"(b)); }
template <int N> DI void fmac_bc(float& h, float c, float x) {
  asm("v_fmac_f32_dpp %0, %1, %2 row_newbcast:%3 row_mask:0xf bank_mask:0xf" : "+v"(h) : "v"(c), "v"(x), "i"(N));
}

__global__ void __launch_bounds__(512, 2) __attribute__((amdgpu_num_vgpr(248)))
fused_block(const u16* __restrict__ x, const u16* __restrict__ wqkv, const u16* __restrict__ wphi, const u16* __restrict__ wo,
            const u16* __restrict__ wg, const u16* __restrict__ wu, const u16* __restrict__ wd, const u16* __restrict__ cvec,
            const u16* __restrict__ wmod, u16* __restrict__ out, char* __restrict__ ws) {
  extern __shared__ __attribute__((aligned(16))) char smem[];
  const u32 LB = (u32)(size_t)(LAS char*)smem;
  unsigned long long lk0, lk1;
  asm volatile("s_memrealtime %0\n\ts_waitcnt lgkmcnt(0)" : "=s"(lk0)::"memory");
  SB();
  u32 cpf;
  {
    unsigned long long pc;
    u32 sl = ((blockIdx.x >> 3) % CODE_SLICES) * 2048u, off;
    asm volatile(
        "s_getpc_b64 %2\n\tv_lshlrev_b32 %1, 7, %3\n\tv_cmp_lt_u32 vcc, 63, %3\n\tv_mov_b32 %0, %4\n\ts_nop 0\n\tv_cndmask_b32 %0, 0, %0, vcc\n\t"
        "v_add_u32 %1, %1, %0\n\tv_cmp_gt_u32 vcc, 0x50, %3\n\ts_nop 1\n\tv_cndmask_b32 %1, 0, %1, vcc\n\tglobal_load_dword %0, %1, %2"
        : "=&v"(cpf), "=&v"(off), "=&s"(pc) : "v"(threadIdx.x), "s"(sl) : "memory", "vcc");
    SB();
  }
  asm volatile("" ::"s"(x), "s"(wqkv), "s"(wphi), "s"(wo), "s"(wg), "s"(wu), "s"(wd), "s"(cvec), "s"(wmod), "s"(out), "s"(ws));
  SB();
  asm volatile("s_memrealtime %0\n\ts_waitcnt lgkmcnt(0)" : "=s"(lk1)::"memory");
  const int tid = threadIdx.x, lane = tid & 63;
  int w = __builtin_amdgcn_readfirstlane(tid >> 6);
  asm volatile("s_nop 4" : "+s"(w));
  const int b = blockIdx.x, g = b & 7, bi = b >> 3;
  const int m2 = 4 * (g >> 1) + (bi >> 3), j2 = 8 * (g & 1) + (bi & 7);
  const int pnl = 2 * g + (bi >> 4), rh = (bi >> 3) & 1, cb = bi & 7;
  const v4u rs_x = mk_srd(x, S_ * D_ * 2), rs_ws = mk_srd(ws, WS_BYTES);
  gu32* const ctl = (gu32*)ws;
  const int tg = w & 3, kh = w >> 2;
  const int c32 = lane & 31, hh = lane >> 5;
  const int l15 = lane & 15, g4 = lane >> 4, tq = (lane & 15) >> 2, tp = lane & 3;

  const bool prod = b < 96, sprod = b >= 96 && b < 224;
  const int cg = b >> 1, kp = b & 1;
  const u32 xld = LB + w * 16384;
  v4u xr[4];
  u32 tagv;
  if (w == 0 && b < 4) __hip_atomic_store(ctl + (WS_CNT + (b * 64 + lane) * 128) / 4, 0u, RLX_AGENT);
  asm volatile("buffer_load_dword %0, off, %1, 0 sc1" : "=v"(tagv) : "s"(rs_ws) : "memory");
  v2u mc2;
  if (prod && w == 0) {
    const v4u rs_m = mk_srd(wmod, D_ * 6 * D_ * 2), rs_c = mk_srd(cvec, D_ * 2);
    mc2 = ld64(rs_c, lane * 8, kp * 512);
    const u32 vo = (lane >> 3) * 6144 + (((lane & 7) ^ (((lane >> 4) & 1) << 1)) << 4);
    const u32 so = 256 * kp * 6144 + cg * 128;
    sfor<0, 32>([&](auto ic) AI { gldsi<ic.v * 8 * 6144, (ic.v < 24 ? L_T + ic.v * 1024 : (ic.v - 24) * 1024)>(rs_m, (ic.v & 1) ? vo ^ 64 : vo, so, LB); });
  }
  if (kh == 0 && sprod) {
#pragma unroll
    for (int r = 0; r < 4; ++r) xr[r] = ld128(rs_x, lane * 16, (16 * (b - 96) + 4 * w + r) * 1024);
  }
  if (w == 5) glds(mk_srd(wphi, 32 * 256 * 2), lane * 16, j2 * 1024, LB + L_WPHI);
  if (prod) {
    if (w == 0) {
      asm volatile("s_waitcnt vmcnt(32)" : "+v"(mc2), "+v"(tagv)::"memory");
      float sv[4] = {bflo(mc2.x), bfhi(mc2.x), bflo(mc2.y), bfhi(mc2.y)};
#pragma unroll
      for (int e = 0; e < 4; ++e) sv[e] = sv[e] * __builtin_amdgcn_rcpf(1.0f + __expf(-sv[e]));
      sts64(LB + 8192 + lane * 8, v2u{pk2(sv[0], sv[1]), pk2(sv[2], sv[3])});
    }
    RBAR();
  }
  const u32 xvo0 = (lane >> 3) * 1024 + (((lane & 7) ^ (lane >> 4)) << 4), xvo1 = xvo0 ^ 64;
  const int rot = bi & 3;
  const u32 xso = (128 * m2 + 32 * tg) * 1024 + kh * 512 + rot * 128;
#define X_ISSUE(q4)                                                                       \
  do {                                                                                    \
    gldsi<0, (q4) * 4096>(rs_x, xvo0, xso ^ ((q4) * 128), xld);                           \
    gldsi<8192, (q4) * 4096 + 1024>(rs_x, xvo1, xso ^ ((q4) * 128), xld);                 \
    gldsi<16384, (q4) * 4096 + 2048>(rs_x, xvo0, xso ^ ((q4) * 128), xld);                \
    gldsi<24576, (q4) * 4096 + 3072>(rs_x, xvo1, xso ^ ((q4) * 128), xld);                \
  } while (0)
  if (kh == 0 && sprod) {
    asm volatile("s_waitcnt vmcnt(0)" : "+v"(xr[0]), "+v"(xr[1]), "+v"(xr[2]), "+v"(xr[3]), "+v"(tagv)::"memory");
    float s[4], q[4], t[2];
#pragma unroll
    for (int r = 0; r < 4; ++r) {
      s[r] = 0.f; q[r] = 0.f;
#pragma unroll
      for (int e = 0; e < 4; ++e) {
        const float lo = bflo(xr[r][e]), hi = bfhi(xr[r][e]);
        s[r] += lo + hi; q[r] = __builtin_fmaf(lo, lo, __builtin_fmaf(hi, hi, q[r]));
      }
    }
#pragma unroll
    for (int r = 0; r < 2; ++r) {
      auto rs = __builtin_amdgcn_permlane32_swap(asu(s[r]), asu(s[r + 2]), false, false);
      auto rq = __builtin_amdgcn_permlane32_swap(asu(q[r]), asu(q[r + 2]), false, false);
      s[r] = asf(rs[0]) + asf(rs[1]); q[r] = asf(rq[0]) + asf(rq[1]);
    }
    {
      auto rs = __builtin_amdgcn_permlane16_swap(asu(s[0]), asu(s[1]), false, false);
      auto rq = __builtin_amdgcn_permlane16_swap(asu(q[0]), asu(q[1]), false, false);
      t[0] = asf(rs[0]) + asf(rs[1]); t[1] = asf(rq[0]) + asf(rq[1]);
    }
#pragma unroll
    for (int r = 0; r < 2; ++r) {
      t[r] = sum4(sum8(t[r]));
      t[r] += asf(__builtin_amdgcn_update_dpp(0, (int)asu(t[r]), 0x122, 0xf, 0xf, false));
      t[r] += asf(__builtin_amdgcn_update_dpp(0, (int)asu(t[r]), 0x121, 0xf, 0xf, false));
    }
    float mean, sd;
    ln_stats(t[0], t[1], mean, sd);
    if (l15 == 0) st64c(v2u{pk2(mean, sd), tagv + 1u}, rs_ws, g4 * 8, WS_ROWST + (16 * (b - 96) + 4 * w) * 8);
  }
  if (prod && w == 0) {
    PRIO(3);
    v4u sb[8];
#pragma unroll
    for (int ks = 0; ks < 8; ++ks) sb[ks] = lds128(LB + 8192 + ks * 64 + g4 * 16);
    u32 ma[4], mb[4];
#pragma unroll
    for (int c = 0; c < 4; ++c) {
      mb[c] = LB + (8 * g4 + tq) * 128 + tp * 8 + ((c ^ (((tq >> 1) & 1) | ((g4 & 1) << 1))) << 5); ma[c] = mb[c] + L_T;
      PIN(ma[c]); PIN(mb[c]);
    }
    f32x4 acc[4];
#pragma unroll
    for (int c = 0; c < 4; ++c) acc[c] = f32x4{0.f, 0.f, 0.f, 0.f};
    v4u fm[2][2][4];
#define M_READ(h2) _Pragma("unroll") for (int k2 = 0; k2 < 2; ++k2) _Pragma("unroll") for (int c = 0; c < 4; ++c) {               \
      const u32 ad = (h2) < 3 ? ma[c] + (2 * (h2) + k2) * 4096 : mb[c] + k2 * 4096;                                                \
      fm[(h2) & 1][k2][c] = cat(trd(ad), trd(ad + 512)); }
    WAITV(24);
    M_READ(0);
    sfor<0, 4>([&](auto ic) AI {
      constexpr int h2 = ic.v;
      if constexpr (h2 < 3) { waitv<16 - 8 * h2>(); M_READ(h2 + 1); }
      SB();
#pragma unroll
      for (int k2 = 0; k2 < 2; ++k2)
#pragma unroll
        for (int c = 0; c < 4; ++c) acc[c] = mfma16(sb[2 * h2 + k2], fm[h2 & 1][k2][c], acc[c]);
      SB();
    });
    const float lo = (g4 & 1) ? acc[1][0] : acc[0][0], hi = (g4 & 1) ? acc[3][0] : acc[2][0];
    st64c(v2u{asu(hh ? hi : lo), tagv + 1u}, rs_ws, lane * 8, WS_GRAN + kp * 24576 + cg * 512);
    PRIO(0);
  }
  STAMP(1);
  X_ISSUE(0); X_ISSUE(1); X_ISSUE(2);
  const v4u rs_w = mk_srd(wqkv, D_ * 3 * D_ * 2);
  const u32 wvo = ((lane >> 2) * 1536 + 8 * (lane & 3)) * 2;
  const u32 wso = ((256 * (tg & 1) + 16 * (tg >> 1)) * 1536 + 32 * j2) * 2;
#define W_ISSUE(part, n) gldsi<((n) & 1) * 32 * 3072 + (part) * 1024, (n) * 1024 + ((part) == 0 ? 8192 : 0)>(rs_w, wvo, wso + (((n) >> 1) ^ rot) * (64 * 3072), xld)
  const bool p4 = j2 < 15;
  const int un0 = w < 4 ? 2 * w : 4 + w;
  f32x2 xf[16][4];
  u32 tagl;
  {
    u32 xa[4];
#pragma unroll
    for (int sp = 0; sp < 4; ++sp) { xa[sp] = LB + w * 16384 + c32 * 128 + (((2 * sp + hh) ^ ((c32 >> 1) & 7)) << 4); PIN(xa[sp]); }
    v4u gr[4];
    v2u gs;
#pragma unroll
    for (int q4 = 0; q4 < 4; ++q4) {
      if (q4 == 0) { X_ISSUE(3); WAITV(12); }
      if (q4 == 1) WAITV(8);
      if (q4 == 2) WAITV(4);
      if (q4 == 3) {
        WAITV(0);
        if (kh == 1) {
          PRIO(3);
          gs = ld64c(rs_ws, c32 * 8, WS_ROWST + (128 * m2 + 32 * tg) * 8);
#pragma unroll
          for (int n = 0; n < 4; ++n) gr[n] = ld128c(rs_ws, lane * 16, WS_GRAN + (n & 1) * 24576 + (tg * 2 + (n >> 1)) * 1024);
          PRIO(0);
        }
      }
      v4u t[4];
#pragma unroll
      for (int sp = 0; sp < 4; ++sp) t[sp] = lds128(xa[sp] + q4 * 4096);
#pragma unroll
      for (int sp = 0; sp < 4; ++sp) {
#pragma unroll
        for (int e = 0; e < 4; ++e) xf[4 * q4 + sp][e] = f32x2{bflo(t[sp][e]), bfhi(t[sp][e])};
        f32x2(&f)[4] = xf[4 * q4 + sp];
        asm volatile("" : "+v"(f[0]), "+v"(f[1]), "+v"(f[2]), "+v"(f[3]));
      }
      SB();
    }
    if (kh == 0) {
      if (w == 1) asm volatile("buffer_load_dword v251, %0, %1, %2 offen" ::"v"(lane * 128), "s"(mk_srd(wd, F_ * D_ * 2)), "s"(b * 5504) : "memory", "v251");
      if (w == 2) asm volatile("buffer_load_dword v251, %0, %1, %2 offen" ::"v"(l15 * 128), "s"(mk_srd(wo, D_ * D_ * 2)), "s"(b * 2048) : "memory", "v251");
    }
    PIN(tagv);
    PIN(cpf);
    tagl = tagv + 1u;
    STAMP(22);
    if (kh == 0) sfor<0, 8>([&](auto ic) AI { W_ISSUE(1, ic.v); });
    if (kh == 1) {
      asm volatile("s_waitcnt vmcnt(0)" : "+v"(gs), "+v"(gr[0]), "+v"(gr[1]), "+v"(gr[2]), "+v"(gr[3])::"memory");
      for (u32 spins = 0;; ++spins) {
        bool ok = gs.y == tagl;
#pragma unroll
        for (int n = 0; n < 4; ++n) ok = ok && gr[n].y == tagl && gr[n].w == tagl;
        if (__all(ok) || spins >= SPIN_CAP) break;
        if (spins) __builtin_amdgcn_s_sleep(4);
        gs = ld64c(rs_ws, c32 * 8, WS_ROWST + (128 * m2 + 32 * tg) * 8);
#pragma unroll
        for (int n = 0; n < 4; ++n) gr[n] = ld128c(rs_ws, lane * 16, WS_GRAN + (n & 1) * 24576 + (tg * 2 + (n >> 1)) * 1024);
        asm volatile("s_waitcnt vmcnt(0)" : "+v"(gs), "+v"(gr[0]), "+v"(gr[1]), "+v"(gr[2]), "+v"(gr[3])::"memory");
      }
      *(LAS u32*)(size_t)(LB + L_STATX + (tg * 32 + c32) * 4) = gs.x;
      const float one = tg >= 2 ? 1.0f : 0.0f;
#pragma unroll
      for (int n = 0; n < 2; ++n) {
        const u32 p = pk2(asf(gr[2 * n].x) + asf(gr[2 * n + 1].x), asf(gr[2 * n].z) + asf(gr[2 * n + 1].z));
        const u32 q = pk2(bflo(p) + one, bfhi(p) + one);
        sts64(LB + L_CS + tg * 1024 + (((2 * n + hh) ^ rot) << 8) + c32 * 8, v2u{q << 16, q & 0xffff0000u});
      }
    }
    BAR();
    STAMP(2);
    const u32 st = *(const LAS u32*)(size_t)(LB + L_STATX + (tg * 32 + c32) * 4);
    const float r = __builtin_amdgcn_rcpf(bfhi(st)), mr = -bflo(st) * r;
    const f32x2 r2 = {r, r}, mr2 = {mr, mr};
    if (kh == 1) sfor<0, 4>([&](auto ic) AI { W_ISSUE(2, ic.v); });
#pragma unroll
    for (int s = 0; s < 16; ++s)
#pragma unroll
      for (int e = 0; e < 4; ++e) fma2_ip(xf[s][e], r2, mr2);
  }
  const u32 tag = __builtin_amdgcn_readfirstlane(tagl);
  f32x16 zero;
#pragma unroll
  for (int i = 0; i < 16; ++i) zero[i] = 0.f;
  v4u hf[16];
  u32 wk = LB + kh * 16384 + (8 * hh + tq) * 64 + (g4 & 1) * 32 + tp * 8, wv = wk + L_B;
#define W_FRAG(base, s) cat(trd((base) + ((s) & 1) * 32768 + ((s) >> 1) * 1024), trd((base) + ((s) & 1) * 32768 + ((s) >> 1) * 1024 + 256))
  f32x16 ak = zero;
  {
    u32 ca = LB + L_CS + (256 * kh + 8 * hh) * 4, cda = ca + 2048 + (l15 >> 3) * 64 + (l15 & 7) * 4;
    PIN(wk); PIN(wv); PIN(ca); PIN(cda);
    float c1[8];
#pragma unroll
    for (int r = 0; r < 8; ++r) c1[r] = *(const LAS float*)(size_t)(cda + r * 128);
    f32x4 hs[2][2];
    v4u fw[2];
#define H_READ(s) do { hs[(s) & 1][0] = __builtin_bit_cast(f32x4, lds128(ca + (s) * 64)); hs[(s) & 1][1] = __builtin_bit_cast(f32x4, lds128(ca + (s) * 64 + 16)); } while (0)
#define H_FMA(s, hf_) do { float t0_ = hs[(s) & 1][hf_][0], t1_ = hs[(s) & 1][hf_][1], t2_ = hs[(s) & 1][hf_][2], t3_ = hs[(s) & 1][hf_][3]; \
    fmac_bc<8 * ((s) & 1) + 4 * (hf_) + 0>(t0_, c1[(s) >> 1], xf[s][2 * (hf_) + 0][0]); fmac_bc<8 * ((s) & 1) + 4 * (hf_) + 1>(t1_, c1[(s) >> 1], xf[s][2 * (hf_) + 0][1]); \
    fmac_bc<8 * ((s) & 1) + 4 * (hf_) + 2>(t2_, c1[(s) >> 1], xf[s][2 * (hf_) + 1][0]); fmac_bc<8 * ((s) & 1) + 4 * (hf_) + 3>(t3_, c1[(s) >> 1], xf[s][2 * (hf_) + 1][1]); \
    u32 p0_ = pk2(t0_, t1_), p1_ = pk2(t2_, t3_); PIN(p0_); PIN(p1_); \
    hf[s][2 * (hf_)] = p0_; hf[s][2 * (hf_) + 1] = p1_; } while (0)
    H_READ(0); H_READ(1);
    SB();
    H_FMA(0, 0); H_FMA(0, 1);
    if (kh == 0) WAITV(4);
    else sfor<4, 8>([&](auto ic) AI { W_ISSUE(2, ic.v); });
    RBAR();
    STAMP(3);
    fw[0] = W_FRAG(wk, 0);
    SB();
    sfor<0, 16>([&](auto ic) AI {
      constexpr int s = ic.v;
      if constexpr (s == 7) {
        if (kh == 0) {
          WAITV(0);
          if (p4 && tg != 0) {
            const v4u rs_g = mk_srd(wg, D_ * F_ * 2), rs_u = mk_srd(wu, D_ * F_ * 2);
            const u32 P = (tg - 1) * 64 + lane, gvo = (P / 12) * (F_ * 2) + (((P % 12) ^ (((P / 12) >> 2) & 1)) << 4);
            const u32 gso = 32 * m2 * (F_ * 2) + j2 * 192, gld = LB + L_T + tg * 1024 - 1024;
            gldsi<0, 0>(rs_g, gvo, gso, gld); gldsi<16 * F_ * 2, 3072>(rs_g, gvo, gso, gld);
            gldsi<0, 6144>(rs_u, gvo ^ 32, gso, gld); gldsi<16 * F_ * 2, 9216>(rs_u, gvo ^ 32, gso, gld);
          }
        }
        RBAR();
      }
      if constexpr (s < 15) fw[(s + 1) & 1] = W_FRAG(wk, s + 1);
      if constexpr (s < 14) H_READ((s + 2) & 15);
      SB();
      if constexpr (s < 15) H_FMA((s + 1) & 15, 0);
      SB();
      ak = mfma32(fw[s & 1], hf[s], ak);
      SB();
      if constexpr (s < 15) H_FMA((s + 1) & 15, 1);
      SB();
    });
  }
  STAMP(4);
  const v4u rs_wo = mk_srd(wo, D_ * D_ * 2);
  u32 dvo;
  {
    const u32 r8 = lane >> 3, pc = lane & 7;
    const u32 f = ((r8 >> 1) & 1) | ((w & 1) << 1);
    dvo = r8 * 1024 + ((((pc >> 1) ^ f) * 2 + (pc & 1)) << 4);
  }
#define SIGNAL(wv, cnt) do { if (tid >= (wv) * 64 && tid < (wv) * 64 + 4) __hip_atomic_fetch_add(ctl + (WS_CNT + (cnt) * CNT_STRIDE + lane * 128) / 4, 1u, RLX_AGENT); } while (0)
#define POLL(cnt, rep, target) do { PRIO(3); poll_cnt(ctl + (WS_CNT + (cnt) * CNT_STRIDE + (rep) * 128) / 4, target); PRIO(0); } while (0)
  v4u wf[2][16];
  const u32 wfo = WS_GT + (j2 * 12 + un0) * 16384;
#define WF_GET(n, lo, hi) _Pragma("unroll") for (int k_ = (lo); k_ < (hi); ++k_) wf[n][k_] = ld128c(rs_ws, lane * 16, wfo + (n) * 16384 + k_ * 1024)
#define WO_ISSUE(n) gldsi<(n) * 32768, ((n) >> 1) * 16384 + ((n) & 1) * 4096>(rs_wo, dvo, tg * 8192 + cb * 128, LB + tg * 1024)
  auto proj = [&](u32 base, auto hook) AI {
    f32x16 acc = zero;
    v4u fq[4];
#pragma unroll
    for (int s = 0; s < 3; ++s) fq[s] = W_FRAG(base, s);
    sfor<0, 16>([&](auto ic) AI {
      constexpr int s = ic.v;
      if constexpr (s + 3 < 16) fq[(s + 3) & 3] = W_FRAG(base, s + 3);
      hook(ic);
      SB();
      acc = mfma32(fq[s & 3], hf[s], acc);
      SB();
    });
    return acc;
  };
  v4u pf0, pf1;
  {
    const u32 sc = LB + tg * 16384 + 8192;
    const u32 pa = LB + L_WPHI + (tp & 1) * 512 + (4 * hh + tq) * 32 + (8 * (g4 & 1) + 4 * (tp >> 1)) * 2;
    const v4u pf = cat(trd(pa), trd(pa + 256));
    u32 m1 = 0u - ((lane >> 2) & 1);
    PIN(m1);
    pf1 = pf & m1; pf0 = pf ^ pf1;
    const u32 sg = kh ? 0x80008000u : 0u;
    const v4u pg0 = pf0 ^ v4u{sg, sg, sg, sg}, pg1 = pf1 ^ v4u{sg, sg, sg, sg};
    const v4u k0 = pack8(ak, 0), k1 = pack8(ak, 1);
    sts128(sc + (kh ^ 1) * 2048 + lane * 16, k0);
    sts128(sc + (kh ^ 1) * 2048 + 1024 + lane * 16, k1);
    f32x16 p = mfma32(pg0, k0, zero);
    p = mfma32(pg1, k1, p);
    if (kh == 1) WAITV(0);
    BAR();
    STAMP(23);
    {
      const v4u o0 = lds128(sc + kh * 2048 + lane * 16), o1 = lds128(sc + kh * 2048 + 1024 + lane * 16);
      p = mfma32(pg0, o0, p);
      p = mfma32(pg1, o1, p);
      SB();
    }
    auto feat = [&]() AI {
      u32 ph[8];
      softmax16(p, ph);
      const u32 row = sc + kh * 2048 + c32 * 64;
      sts128(row + (((2 * hh) ^ ((c32 >> 1) & 3)) << 4), v4u{ph[0], ph[1], ph[2], ph[3]});
      sts128(row + (((2 * hh + 1) ^ ((c32 >> 1) & 3)) << 4), v4u{ph[4], ph[5], ph[6], ph[7]});
    };
    if (kh == 0) feat();
    else sfor<0, 8>([&](auto ic) AI { W_ISSUE(0, ic.v); });
    {
      const f32x16 av = proj(wv, [](auto) {});
      const u32 row = sc + 4096 + kh * 2048 + c32 * 64 + 8 * hh;
#pragma unroll
      for (int i = 0; i < 4; ++i)
        sts64(row + ((i ^ ((c32 >> 1) & 3)) << 4), v2u{pk2(av[4 * i], av[4 * i + 1]), pk2(av[4 * i + 2], av[4 * i + 3])});
    }
    if (kh == 1) feat();
    u32 aa[2], ba[2];
#pragma unroll
    for (int t = 0; t < 2; ++t) {
      const u32 tok = 8 * g4 + 4 * t + tq;
      const u32 o = LB + 8192 + tok * 64 + (((2 * (tg >> 1) + (tp >> 1)) ^ ((tok >> 1) & 3)) << 4) + 8 * (tp & 1);
      aa[t] = o + (tg & 1) * 2048; ba[t] = o + 4096;
      PIN(aa[t]); PIN(ba[t]);
    }
    if (kh == 0) WAITV(0);
    BAR();
    STAMP(18);
    if (kh == 0) {
      PRIO(3);
      f32x4 kv0 = {0.f, 0.f, 0.f, 0.f}, kv1 = kv0, zz = kv0;
      const v4u ones = v4u{0x3f803f80u, 0x3f803f80u, 0x3f803f80u, 0x3f803f80u};
      v4u a[4], b0[4], b1[4];
#pragma unroll
      for (int ks = 0; ks < 4; ++ks) {
        a[ks] = cat(trd(aa[0] + ks * 16384), trd(aa[1] + ks * 16384));
        b0[ks] = cat(trd(ba[0] + ks * 16384), trd(ba[1] + ks * 16384));
        b1[ks] = cat(trd(ba[0] + ks * 16384 + 2048), trd(ba[1] + ks * 16384 + 2048));
      }
      SB();
#pragma unroll
      for (int ks = 0; ks < 4; ++ks) {
        kv0 = mfma16(a[ks], b0[ks], kv0); kv1 = mfma16(a[ks], b1[ks], kv1); zz = mfma16(a[ks], ones, zz);
      }
      kv0 += kv1;
      st64c(v2u{pk2(kv0[0], kv0[1]), pk2(kv0[2], kv0[3])}, rs_ws, lane * 8, WS_SLAB + ((j2 * 4 + w) * 16 + m2) * 512);
      if (l15 == 0) st64c(v2u{pk2(zz[0], zz[1]), pk2(zz[2], zz[3])}, rs_ws, g4 * 8, WS_Z + ((j2 * 4 + w) * 16 + m2) * 32);
      sfor<0, 8>([&](auto ic) AI { WO_ISSUE(ic.v); });
      WAITV(8);
      PRIO(0);
    } else {
      const u32 gb = LB + L_T + (4 * g4 + tq) * 192 + 8 * (tp & 1);
      const u32 ga = gb + (((2 * tg + (tp >> 1)) ^ (g4 & 1)) << 4), gc = gb + (tp >> 1) * 6144 + (((8 + tg) ^ (g4 & 1) ^ (tp & 2)) << 4);
      const v4u gt0 = cat(trd(ga), trd(ga + 3072)), gt1 = cat(trd((ga ^ 32) + 6144), trd((ga ^ 32) + 9216)), gt2 = cat(trd(gc), trd(gc + 3072));
      const u32 gto = p4 ? WS_GT + j2 * (12 * 16384) + m2 * 1024 : WS_BYTES;
      st128c(gt0, rs_ws, lane * 16, gto + tg * 32768);
      st128c(gt1, rs_ws, lane * 16, gto + tg * 32768 + 16384);
      st128c(gt2, rs_ws, lane * 16, gto + (8 + tg) * 16384);
      WAITV(0);
    }
    RBAR();
    SIGNAL(1, 0 * 16 + j2);
  }
  STAMP(5);
  const u32 orow = (128 * pnl + 64 * rh + 16 * tg + l15) * 1024 + (64 * cb + 32 * kh + 16 * (g4 & 1) + 8 * (g4 >> 1)) * 2;
  v4u xres;
  u32 phq[16];
  {
    if (kh == 1) sfor<8, 16>([&](auto ic) AI { WO_ISSUE(ic.v); });
    xres = ld128(rs_x, orow, 0);
    gu32* const e2 = ctl + (WS_CNT + (0 * 16 + j2) * CNT_STRIDE + (m2 & 3) * 128) / 4;
    const f32x16 aq = proj(wv + 8192, [&](auto ic) AI {
      if constexpr (ic.v == 7 || ic.v == 11 || ic.v == 15) { if (w == 0) poll_kick<((ic.v - 7) >> 2) & 3>(e2); }
    });
    const v4u q0 = pack8(aq, 0), q1 = pack8(aq, 1);
    const u32 ea = LB + L_QEX + tg * 2048 + lane * 16;
    f32x16 p;
    if (kh == 0) { sts128(ea, q0); sts128(ea + 1024, q1); }
    else {
      p = mfma32(pf0, q0, zero);
      p = mfma32(pf1, q1, p);
      if (lane < 16) *(LAS u32*)(size_t)(LB + L_KVT + 34 * 64 + lane * 4) = 0u;
    }
    if (w == 0) poll_wait(e2, 16u);
    BAR();
    STAMP(6);
    if (kh == 1) {
      const v4u o0 = lds128(ea), o1 = lds128(ea + 1024);
      p = mfma32(pf0, o0, p);
      p = mfma32(pf1, o1, p);
      hedgehog(p, phq);
    }
  }
  if (kh == 0) {
    PRIO(3);
    v4u sl[8];
    v2u zl = ld64c(rs_ws, lane * 8, WS_Z + (j2 * 4 + w) * 512);
#pragma unroll
    for (int m = 0; m < 8; ++m) sl[m] = ld128c(rs_ws, lane * 16, WS_SLAB + ((j2 * 4 + w) * 8 + m) * 1024);
    WF_GET(0, 0, 4); WF_GET(1, 0, 4);
    asm volatile("s_waitcnt vmcnt(16)" : "+v"(zl)::"memory");
    const int hd = w >> 1, fh = w & 1;
    {
      auto r = __builtin_amdgcn_permlane32_swap(zl.x, zl.y, false, false);
      float z0 = bflo(r[0]) + bflo(r[1]), z1 = bfhi(r[0]) + bfhi(r[1]);
      z0 = sum16(sum8(sum4(z0))); z1 = sum16(sum8(sum4(z1)));
      *(LAS u32*)(size_t)(LB + L_KVT + (hd * 17 + 16) * 64 + (16 * fh + 4 * tp + 2 * hh) * 2) = pk2(z0, z1);
    }
    float sa[8] = {0.f, 0.f, 0.f, 0.f, 0.f, 0.f, 0.f, 0.f};
    sfor<0, 8>([&](auto ic) AI {
      constexpr int m = ic.v;
      asm volatile("s_waitcnt vmcnt(%1)" : "+v"(sl[m]) : "i"(15 - m) : "memory");
#pragma unroll
      for (int e = 0; e < 4; ++e) { sa[2 * e] += bflo(sl[m][e]); sa[2 * e + 1] += bfhi(sl[m][e]); }
    });
#pragma unroll
    for (int e = 0; e < 4; ++e) {
      auto r = __builtin_amdgcn_permlane32_swap(asu(sa[e]), asu(sa[e + 4]), false, false);
      sa[e] = asf(r[0]) + asf(r[1]);
    }
    sts64(LB + L_KVT + (hd * 17 + 2 * (lane & 7) + hh) * 64 + (16 * fh + 4 * (c32 >> 3)) * 2, v2u{pk2(sa[0], sa[1]), pk2(sa[2], sa[3])});
    PRIO(0);
  }
  u32 ka, za;
  {
    const bool own = ((lane >> 2) & 1) == hh;
    const u32 d = (c32 & 3) + 4 * (c32 >> 3);
    ka = LB + L_KVT + (own ? hh * 17 + d : 34) * 64; za = LB + L_KVT + (own ? hh * 17 + 16 : 34) * 64;
    PIN(ka); PIN(za);
  }
  BAR();
  STAMP(7);
  if (kh == 1) {
    PRIO(3);
    f32x16 num = zero, den = zero;
    v4u a[4], z[4];
#pragma unroll
    for (int s = 0; s < 4; ++s) z[s] = lds128(za + s * 16);
#pragma unroll
    for (int s = 0; s < 4; ++s) a[s] = lds128(ka + s * 16);
    SB();
#pragma unroll
    for (int s = 0; s < 4; ++s) den = mfma32(z[s], v4u{phq[4 * s], phq[4 * s + 1], phq[4 * s + 2], phq[4 * s + 3]}, den);
    SB();
#pragma unroll
    for (int s = 0; s < 4; ++s) num = mfma32(a[s], v4u{phq[4 * s], phq[4 * s + 1], phq[4 * s + 2], phq[4 * s + 3]}, num);
    const float r = __builtin_amdgcn_rcpf(den[0] + 1e-6f);
    f32x16 o;
#pragma unroll
    for (int i = 0; i < 16; ++i) o[i] = num[i] * r;
    const u32 vo = (c32 >> 4) * 16384 + hh * 512 + l15 * 16;
    const u32 so = WS_O + ((8 * m2 + 2 * tg) * 16 + j2) * 1024;
    st128c(pack8(o, 0), rs_ws, vo, so);
    st128c(pack8(o, 1), rs_ws, vo + 256, so);
    WF_GET(0, 0, 8);
    WAITV(8);
    PRIO(0);
  }
  RBAR();
  SIGNAL(1, 1 * 16 + m2);
  STAMP(8);
  if (kh == 1) {
    v4u gr[5];
    for (u32 spins = 0;; ++spins) {
#pragma unroll
      for (int n = 0; n < 4; ++n) gr[n] = ld128c(rs_ws, lane * 16, WS_GRAN + (n & 1) * 24576 + (12 + tg * 2 + (n >> 1)) * 1024);
      gr[4] = ld128c(rs_ws, c32 * 16 + hh * 24576, WS_GRAN + ((tg & 1) ? 40 + cb : 16 + cb) * 512);
      asm volatile("s_waitcnt vmcnt(0)" : "+v"(gr[0]), "+v"(gr[1]), "+v"(gr[2]), "+v"(gr[3]), "+v"(gr[4])::"memory");
      bool ok = true;
#pragma unroll
      for (int n = 0; n < 5; ++n) ok = ok && gr[n].y == tag && gr[n].w == tag;
      if (__all(ok) || spins >= SPIN_CAP) break;
    }
    const float one = tg >= 2 ? 1.0f : 0.0f;
    const u32 vperm = g4 * 64 + ((lane >> 1) & 3) * 16 + ((lane >> 3) & 1) * 8 + (lane & 1) * 4;
#pragma unroll
    for (int n = 0; n < 2; ++n) {
      const u32 p = pk2(asf(gr[2 * n].x) + asf(gr[2 * n + 1].x), asf(gr[2 * n].z) + asf(gr[2 * n + 1].z));
      *(LAS u32*)(size_t)(LB + L_VEC + 1024 + (tg * 2 + n) * 256 + vperm) = pk2(bflo(p) + one, bfhi(p) + one);
    }
    *(LAS u32*)(size_t)(LB + L_VEC + (tg & 1) * 128 + (vperm & 127)) = pk2(sum32(asf(gr[4].x)), sum32(asf(gr[4].z)));
  } else {
    WF_GET(0, 4, 8);
    WAITV(4);
    if (w == 0) POLL(1 * 16 + pnl, bi & 3, 16u);
  }
  u32 wra[4], wo_a[4];
#pragma unroll
  for (int c = 0; c < 4; ++c) {
    wra[c] = LB + (8 * g4 + tq) * 128 + tp * 8 + ((c ^ (2 * kh) ^ (((tq >> 1) & 1) | ((g4 & 1) << 1))) << 5);
    wo_a[c] = wra[c] + kh * 65536;
    PIN(wo_a[c]);
  }
  BAR();
  STAMP(9);
  f32x4 cs0 = {0.f, 0.f, 0.f, 0.f}, cs1 = {0.f, 0.f, 0.f, 0.f};
  u32 cva = LB + L_VEC + 2048 - (l15 >> 3) * 1024 + g4 * 16;
  u32 x1p[4];
  {
    v4u of[8];
#pragma unroll
    for (int i = 0; i < 8; ++i) of[i] = ld128c(rs_ws, lane * 16, WS_O + ((8 * pnl + 4 * rh + tg) * 16 + 8 * kh + i) * 1024);
#define WO_FRAG(i, c) cat(trd(wo_a[c] + ((i) >> 1) * 16384 + ((i) & 1) * 4096), trd(wo_a[c] + ((i) >> 1) * 16384 + ((i) & 1) * 4096 + 512))
    const u32 va = LB + L_VEC + kh * 64 + g4 * 16;
    const v4u ga4 = lds128(va), cb4 = lds128(va + 2048 + cb * 128);
    v4u fa[4][4];
#pragma unroll
    for (int i = 0; i < 4; ++i)
#pragma unroll
      for (int c = 0; c < 4; ++c) fa[i][c] = WO_FRAG(i, c);
    PIN(xres);
    auto r0 = __builtin_amdgcn_permlane16_swap(xres.x, xres.z, false, false);
    auto r1 = __builtin_amdgcn_permlane16_swap(xres.y, xres.w, false, false);
    f32x2 xv[4] = {unp(r0[0]), unp(r1[0]), unp(r0[1]), unp(r1[1])};
    f32x2 gv[4] = {unp(ga4.x), unp(ga4.y), unp(ga4.z), unp(ga4.w)};
    f32x2 cv[4] = {unp(cb4.x), unp(cb4.y), unp(cb4.z), unp(cb4.w)};
#pragma unroll
    for (int e = 0; e < 4; ++e) { PIN(xv[e]); PIN(gv[e]); PIN(cv[e]); }
    u32 ea = LB + L_EX3 + tg * 4096 + lane * 16 + kh * 2048;
    PIN(ea);
    f32x4 acc[4];
    sfor<0, 8>([&](auto ic) AI {
      constexpr int i = ic.v;
      waitv<7 - i>();
      pin4(of[i]);
      SB();
#pragma unroll
      for (int c = 3; c >= 0; --c) {
        if constexpr (i == 0) mfma16_z(acc[c], fa[0][c], of[0]);
        else mfma16_ip(acc[c], fa[i & 3][c], of[i]);
        if constexpr (i + 4 < 8) fa[i & 3][c] = WO_FRAG(i + 4, c);
        SB();
      }
    });
    STAMP(10);
    asm volatile("s_nop 4" ::: "memory");
    SB();
    sts128(ea + 1024, __builtin_bit_cast(v4u, acc[3]));
    sts128(ea, __builtin_bit_cast(v4u, acc[2]));
    WF_GET(0, 8, 12);
    BAR();
    const f32x4 t0 = acc[0] + __builtin_bit_cast(f32x4, lds128(ea ^ 2048));
    const f32x4 t1 = acc[1] + __builtin_bit_cast(f32x4, lds128((ea ^ 2048) + 1024));
    f32x2 sum = {0.f, 0.f}, sq = {0.f, 0.f};
    v4u y;
#pragma unroll
    for (int e = 0; e < 4; ++e) {
      const f32x2 at = unp(e < 2 ? pk2(t0[2 * e], t0[2 * e + 1]) : pk2(t1[2 * e - 4], t1[2 * e - 3]));
      const f32x2 gp = gv[e] * at, xs = xv[e] + unp(pk2(gp[0], gp[1]));
      x1p[e] = pk2(xs[0], xs[1]);
      const f32x2 x1 = unp(x1p[e]), yy = x1 * cv[e];
      y[e] = pk2(yy[0], yy[1]);
      sum += x1; sq = fma2(x1, x1, sq);
    }
    st128c(y, rs_ws, lane * 16, WS_Y1 + ((8 * pnl + 4 * rh + tg) * 16 + 2 * cb + kh) * 1024);
    const float s1 = sum32(sum16(sum[0] + sum[1])), q1 = sum32(sum16(sq[0] + sq[1]));
    st64c(v2u{asu(s1), asu(q1)}, rs_ws, lane < 16 ? lane * 8 : WS_BYTES, WS_STAT + ((8 * pnl + 4 * rh + tg) * 16 + 2 * cb + kh) * 128);
    WF_GET(0, 12, 16);
    WAITV(4);
  }
  BAR();
  SIGNAL(4, 2 * 16 + pnl);
  STAMP(11);
  const v4u rs_wd = mk_srd(wd, F_ * D_ * 2);
  const u32 dso = tg * 8192 + cb * 128, dld = LB + tg * 1024;
#define WD_ISSUE(i, h) gldsi<(22 * (h) + (i)) * 32768, (2 * wd_sg(i) + (h)) * 4096>(rs_wd, dvo, dso, dld)
#define WD_ISSUE_K(i) gldsi<(i) * 32768, 2 * wd_sg(i) * 4096>(rs_wd, dvo, dso + kh * (22 * 32768), dld + kh * 4096)
  if (p4) {
    auto cs_part = [&](auto lo, auto hi) AI {
      v4u cf[hi.v - lo.v];
#pragma unroll
      for (int ks = lo.v; ks < hi.v; ++ks) { pin4(wf[0][ks]); cf[ks - lo.v] = lds128(cva + ks * 64); }
      SB();
#pragma unroll
      for (int ks = lo.v; ks < hi.v; ++ks) cs0 = mfma16(wf[0][ks], cf[ks - lo.v], cs0);
      pinf(cs0);
    };
    PIN(cva);
    if (kh == 0) { WF_GET(1, 4, 10); }
    gu32* const e4 = ctl + (WS_CNT + (2 * 16 + m2) * CNT_STRIDE + (bi & 3) * 128) / 4;
    if (w == 7) poll_kick<0>(e4);
    cs_part(IC<0>{}, IC<6>{});
    if (w == 7) poll_kick<1>(e4);
    cs_part(IC<6>{}, IC<12>{});
    if (w == 7) { poll_kick<2>(e4); poll_wait(e4, 16u); }
    STAMP(21);
    RBAR();
    STAMP(12);
    u32 ya0 = LB + lane * 16, ya1 = ya0 + 65536;
    PIN(ya0); PIN(ya1);
#define Y_READ(i) lds128(((i) < 64 ? ya0 : ya1) + ((i) & 63) * 1024)
    constexpr int PD = 6, RING = 7;
    const u32 hvo = l15 * 16, hso = WS_HH + 8 * m2 * (43 * 1024);
    if (w >= 4) {
      f32x4 acc0[8];
      v4u st[4], yr[RING];
      const u32 yso = WS_Y1 + (8 * m2 + 2 * tg) * 16384, yld = LB + tg * 2048;
#define Y_ISSUE(n) gldsic<((n) & 1) * 16384 + ((n) >> 1) * 1024, (((n) >> 1) * 8 + ((n) & 1)) * 1024>(rs_ws, lane * 16, yso, yld)
      PRIO(2);
      sfor<0, 12>([&](auto ic) AI { Y_ISSUE(ic.v); });
      WAITV(12);
      cs_part(IC<12>{}, IC<16>{});
      WAITV(8);
      RBAR();
      STAMP(19);
#pragma unroll
      for (int i = 0; i < PD; ++i) yr[i] = Y_READ(i);
      sfor<0, 128>([&](auto ic) AI {
        constexpr int i = ic.v, rd = i + PD;
        if constexpr (rd < 128) {
          if constexpr ((rd & 15) == 0) { waitv<ops_before(i) - (rd == 112 ? 46 : 4 * (rd >> 4) + 4)>(); RBAR(); }
          if constexpr (rd == 64) STAMP(20);
          yr[rd % RING] = Y_READ(rd);
        }
        if constexpr (i < 20) Y_ISSUE(12 + i);
        if constexpr (i >= 20 && i < 24) st[i - 20] = ld128c(rs_ws, lane * 16, WS_STAT + (8 * m2 + 2 * tg) * 2048 + (i - 20) * 1024);
        if constexpr (wd_slot(i) >= 0) {
          static_assert((wd_slot(i) >> 1) - 3 <= 2 * (rd >> 4) - 2, "slot still holds activations");
          WD_ISSUE(wd_slot(i) >> 1, wd_slot(i) & 1);
        }
        if constexpr (i == 76) {
          waitv<ops_before(i) - 36>();
          pin4(st[0]); pin4(st[1]); pin4(st[2]); pin4(st[3]);
          const f32x4 t0 = __builtin_bit_cast(f32x4, st[0]) + __builtin_bit_cast(f32x4, st[1]);
          const f32x4 t1 = __builtin_bit_cast(f32x4, st[2]) + __builtin_bit_cast(f32x4, st[3]);
          float t[4], q[2];
#pragma unroll
          for (int e = 0; e < 4; ++e) {
            auto r = __builtin_amdgcn_permlane32_swap(asu(t0[e]), asu(t1[e]), false, false);
            t[e] = asf(r[0]) + asf(r[1]);
          }
#pragma unroll
          for (int e = 0; e < 2; ++e) {
            auto r = __builtin_amdgcn_permlane16_swap(asu(t[e]), asu(t[e + 2]), false, false);
            q[e] = sum8(asf(r[0]) + asf(r[1]));
          }
          float m0, d0;
          ln_stats(q[0], q[1], m0, d0);
          sts64(LB + L_STAT2 + (2 * tg + hh) * 128 + (lane & 7) * 16 + (g4 & 1) * 8, v2u{asu(-m0), asu(__builtin_amdgcn_rcpf(d0))});
        }
        if constexpr ((i & 1) == 0) pin4(yr[(i + 1) % RING]);
        SB();
        if constexpr (i < 8) mfma16_z(acc0[i], wf[0][0], yr[i % RING]);
        else mfma16_ip(acc0[i & 7], wf[0][i >> 3], yr[i % RING]);
        SB();
      });
      SB();
      asm volatile("s_nop 7" ::: "memory");
      SB();
      PRIO(0);
      STAMP(13);
      {
        v2u sv[8];
#pragma unroll
        for (int t = 0; t < 8; ++t) sv[t] = lds64(LB + L_STAT2 + t * 128 + l15 * 8);
        const f32x2 cwa = {bcast<0>(cs0[0]), bcast<0>(cs0[1])}, cwb = {bcast<0>(cs0[2]), bcast<0>(cs0[3])};
        const f32x2 swa = {bcast<8>(cs0[0]), bcast<8>(cs0[1])}, swb = {bcast<8>(cs0[2]), bcast<8>(cs0[3])};
        f32x2 va[8], vb[8];
#pragma unroll
        for (int t = 0; t < 8; ++t) {
          const f32x2 m = {asf(sv[t].x), asf(sv[t].x)}, d = {asf(sv[t].y), asf(sv[t].y)};
          va[t] = fma2(fma2(m, cwa, f32x2{acc0[t][0], acc0[t][1]}), d, swa);
          vb[t] = fma2(fma2(m, cwb, f32x2{acc0[t][2], acc0[t][3]}), d, swb);
        }
        u32 hp[4][2];
#pragma unroll
        for (int t2 = 0; t2 < 4; ++t2) {
          auto r0 = __builtin_amdgcn_permlane32_swap(asu(va[2 * t2][0]), asu(va[2 * t2 + 1][0]), false, false);
          auto r1 = __builtin_amdgcn_permlane32_swap(asu(va[2 * t2][1]), asu(va[2 * t2 + 1][1]), false, false);
          auto r2 = __builtin_amdgcn_permlane32_swap(asu(vb[2 * t2][0]), asu(vb[2 * t2 + 1][0]), false, false);
          auto r3 = __builtin_amdgcn_permlane32_swap(asu(vb[2 * t2][1]), asu(vb[2 * t2 + 1][1]), false, false);
          hp[t2][0] = pk2(asf(r0[0]) * asf(r0[1]), asf(r1[0]) * asf(r1[1]));
          hp[t2][1] = pk2(asf(r2[0]) * asf(r2[1]), asf(r3[0]) * asf(r3[1]));
        }
        const int ug = 12 * j2 + un0;
        const u32 so = ug < 172 ? hso + ug * 256 : WS_BYTES;
#pragma unroll
        for (int qd = 0; qd < 2; ++qd) {
          auto r0 = __builtin_amdgcn_permlane16_swap(hp[2 * qd][0], hp[2 * qd + 1][0], false, false);
          auto r1 = __builtin_amdgcn_permlane16_swap(hp[2 * qd][1], hp[2 * qd + 1][1], false, false);
          st128c(v4u{r0[0], r1[0], r0[1], r1[1]}, rs_ws, hvo + (2 * (g4 & 1) + (g4 >> 1)) * (43 * 1024), so + qd * (4 * 43 * 1024));
        }
      }
#pragma unroll
      for (int ks = 0; ks < 16; ++ks) { asm volatile("" : "=v"(wf[0][ks])); asm volatile("" : "=v"(wf[1][ks])); }
      asm volatile("" : "=v"(cs0), "=v"(cs1));
    }
    int w2 = w;
    asm volatile("" : "+s"(w2));
    if (w2 < 4) {
      f32x4 acc0[8], acc1[8];
      v4u yr[RING], cfr;
      WAITV(6);
      cs_part(IC<12>{}, IC<16>{});
      WAITV(0);
#pragma unroll
      for (int ks = 0; ks < 10; ++ks) PIN(wf[1][ks]);
      RBAR();
      STAMP(19);
#pragma unroll
      for (int i = 0; i < PD; ++i) yr[i] = Y_READ(i);
      sfor<0, 128>([&](auto ic) AI {
        constexpr int i = ic.v, rd = i + PD;
        if constexpr (i == 80) { WAITV(0); _Pragma("unroll") for (int ks = 10; ks < 16; ++ks) pin4(wf[1][ks]); }
        if constexpr ((i & 1) == 0) pin4(yr[(i + 1) % RING]);
        SB();
        if constexpr (i < 8) mfma16_z(acc0[i], wf[0][0], yr[i % RING]);
        else mfma16_ip(acc0[i & 7], wf[0][i >> 3], yr[i % RING]);
        SB();
        if constexpr (rd < 128) {
          if constexpr ((rd & 15) == 0) RBAR();
          if constexpr (rd == 64) STAMP(20);
          yr[rd % RING] = Y_READ(rd);
        }
        SB();
        if constexpr (i < 8) mfma16_z(acc1[i], wf[1][0], yr[i % RING]);
        else mfma16_ip(acc1[i & 7], wf[1][i >> 3], yr[i % RING]);
        SB();
        if constexpr ((i & 7) == 0) cfr = lds128(cva + (i >> 3) * 64);
        if constexpr ((i & 7) == 6) mfma16_ip(cs1, wf[1][i >> 3], cfr);
        if constexpr (i >= 36 && i < 60 && (i & 3) == 2) { WF_GET(1, 10 + ((i - 36) >> 2), 11 + ((i - 36) >> 2)); }
        SB();
      });
      SB();
      asm volatile("s_nop 7" ::: "memory");
      SB();
      STAMP(13);
      {
        v2u sv[8];
#pragma unroll
        for (int t = 0; t < 8; ++t) sv[t] = lds64(LB + L_STAT2 + t * 128 + l15 * 8);
        const f32x2 cga = {bcast<0>(cs0[0]), bcast<0>(cs0[1])}, cgb = {bcast<0>(cs0[2]), bcast<0>(cs0[3])};
        const f32x2 sga = {bcast<8>(cs0[0]), bcast<8>(cs0[1])}, sgb = {bcast<8>(cs0[2]), bcast<8>(cs0[3])};
        const f32x2 cua = {bcast<0>(cs1[0]), bcast<0>(cs1[1])}, cub = {bcast<0>(cs1[2]), bcast<0>(cs1[3])};
        const f32x2 sua = {bcast<8>(cs1[0]), bcast<8>(cs1[1])}, sub = {bcast<8>(cs1[2]), bcast<8>(cs1[3])};
        const int ug = 12 * j2 + un0;
        const u32 so = ug < 172 ? hso + ug * 256 : WS_BYTES;
        const u32 vo = hvo + (g4 & 1) * (43 * 1024) + (g4 >> 1) * 256;
#pragma unroll
        for (int t2 = 0; t2 < 4; ++t2) {
          u32 hp[2][2];
#pragma unroll
          for (int k = 0; k < 2; ++k) {
            const int t = 2 * t2 + k;
            const f32x2 m = {asf(sv[t].x), asf(sv[t].x)}, d = {asf(sv[t].y), asf(sv[t].y)};
            const f32x2 pa = fma2(fma2(m, cga, f32x2{acc0[t][0], acc0[t][1]}), d, sga) * fma2(fma2(m, cua, f32x2{acc1[t][0], acc1[t][1]}), d, sua);
            const f32x2 pb = fma2(fma2(m, cgb, f32x2{acc0[t][2], acc0[t][3]}), d, sgb) * fma2(fma2(m, cub, f32x2{acc1[t][2], acc1[t][3]}), d, sub);
            hp[k][0] = pk2(pa[0], pa[1]); hp[k][1] = pk2(pb[0], pb[1]);
          }
          auto r0 = __builtin_amdgcn_permlane16_swap(hp[0][0], hp[1][0], false, false);
          auto r1 = __builtin_amdgcn_permlane16_swap(hp[0][1], hp[1][1], false, false);
          st128c(v4u{r0[0], r1[0], r0[1], r1[1]}, rs_ws, vo, so + t2 * (2 * 43 * 1024));
        }
      }
    }
  } else {
    sfor<0, 16>([&](auto ic) AI { WD_ISSUE_K(ic.v); });
    WAITV(0);
    RBAR();
  }
  {
    u32 da0[4], da1[4], da2[4];
#pragma unroll
    for (int c = 0; c < 4; ++c) { da0[c] = wra[c] + kh * 4096; da1[c] = da0[c] + 65536; da2[c] = da0[c] + 131072; PIN(da0[c]); PIN(da1[c]); PIN(da2[c]); }
#define D_ADDR(i, c) (wd_sg(i) < 8 ? da0[c] + wd_sg(i) * 8192 : (wd_sg(i) < 16 ? da1[c] + (wd_sg(i) - 8) * 8192 : da2[c] + (wd_sg(i) - 16) * 8192))
#define D_READ(i, c) cat(trd(D_ADDR(i, c)), trd(D_ADDR(i, c) + 512))
    v4u fa[5][4];
#pragma unroll
    for (int i = 0; i < 5; ++i)
#pragma unroll
      for (int c = 0; c < 4; ++c) fa[i][c] = D_READ(i, c);
    const v4u gb4 = lds128(LB + L_VEC + 128 + kh * 64 + g4 * 16);
    WAITV(0);
    BAR();
    if (p4) SIGNAL(1, 3 * 16 + m2);
    STAMP(14);
    if (w == 0) POLL(3 * 16 + pnl, bi & 3, 15u);
    sfor<16, 22>([&](auto ic) AI { WD_ISSUE_K(ic.v); });
    RBAR();
    STAMP(15);
    constexpr int HA = 8;
    v4u hf2[HA + 1];
    const u32 hso = WS_HH + (8 * pnl + 4 * rh + tg) * (43 * 1024) + kh * (22 * 1024);
#define H_GET(i) hf2[(i) % (HA + 1)] = ld128c(rs_ws, lane * 16, (i) < 21 ? hso + (i) * 1024 : hso + (21 - kh) * 1024)
#pragma unroll
    for (int i = 0; i < HA; ++i) H_GET(i);
    f32x2 gv[4] = {unp(gb4.x), unp(gb4.y), unp(gb4.z), unp(gb4.w)}, xv[4] = {unp(x1p[0]), unp(x1p[1]), unp(x1p[2]), unp(x1p[3])};
#pragma unroll
    for (int e = 0; e < 4; ++e) { PIN(gv[e]); PIN(xv[e]); }
    u32 ea = LB + tg * 4096 + lane * 16 + kh * 2048;
    PIN(ea);
    f32x4 acc[4];
    sfor<0, 22>([&](auto ic) AI {
      constexpr int i = ic.v;
      if constexpr (i == 3 || i == 8 || i == 13) RBAR();
      if constexpr (i + HA < 22) H_GET(i + HA);
      waitv<(i + HA < 22 ? HA : 21 - i)>();
      pin4(hf2[i % (HA + 1)]);
      SB();
#pragma unroll
      for (int c = 3; c >= 0; --c) {
        if constexpr (i == 0) mfma16_z(acc[c], fa[0][c], hf2[0]);
        else mfma16_ip(acc[c], fa[i % 5][c], hf2[i % (HA + 1)]);
        if constexpr (i + 5 < 22) fa[i % 5][c] = D_READ(i + 5, c);
        SB();
      }
    });
    STAMP(16);
    asm volatile("s_nop 4" ::: "memory");
    SB();
    sts128(ea + 1024, __builtin_bit_cast(v4u, acc[3]));
    sts128(ea, __builtin_bit_cast(v4u, acc[2]));
    BAR();
    const f32x4 t0 = acc[0] + __builtin_bit_cast(f32x4, lds128(ea ^ 2048));
    const f32x4 t1 = acc[1] + __builtin_bit_cast(f32x4, lds128((ea ^ 2048) + 1024));
    u32 op[4];
#pragma unroll
    for (int e = 0; e < 4; ++e) {
      const f32x2 gp = gv[e] * unp(e < 2 ? pk2(t0[2 * e], t0[2 * e + 1]) : pk2(t1[2 * e - 4], t1[2 * e - 3]));
      const f32x2 o2 = xv[e] + unp(pk2(gp[0], gp[1]));
      op[e] = pk2(o2[0], o2[1]);
    }
    auto r0 = __builtin_amdgcn_permlane16_swap(op[0], op[2], false, false);
    auto r1 = __builtin_amdgcn_permlane16_swap(op[1], op[3], false, false);
    *(GAS v4u*)((GAS char*)out + orow) = v4u{r0[0], r1[0], r0[1], r1[1]};
  }
  if (b == 0 && tid == 0) __hip_atomic_store(ctl + WS_EPOCH / 4, tag, RLX_AGENT);
  { const u32 te = (u32)__builtin_amdgcn_s_memrealtime(); const u32 d = 8u * ((u32)lk1 - (u32)lk0); while ((u32)__builtin_amdgcn_s_memrealtime() - te < d) __builtin_amdgcn_s_sleep(1); }
}

extern "C" void kernel_launch(void* const* d_in, const int* in_sizes, int n_in, void* d_out, int out_size, void* d_ws, size_t ws_size,
                              hipStream_t stream) {
  (void)in_sizes; (void)n_in; (void)out_size; (void)ws_size;
  (void)hipFuncSetAttribute((const void*)fused_block, hipFuncAttributeMaxDynamicSharedMemorySize, L_TOTAL);
  fused_block<<<256, 512, L_TOTAL, stream>>>((const u16*)d_in[0], (const u16*)d_in[1], (const u16*)d_in[2], (const u16*)d_in[3],
                                             (const u16*)d_in[5], (const u16*)d_in[6], (const u16*)d_in[7], (const u16*)d_in[8],
                                             (const u16*)d_in[9], (u16*)d_out, (char*)d_ws);
}
```

```cpp
#include <hip/hip_runtime.h>

typedef unsigned short u16;
typedef unsigned int u32;
typedef u32 v4u __attribute__((ext_vector_type(4)));
typedef u32 v2u __attribute__((ext_vector_type(2)));
typedef float f32x2 __attribute__((ext_vector_type(2)));
typedef float f32x4 __attribute__((ext_vector_type(4)));
typedef float f32x16 __attribute__((ext_vector_type(16)));
typedef short bf16x8 __attribute__((ext_vector_type(8)));
typedef short s16x4 __attribute__((ext_vector_type(4)));
typedef __bf16 bf16x2 __attribute__((ext_vector_type(2)));
#define GAS __attribute__((address_space(1)))
#define LAS __attribute__((address_space(3)))
#define DI __device__ __forceinline__
#define RLX_AGENT __ATOMIC_RELAXED, __HIP_MEMORY_SCOPE_AGENT
typedef GAS u32 gu32;

constexpr int S_ = 2048, D_ = 512, F_ = 1376;
constexpr int WS_EPOCH = 0, WS_CNT = 983040, CNT_STRIDE = 512;
constexpr int WS_GRAN = 32768, WS_ROWST = 98304, WS_SLAB = 131072, WS_Z = 655360, WS_STAT = 720896;
constexpr int WS_O = 1 << 20, WS_Y1 = 3 << 20, WS_HH = 5 << 20, WS_GT = 11 << 20, WS_BYTES = 16 << 20;
constexpr int L_B = 65536, L_T = 131072, L_MISC = 155648, L_TOTAL = 163840;
constexpr int L_CS = L_MISC, L_VEC = L_MISC, L_SILU = L_MISC + 4608, L_STAT2 = L_MISC + 4096;
constexpr int L_WPHI = L_MISC + 7168;
constexpr int L_KVT = 139264, L_QEX = 147456, L_EX3 = 139264;
constexpr unsigned CODE_SLICES = 14;
constexpr unsigned SPIN_CAP = 1u << 18;

DI float asf(u32 v) { return __builtin_bit_cast(float, v); }
DI u32 asu(float v) { return __builtin_bit_cast(u32, v); }
DI u32 asu(bf16x2 v) { return __builtin_bit_cast(u32, v); }
DI float bflo(u32 p) { return asf(p << 16); }
DI float bfhi(u32 p) { return asf(p & 0xffff0000u); }
DI u32 pk2(float a, float b) { f32x2 v = {a, b}; return asu(__builtin_convertvector(v, bf16x2)); }
DI float rb(float a) { return bflo(pk2(a, a) & 0xffffu); }
DI v4u mk_srd(const void* p, u32 bytes) {
  unsigned long long a = (unsigned long long)p;
  return v4u{(u32)a, (u32)(a >> 32) & 0xffffu, bytes, 0x00020000u};
}
DI void glds(v4u rs, u32 voff, u32 soff, u32 ldsaddr) {
  asm volatile("s_mov_b32 m0, %3\n\ts_nop 0\n\tbuffer_load_dwordx4 %0, %1, %2 offen lds" ::"v"(voff), "s"(rs), "s"(soff), "s"(ldsaddr) : "memory");
}
template <int SO, int LO> DI void gldsi(v4u rs, u32 voff, u32 soff, u32 ldsaddr) {
  u32 t;
  asm volatile("s_add_u32 m0, %4, %6\n\ts_add_u32 %0, %3, %5\n\tbuffer_load_dwordx4 %1, %2, %0 offen lds"
               : "=&s"(t) : "v"(voff), "s"(rs), "s"(soff), "s"(ldsaddr), "i"(SO), "i"(LO) : "memory", "scc");
}
template <int SO, int LO> DI void gldsic(v4u rs, u32 voff, u32 soff, u32 ldsaddr) {
  u32 t;
  asm volatile("s_add_u32 m0, %4, %6\n\ts_add_u32 %0, %3, %5\n\tbuffer_load_dwordx4 %1, %2, %0 offen sc1 lds"
               : "=&s"(t) : "v"(voff), "s"(rs), "s"(soff), "s"(ldsaddr), "i"(SO), "i"(LO) : "memory", "scc");
}
DI v2u ld64(v4u rs, u32 voff, u32 soff) {
  v2u r;
  asm volatile("buffer_load_dwordx2 %0, %1, %2, %3 offen" : "=v"(r) : "v"(voff), "s"(rs), "s"(soff) : "memory");
  return r;
}
DI v4u ld128(v4u rs, u32 voff, u32 soff) {
  v4u r;
  asm volatile("buffer_load_dwordx4 %0, %1, %2, %3 offen" : "=v"(r) : "v"(voff), "s"(rs), "s"(soff) : "memory");
  return r;
}
DI v4u ld128c(v4u rs, u32 voff, u32 soff) {
  v4u r;
  asm volatile("buffer_load_dwordx4 %0, %1, %2, %3 offen sc1" : "=v"(r) : "v"(voff), "s"(rs), "s"(soff) : "memory");
  return r;
}
DI v2u ld64c(v4u rs, u32 voff, u32 soff) {
  v2u r;
  asm volatile("buffer_load_dwordx2 %0, %1, %2, %3 offen sc1" : "=v"(r) : "v"(voff), "s"(rs), "s"(soff) : "memory");
  return r;
}
DI void st128c(v4u v, v4u rs, u32 voff, u32 soff) {
  asm volatile("buffer_store_dwordx4 %0, %1, %2, %3 offen sc1\n\ts_nop 1" ::"v"(v), "v"(voff), "s"(rs), "s"(soff) : "memory");
}
DI void st64c(v2u v, v4u rs, u32 voff, u32 soff) {
  asm volatile("buffer_store_dwordx2 %0, %1, %2, %3 offen sc1" ::"v"(v), "v"(voff), "s"(rs), "s"(soff) : "memory");
}
#define WAITV(n) asm volatile("s_waitcnt vmcnt(" #n ")" ::: "memory")
#define BAR() asm volatile("s_waitcnt lgkmcnt(0)\n\ts_barrier" ::: "memory")
#define PIN(x) asm volatile("" : "+v"(x))
#define SB() __builtin_amdgcn_sched_barrier(0)
#define RBAR() asm volatile("s_barrier" ::: "memory")
template <int N> DI void waitv() { asm volatile("s_waitcnt vmcnt(%0)" ::"i"(N) : "memory"); }

DI v4u lds128(u32 a) { return *(const LAS v4u*)(size_t)a; }
DI v2u lds64(u32 a) { return *(const LAS v2u*)(size_t)a; }
DI void sts128(u32 a, v4u v) { *(LAS v4u*)(size_t)a = v; }
DI void sts64(u32 a, v2u v) { *(LAS v2u*)(size_t)a = v; }
DI v2u trd(u32 a) { return __builtin_bit_cast(v2u, __builtin_amdgcn_ds_read_tr16_b64_v4i16((LAS s16x4*)(size_t)a)); }
DI v4u cat(v2u a, v2u b) { return v4u{a.x, a.y, b.x, b.y}; }
DI f32x16 mfma32(v4u a, v4u b, f32x16 c) {
  return __builtin_amdgcn_mfma_f32_32x32x16_bf16(__builtin_bit_cast(bf16x8, a), __builtin_bit_cast(bf16x8, b), c, 0, 0, 0);
}
DI f32x4 mfma16(v4u a, v4u b, f32x4 c) {
  return __builtin_amdgcn_mfma_f32_16x16x32_bf16(__builtin_bit_cast(bf16x8, a), __builtin_bit_cast(bf16x8, b), c, 0, 0, 0);
}
DI float sum32(float v) {
  u32 a = asu(v), c = a;
  auto r = __builtin_amdgcn_permlane32_swap(a, c, false, false);
  return asf(r[0]) + asf(r[1]);
}
DI float sum16(float v) {
  u32 a = asu(v), c = a;
  auto r = __builtin_amdgcn_permlane16_swap(a, c, false, false);
  return asf(r[0]) + asf(r[1]);
}
DI float sum8(float v) {
  return v + asf(__builtin_amdgcn_update_dpp(0, __builtin_bit_cast(int, v), 0x128, 0xf, 0xf, false));
}
DI float sum4(float v) {
  return v + asf(__builtin_amdgcn_update_dpp(0, __builtin_bit_cast(int, v), 0x124, 0xf, 0xf, false));
}
DI void ln_stats(float s, float q, float& mean, float& sd) {
  float m = s * (1.0f / D_);
  PIN(m);
  float var = q * (1.0f / D_) - m * m;
  var = fmaxf(var, 0.0f);
  mean = rb(m);
  sd = rb(__builtin_amdgcn_sqrtf(rb(rb(var) + 1e-5f)));
}
template <int I, int NW> DI void poll_kick(gu32* line) {
  unsigned long long ex;
  asm volatile(
      "v_mbcnt_lo_u32_b32 v251, -1, 0\n\tv_mov_b32 v255, 0\n\tv_lshlrev_b32 v251, 2, v251\n\t"
      "s_mov_b64 %0, exec\n\ts_mov_b64 exec, %3\n\tglobal_atomic_add v%c2, v251, v255, %1 sc0\n\ts_mov_b64 exec, %0"
      : "=&s"(ex) : "s"(line), "i"(252 + I), "i"((1 << NW) - 1) : "memory", "v251", "v252", "v253", "v254", "v255");
}
template <int NW> DI void poll_wait(gu32* line, u32 tag) {
  unsigned long long ex;
  u32 n;
  asm volatile(
      "s_mov_b64 %0, exec\n\ts_mov_b64 exec, %5\n\ts_mov_b32 %1, %4\n"
      "1:\n\t"
      "s_waitcnt vmcnt(2)\n\tv_cmp_ne_u32 vcc, %3, v252\n\ts_nop 4\n\ts_cbranch_vccz 2f\n\t"
      "global_atomic_add v252, v251, v255, %2 sc0\n\t"
      "s_waitcnt vmcnt(2)\n\tv_cmp_ne_u32 vcc, %3, v253\n\ts_nop 4\n\ts_cbranch_vccz 2f\n\t"
      "global_atomic_add v253, v251, v255, %2 sc0\n\t"
      "s_waitcnt vmcnt(2)\n\tv_cmp_ne_u32 vcc, %3, v254\n\ts_nop 4\n\ts_cbranch_vccz 2f\n\t"
      "global_atomic_add v254, v251, v255, %2 sc0\n\t"
      "s_sub_u32 %1, %1, 1\n\ts_cmp_lg_u32 %1, 0\n\ts_cbranch_scc1 1b\n"
      "2:\n\t"
      "s_mov_b64 exec, %0"
      : "=&s"(ex), "=&s"(n)
      : "s"(line), "s"(tag), "i"(SPIN_CAP), "i"((1 << NW) - 1)
      : "memory", "scc", "vcc", "v251", "v252", "v253", "v254", "v255");
}
template <int NW> DI void poll_line(gu32* line, u32 tag) {
  poll_kick<0, NW>(line); __builtin_amdgcn_s_sleep(2);
  poll_kick<1, NW>(line); __builtin_amdgcn_s_sleep(2);
  poll_kick<2, NW>(line);
  poll_wait<NW>(line, tag);
}
DI f32x2 dup(float v) { f32x2 r = {v, v}; PIN(r); return r; }
DI float hadd(f32x2 v) { float x = v[0], y = v[1]; PIN(x); PIN(y); return x + y; }
DI float max16(const f32x16& p) {
  const float a = fmaxf(fmaxf(p[0], p[1]), p[2]), b = fmaxf(fmaxf(p[3], p[4]), p[5]), c = fmaxf(fmaxf(p[6], p[7]), p[8]);
  const float d = fmaxf(fmaxf(p[9], p[10]), p[11]), e = fmaxf(fmaxf(p[12], p[13]), p[14]);
  return fmaxf(fmaxf(fmaxf(a, b), c), fmaxf(fmaxf(d, e), p[15]));
}
DI float min16(const f32x16& p) {
  const float a = fminf(fminf(p[0], p[1]), p[2]), b = fminf(fminf(p[3], p[4]), p[5]), c = fminf(fminf(p[6], p[7]), p[8]);
  const float d = fminf(fminf(p[9], p[10]), p[11]), e = fminf(fminf(p[12], p[13]), p[14]);
  return fminf(fminf(fminf(a, b), c), fminf(fminf(d, e), p[15]));
}
DI void expnorm(const f32x16& p, float l, float a, u32* ph) {
  f32x2 e[8];
  const f32x2 l2 = dup(l), a2 = dup(a);
#pragma unroll
  for (int i = 0; i < 8; ++i) {
    const f32x2 t = __builtin_elementwise_fma(f32x2{p[2 * i], p[2 * i + 1]}, l2, a2);
    e[i] = f32x2{__builtin_amdgcn_exp2f(t[0]), __builtin_amdgcn_exp2f(t[1])};
  }
  const f32x2 s = ((e[0] + e[1]) + (e[2] + e[3])) + ((e[4] + e[5]) + (e[6] + e[7]));
  const f32x2 r = dup(__builtin_amdgcn_rcpf(hadd(s)));
#pragma unroll
  for (int i = 0; i < 8; ++i) { const f32x2 t = e[i] * r; ph[i] = pk2(t[0], t[1]); }
}
constexpr float LOG2E = 1.44269504088896341f;
DI void softmax16(const f32x16& p, u32 (&ph)[8]) { expnorm(p, LOG2E, -max16(p) * LOG2E, ph); }
DI void hedgehog(const f32x16& p, u32 (&ph)[16]) {
  expnorm(p, LOG2E, -max16(p) * LOG2E, ph);
  expnorm(p, -LOG2E, min16(p) * LOG2E, ph + 8);
}
DI v4u pack8(const f32x16& x, int s) {
  return v4u{pk2(x[8 * s], x[8 * s + 1]), pk2(x[8 * s + 2], x[8 * s + 3]), pk2(x[8 * s + 4], x[8 * s + 5]), pk2(x[8 * s + 6], x[8 * s + 7])};
}

template <int I> struct IC { static constexpr int v = I; };
template <int I, int N, class Fn> DI void sfor(Fn&& f) {
  if constexpr (I < N) { f(IC<I>{}); sfor<I + 1, N>(f); }
}
#define AI __attribute__((always_inline))
#define PRIO(n) __builtin_amdgcn_s_setprio(n)
constexpr int wd_slot(int i) { return (i >= 24 && i < 120 && (i - 24) % 3 == 0) ? (i - 24) / 3 : -1; }
constexpr int ops_before(int i) { return i <= 24 ? 12 + i : (i >= 118 ? 68 : 36 + (i - 24 + 2) / 3); }
constexpr int wd_sg(int i) { return i < 3 ? 16 + i : i - 3; }
template <int N> DI float bcast(float v) { return asf(__builtin_amdgcn_mov_dpp((int)asu(v), 0x150 + N, 0xf, 0xf, true)); }
DI f32x2 unp(u32 p) { return f32x2{bflo(p), bfhi(p)}; }
DI u32 pk2v(f32x2 v) { return pk2(v[0], v[1]); }
DI f32x2 fma2(f32x2 a, f32x2 b, f32x2 c) { return __builtin_elementwise_fma(a, b, c); }
DI void mfma16_ip(f32x4& c, v4u a, v4u b) { asm volatile("v_mfma_f32_16x16x32_bf16 %0, %1, %2, %0" : "+v"(c) : "v"(a), "v"(b)); }
DI void mfma16_z(f32x4& c, v4u a, v4u b) { asm volatile("v_mfma_f32_16x16x32_bf16 %0, %1, %2, 0" : "=&v"(c) : "v"(a), "v"(b)); }
DI void pinf(f32x4& v) { PIN(v); }
DI void pin4(v4u& v) { PIN(v); }
DI void fma2_ip(f32x2& x, f32x2 a, f32x2 c) { asm volatile("v_pk_fma_f32 %0, %0, %1, %2" : "+v"(x) : "v"(a), "v"(c)); }
template <int N> DI void fmac_bc(float& h, float c, float x) {
  asm("v_fmac_f32_dpp %0, %1, %2 row_newbcast:%3 row_mask:0xf bank_mask:0xf" : "+v"(h) : "v"(c), "v"(x), "i"(N));
}

__global__ void __launch_bounds__(512, 2) __attribute__((amdgpu_num_vgpr(124)))
fused_block(const u16* __restrict__ x, const u16* __restrict__ wqkv, const u16* __restrict__ wphi, const u16* __restrict__ wo,
            const u16* __restrict__ wg, const u16* __restrict__ wu, const u16* __restrict__ wd, const u16* __restrict__ cvec,
            const u16* __restrict__ wmod, u16* __restrict__ out, char* __restrict__ ws) {
  extern __shared__ __attribute__((aligned(16))) char smem[];
  const u32 LB = (u32)(size_t)(LAS char*)smem;
  u32 cpf;
  {
    unsigned long long pc;
    u32 sl = ((blockIdx.x >> 3) % CODE_SLICES) * 2048u, off;
    asm volatile(
        "s_getpc_b64 %2\n.Lfb_code_begin:\n\tv_lshlrev_b32 %1, 7, %3\n\tv_cmp_lt_u32 vcc, 63, %3\n\tv_mov_b32 %0, %4\n\ts_nop 0\n\tv_cndmask_b32 %0, 0, %0, vcc\n\t"
        "v_add_u32 %1, %1, %0\n\tv_cmp_gt_u32 vcc, 0x50, %3\n\ts_nop 1\n\tv_cndmask_b32 %1, 0, %1, vcc\n\t"
        "v_min_u32 %1, .Lfb_code_end-.Lfb_code_begin-4, %1\n\tglobal_load_dword %0, %1, %2"
        : "=&v"(cpf), "=&v"(off), "=&s"(pc) : "v"(threadIdx.x), "s"(sl) : "memory", "vcc");
    SB();
  }
  asm volatile("" ::"s"(x), "s"(wqkv), "s"(wphi), "s"(wo), "s"(wg), "s"(wu), "s"(wd), "s"(cvec), "s"(wmod), "s"(out), "s"(ws));
  const int tid = threadIdx.x, lane = tid & 63;
  int w = __builtin_amdgcn_readfirstlane(tid >> 6);
  asm volatile("s_nop 4" : "+s"(w));
  const int b = blockIdx.x, g = b & 7, bi = b >> 3;
  const int m2 = 4 * (g >> 1) + (bi >> 3), j2 = 8 * (g & 1) + (bi & 7);
  const int pnl = 2 * g + (bi >> 4), rh = (bi >> 3) & 1, cb = bi & 7;
  const v4u rs_x = mk_srd(x, S_ * D_ * 2), rs_ws = mk_srd(ws, WS_BYTES);
  gu32* const ctl = (gu32*)ws;
  const int tg = w & 3, kh = w >> 2;
  const int c32 = lane & 31, hh = lane >> 5;
  const int l15 = lane & 15, g4 = lane >> 4, tq = (lane & 15) >> 2, tp = lane & 3;

  const bool prod = b < 96, sprod = b >= 96 && b < 224;
  const int cg = b >> 1, kp = b & 1;
  const u32 xld = LB + w * 16384;
  const int srow = 256 * g + 16 * (bi - 12) + 4 * w;
  v4u xr[4];
  u32 tagv;
  asm volatile("buffer_load_dword %0, off, %1, 0 sc1" : "=v"(tagv) : "s"(rs_ws) : "memory");
  v2u mc2;
  if (prod && w == 0) {
    const v4u rs_m = mk_srd(wmod, D_ * 6 * D_ * 2), rs_c = mk_srd(cvec, D_ * 2);
    mc2 = ld64(rs_c, lane * 8, kp * 512);
    const u32 vo = (lane >> 3) * 6144 + (((lane & 7) ^ (((lane >> 4) & 1) << 1)) << 4);
    const u32 so = 256 * kp * 6144 + cg * 128;
    sfor<0, 32>([&](auto ic) AI { gldsi<ic.v * 8 * 6144, (ic.v < 24 ? L_T + ic.v * 1024 : (ic.v - 24) * 1024)>(rs_m, (ic.v & 1) ? vo ^ 64 : vo, so, LB); });
  }
  if (kh == 0 && sprod) {
#pragma unroll
    for (int r = 0; r < 4; ++r) xr[r] = ld128(rs_x, lane * 16, (srow + r) * 1024);
  }
  if (w == 5) glds(mk_srd(wphi, 32 * 256 * 2), lane * 16, j2 * 1024, LB + L_WPHI);
  if (prod) {
    if (w == 0) {
      asm volatile("s_waitcnt vmcnt(32)" : "+v"(mc2), "+v"(tagv)::"memory");
      float sv[4] = {bflo(mc2.x), bfhi(mc2.x), bflo(mc2.y), bfhi(mc2.y)};
#pragma unroll
      for (int e = 0; e < 4; ++e) sv[e] = sv[e] * __builtin_amdgcn_rcpf(1.0f + __expf(-sv[e]));
      sts64(LB + L_SILU + lane * 8, v2u{pk2(sv[0], sv[1]), pk2(sv[2], sv[3])});
    }
    RBAR();
  }
  const u32 xvo0 = (lane >> 3) * 1024 + (((lane & 7) ^ (lane >> 4)) << 4), xvo1 = xvo0 ^ 64;
  const int rot = bi & 3;
  const u32 xso = (128 * m2 + 32 * tg) * 1024 + kh * 512 + rot * 128;
#define X_ISSUE(q4)                                                                       \
  do {                                                                                    \
    gldsi<0, (q4) * 4096>(rs_x, xvo0, xso ^ ((q4) * 128), xld);                           \
    gldsi<8192, (q4) * 4096 + 1024>(rs_x, xvo1, xso ^ ((q4) * 128), xld);                 \
    gldsi<16384, (q4) * 4096 + 2048>(rs_x, xvo0, xso ^ ((q4) * 128), xld);                \
    gldsi<24576, (q4) * 4096 + 3072>(rs_x, xvo1, xso ^ ((q4) * 128), xld);                \
  } while (0)
  const bool sw = kh == 0 && sprod;
  if (sw) {
    X_ISSUE(0); X_ISSUE(1);
    asm volatile("s_waitcnt vmcnt(8)" : "+v"(xr[0]), "+v"(xr[1]), "+v"(xr[2]), "+v"(xr[3]), "+v"(tagv)::"memory");
    float s[4], q[4], t[2];
#pragma unroll
    for (int r = 0; r < 4; ++r) {
      s[r] = 0.f; q[r] = 0.f;
#pragma unroll
      for (int e = 0; e < 4; ++e) {
        const float lo = bflo(xr[r][e]), hi = bfhi(xr[r][e]);
        s[r] += lo + hi; q[r] = __builtin_fmaf(lo, lo, __builtin_fmaf(hi, hi, q[r]));
      }
    }
#pragma unroll
    for (int r = 0; r < 2; ++r) {
      auto rs = __builtin_amdgcn_permlane32_swap(asu(s[r]), asu(s[r + 2]), false, false);
      auto rq = __builtin_amdgcn_permlane32_swap(asu(q[r]), asu(q[r + 2]), false, false);
      s[r] = asf(rs[0]) + asf(rs[1]); q[r] = asf(rq[0]) + asf(rq[1]);
    }
    {
      auto rs = __builtin_amdgcn_permlane16_swap(asu(s[0]), asu(s[1]), false, false);
      auto rq = __builtin_amdgcn_permlane16_swap(asu(q[0]), asu(q[1]), false, false);
      t[0] = asf(rs[0]) + asf(rs[1]); t[1] = asf(rq[0]) + asf(rq[1]);
    }
#pragma unroll
    for (int r = 0; r < 2; ++r) {
      t[r] = sum4(sum8(t[r]));
      t[r] += asf(__builtin_amdgcn_update_dpp(0, (int)asu(t[r]), 0x122, 0xf, 0xf, false));
      t[r] += asf(__builtin_amdgcn_update_dpp(0, (int)asu(t[r]), 0x121, 0xf, 0xf, false));
    }
    float mean, sd;
    ln_stats(t[0], t[1], mean, sd);
    if (l15 == 0) st64c(v2u{pk2(mean, sd), tagv + 1u}, rs_ws, g4 * 8, WS_ROWST + srow * 8);
  }
  if (prod && w == 0) {
    PRIO(3);
    X_ISSUE(2); X_ISSUE(3);
    v4u sb[8];
#pragma unroll
    for (int ks = 0; ks < 8; ++ks) sb[ks] = lds128(LB + L_SILU + ks * 64 + g4 * 16);
    u32 ma[4], mb[4];
#pragma unroll
    for (int c = 0; c < 4; ++c) {
      mb[c] = LB + (8 * g4 + tq) * 128 + tp * 8 + ((c ^ (((tq >> 1) & 1) | ((g4 & 1) << 1))) << 5); ma[c] = mb[c] + L_T;
      PIN(ma[c]); PIN(mb[c]);
    }
    f32x4 acc[4];
#pragma unroll
    for (int c = 0; c < 4; ++c) acc[c] = f32x4{0.f, 0.f, 0.f, 0.f};
    v4u fm[2][2][4];
#define M_READ(h2) _Pragma("unroll") for (int k2 = 0; k2 < 2; ++k2) _Pragma("unroll") for (int c = 0; c < 4; ++c) {               \
      const u32 ad = (h2) < 3 ? ma[c] + (2 * (h2) + k2) * 4096 : mb[c] + k2 * 4096;                                                \
      fm[(h2) & 1][k2][c] = cat(trd(ad), trd(ad + 512)); }
    WAITV(32);
    M_READ(0);
    sfor<0, 4>([&](auto ic) AI {
      constexpr int h2 = ic.v;
      if constexpr (h2 < 3) { waitv<24 - 8 * h2>(); M_READ(h2 + 1); }
      SB();
#pragma unroll
      for (int k2 = 0; k2 < 2; ++k2)
#pragma unroll
        for (int c = 0; c < 4; ++c) acc[c] = mfma16(sb[2 * h2 + k2], fm[h2 & 1][k2][c], acc[c]);
      SB();
    });
    const float lo = (g4 & 1) ? acc[1][0] : acc[0][0], hi = (g4 & 1) ? acc[3][0] : acc[2][0];
    st64c(v2u{asu(hh ? hi : lo), tagv + 1u}, rs_ws, lane * 8, WS_GRAN + kp * 24576 + cg * 512);
    PRIO(0);
  }
  const bool pw = prod && w == 0;
  if (kh == 0 && b >= 224) __builtin_amdgcn_s_sleep(12);
  if (!sw) { X_ISSUE(0); X_ISSUE(1); }
  if (!pw) X_ISSUE(2);
  const v4u rs_w = mk_srd(wqkv, D_ * 3 * D_ * 2);
  const u32 wvo = ((lane >> 2) * 1536 + 8 * (lane & 3)) * 2;
  const u32 wso = ((256 * (tg & 1) + 16 * (tg >> 1)) * 1536 + 32 * j2) * 2;
#define W_ISSUE(part, n) gldsi<((n) & 1) * 32 * 3072 + (part) * 1024, (n) * 1024 + ((part) == 0 ? 8192 : 0)>(rs_w, wvo, wso + (((n) >> 1) ^ rot) * (64 * 3072), xld)
  const bool p4 = j2 < 15;
  const int un0 = w < 4 ? 2 * w : 4 + w;
  f32x2 xf[16][4];
  u32 tagl;
  {
    u32 xa[4];
#pragma unroll
    for (int sp = 0; sp < 4; ++sp) { xa[sp] = LB + w * 16384 + c32 * 128 + (((2 * sp + hh) ^ ((c32 >> 1) & 7)) << 4); PIN(xa[sp]); }
    v4u gr[4];
    v2u gs, gs0;
#pragma unroll
    for (int q4 = 0; q4 < 4; ++q4) {
      if (q4 == 0) { if (pw) WAITV(4); else { X_ISSUE(3); WAITV(12); } }
      if (q4 == 1) { if (pw) WAITV(0); else WAITV(8); }
      if (q4 == 2) { gs0 = ld64c(rs_ws, c32 * 8, WS_ROWST + (128 * m2 + 32 * tg) * 8); WAITV(5); }
      if (q4 == 3) {
        WAITV(1);
        if (kh == 1) {
          PRIO(3);
#pragma unroll
          for (int n = 0; n < 4; ++n) gr[n] = ld128c(rs_ws, lane * 16, WS_GRAN + (n & 1) * 24576 + (tg * 2 + (n >> 1)) * 1024);
          PRIO(0);
        } else {
          sfor<0, 4>([&](auto ic) AI { W_ISSUE(1, ic.v); });
        }
      }
      v4u t[4];
#pragma unroll
      for (int sp = 0; sp < 4; ++sp) t[sp] = lds128(xa[sp] + q4 * 4096);
#pragma unroll
      for (int sp = 0; sp < 4; ++sp) {
#pragma unroll
        for (int e = 0; e < 4; ++e) xf[4 * q4 + sp][e] = f32x2{bflo(t[sp][e]), bfhi(t[sp][e])};
        f32x2(&f)[4] = xf[4 * q4 + sp];
        asm volatile("" : "+v"(f[0]), "+v"(f[1]), "+v"(f[2]), "+v"(f[3]));
      }
      SB();
    }
    PIN(tagv);
    PIN(cpf);
    tagl = tagv + 1u;
    asm volatile("s_waitcnt vmcnt(4)\n\tv_mov_b64 %0, %1" : "=v"(gs) : "v"(gs0) : "memory");
    for (u32 spins = 0; !__all(gs.y == tagl) && spins < SPIN_CAP; ++spins) {
      __builtin_amdgcn_s_sleep(4);
      gs = ld64c(rs_ws, c32 * 8, WS_ROWST + (128 * m2 + 32 * tg) * 8);
      asm volatile("s_waitcnt vmcnt(0)" : "+v"(gs)::"memory");
    }
    {
      const float r = __builtin_amdgcn_rcpf(bfhi(gs.x)), mr = -bflo(gs.x) * r;
      const f32x2 r2 = {r, r}, mr2 = {mr, mr};
#pragma unroll
      for (int s = 0; s < 16; ++s)
#pragma unroll
        for (int e = 0; e < 4; ++e) fma2_ip(xf[s][e], r2, mr2);
    }
    if (kh == 1) {
      asm volatile("s_waitcnt vmcnt(0)" : "+v"(gr[0]), "+v"(gr[1]), "+v"(gr[2]), "+v"(gr[3])::"memory");
      for (u32 spins = 0;; ++spins) {
        bool ok = true;
#pragma unroll
        for (int n = 0; n < 4; ++n) ok = ok && gr[n].y == tagl && gr[n].w == tagl;
        if (__all(ok) || spins >= SPIN_CAP) break;
        if (spins) __builtin_amdgcn_s_sleep(4);
#pragma unroll
        for (int n = 0; n < 4; ++n) gr[n] = ld128c(rs_ws, lane * 16, WS_GRAN + (n & 1) * 24576 + (tg * 2 + (n >> 1)) * 1024);
        asm volatile("s_waitcnt vmcnt(0)" : "+v"(gr[0]), "+v"(gr[1]), "+v"(gr[2]), "+v"(gr[3])::"memory");
      }
      const f32x2 one = dup(tg >= 2 ? 1.0f : 0.0f);
#pragma unroll
      for (int n = 0; n < 2; ++n) {
        const u32 p = pk2(asf(gr[2 * n].x) + asf(gr[2 * n + 1].x), asf(gr[2 * n].z) + asf(gr[2 * n + 1].z));
        const u32 q = pk2v(unp(p) + one);
        sts64(LB + L_CS + tg * 1024 + (((2 * n + hh) ^ rot) << 8) + c32 * 8, v2u{q << 16, q & 0xffff0000u});
      }
    } else {
      sfor<0, 4>([&](auto ic) AI { gldsi<(ic.v & 1) * 32 * 3072 + 2048, 4096 + ic.v * 1024>(rs_w, wvo, wso + ((ic.v >> 1) ^ rot) * (64 * 3072), xld); });
    }
  }
  const u32 tag = __builtin_amdgcn_readfirstlane(tagl);
  f32x16 zero;
#pragma unroll
  for (int i = 0; i < 16; ++i) zero[i] = 0.f;
  v4u hf[16];
  u32 wk = LB + kh * 16384 + (8 * hh + tq) * 64 + (g4 & 1) * 32 + tp * 8, wv = wk + L_B;
#define W_FRAG(base, s) cat(trd((base) + ((s) & 1) * 32768 + ((s) >> 1) * 1024), trd((base) + ((s) & 1) * 32768 + ((s) >> 1) * 1024 + 256))
#define KV_FRAG(s, p) W_FRAG(((s) < 8 ? wk : wv) + (p) * 4096, (s) & 7)
  f32x16 ak = zero, av = zero;
  {
    u32 ca = LB + L_CS + (256 * kh + 8 * hh) * 4, cda = ca + 2048 + (l15 >> 3) * 64 + (l15 & 7) * 4;
    PIN(wk); PIN(wv); PIN(ca); PIN(cda);
    if (kh == 0) WAITV(2);
    BAR();
    if (kh == 1) sfor<0, 4>([&](auto ic) AI {
      gldsi<(ic.v & 1) * 32 * 3072 + 1024, ic.v * 1024>(rs_w, wvo, wso + ((2 + (ic.v >> 1)) ^ rot) * (64 * 3072), xld);
      gldsi<(ic.v & 1) * 32 * 3072 + 2048, 4096 + ic.v * 1024>(rs_w, wvo, wso + ((2 + (ic.v >> 1)) ^ rot) * (64 * 3072), xld);
    });
    float c1[8];
#pragma unroll
    for (int r = 0; r < 8; ++r) c1[r] = *(const LAS float*)(size_t)(cda + r * 128);
    f32x4 hs[2][2];
    v4u fk[2], fv[2];
#define H_READ(s) do { hs[(s) & 1][0] = __builtin_bit_cast(f32x4, lds128(ca + (s) * 64)); hs[(s) & 1][1] = __builtin_bit_cast(f32x4, lds128(ca + (s) * 64 + 16)); } while (0)
#define H_FMA(s, hf_) do { float t0_ = hs[(s) & 1][hf_][0], t1_ = hs[(s) & 1][hf_][1], t2_ = hs[(s) & 1][hf_][2], t3_ = hs[(s) & 1][hf_][3]; \
    fmac_bc<8 * ((s) & 1) + 4 * (hf_) + 0>(t0_, c1[(s) >> 1], xf[s][2 * (hf_)][0]); fmac_bc<8 * ((s) & 1) + 4 * (hf_) + 1>(t1_, c1[(s) >> 1], xf[s][2 * (hf_)][1]); \
    fmac_bc<8 * ((s) & 1) + 4 * (hf_) + 2>(t2_, c1[(s) >> 1], xf[s][2 * (hf_) + 1][0]); fmac_bc<8 * ((s) & 1) + 4 * (hf_) + 3>(t3_, c1[(s) >> 1], xf[s][2 * (hf_) + 1][1]); \
    u32 p0_ = pk2(t0_, t1_), p1_ = pk2(t2_, t3_); PIN(p0_); PIN(p1_); \
    hf[s][2 * (hf_)] = p0_; hf[s][2 * (hf_) + 1] = p1_; } while (0)
    H_READ(0); H_READ(1);
    fk[0] = KV_FRAG(0, 0); fv[0] = KV_FRAG(0, 1);
    SB();
    H_FMA(0, 0); H_FMA(0, 1);
    if (kh == 1) PRIO(1);
    SB();
    sfor<0, 16>([&](auto ic) AI {
      constexpr int s = ic.v;
      if constexpr (s == 3) {
        if (kh == 0) {
          WAITV(0);
          if (p4 && tg != 0) {
            const v4u rs_g = mk_srd(wg, D_ * F_ * 2), rs_u = mk_srd(wu, D_ * F_ * 2);
            const u32 P = (tg - 1) * 64 + lane, gvo = (P / 12) * (F_ * 2) + (((P % 12) ^ (((P / 12) >> 2) & 1)) << 4);
            const u32 gso = 32 * m2 * (F_ * 2) + j2 * 192, gld = LB + L_T + tg * 1024 - 1024;
            gldsi<0, 0>(rs_g, gvo, gso, gld); gldsi<16 * F_ * 2, 3072>(rs_g, gvo, gso, gld);
            gldsi<0, 6144>(rs_u, gvo ^ 32, gso, gld); gldsi<16 * F_ * 2, 9216>(rs_u, gvo ^ 32, gso, gld);
          }
          if (w == 0) {
            asm volatile("buffer_load_dword v250, %0, %1, %2 offen" ::"v"(lane * 128), "s"(mk_srd(wd, F_ * D_ * 2)), "s"(b * 5504) : "memory", "v250");
            asm volatile("buffer_load_dword v250, %0, %1, %2 offen" ::"v"(l15 * 128), "s"(mk_srd(wo, D_ * D_ * 2)), "s"(b * 2048) : "memory", "v250");
          }
        }
        RBAR();
      }
      if constexpr (s == 7) { if (kh == 1) WAITV(4); RBAR(); }
      if constexpr (s == 11) { if (kh == 1) WAITV(0); RBAR(); }
      if constexpr (s >= 12) { if (kh == 1) { W_ISSUE(0, 2 * (s & 3)); W_ISSUE(0, 2 * (s & 3) + 1); } }
      if constexpr (s < 15) { fk[(s + 1) & 1] = KV_FRAG(s + 1, 0); fv[(s + 1) & 1] = KV_FRAG(s + 1, 1); }
      if constexpr (s < 14) H_READ((s + 2) & 15);
      SB();
      ak = mfma32(fk[s & 1], hf[s], ak);
      SB();
      if constexpr (s < 15) H_FMA((s + 1) & 15, 0);
      SB();
      av = mfma32(fv[s & 1], hf[s], av);
      SB();
      if constexpr (s < 15) H_FMA((s + 1) & 15, 1);
      SB();
    });
  }
  PRIO(0);
  const v4u rs_wo = mk_srd(wo, D_ * D_ * 2);
  u32 dvo;
  {
    const u32 r8 = lane >> 3, pc = lane & 7;
    const u32 f = ((r8 >> 1) & 1) | ((w & 1) << 1);
    dvo = r8 * 1024 + ((((pc >> 1) ^ f) * 2 + (pc & 1)) << 4);
  }
#define SIGNAL(wv, cnt, me) do { if (tid >= (wv) * 64 && tid < (wv) * 64 + 4) (void)__hip_atomic_exchange(ctl + (WS_CNT + (cnt) * CNT_STRIDE + lane * 128) / 4 + (me), tag, RLX_AGENT); } while (0)
#define POLL(cnt, rep, nw) do { PRIO(3); poll_line<nw>(ctl + (WS_CNT + (cnt) * CNT_STRIDE + (rep) * 128) / 4, tag); PRIO(0); } while (0)
  v4u wf[2][16];
  const u32 wfo = 12 * j2 + un0 < 172 ? WS_GT + (j2 * 12 + un0) * 16384 : WS_BYTES;
#define WF_GET(n, lo, hi) _Pragma("unroll") for (int k_ = (lo); k_ < (hi); ++k_) wf[n][k_] = ld128c(rs_ws, lane * 16, wfo + (n) * 16384 + k_ * 1024)
#define WO_ISSUE(n) gldsi<(n) * 32768, ((n) >> 1) * 16384 + ((n) & 1) * 4096>(rs_wo, dvo, tg * 8192 + cb * 128, LB + tg * 1024)
  auto proj = [&](u32 base, auto hook) AI {
    f32x16 acc = zero;
    v4u fq[4];
#pragma unroll
    for (int s = 0; s < 3; ++s) fq[s] = W_FRAG(base, s);
    sfor<0, 16>([&](auto ic) AI {
      constexpr int s = ic.v;
      if constexpr (s + 3 < 16) fq[(s + 3) & 3] = W_FRAG(base, s + 3);
      hook(ic);
      SB();
      acc = mfma32(fq[s & 3], hf[s], acc);
      SB();
    });
    return acc;
  };
  v4u pf0, pf1;
  {
    const u32 sc = LB + tg * 16384 + 8192;
    const u32 pa = LB + L_WPHI + (tp & 1) * 512 + (4 * hh + tq) * 32 + (8 * (g4 & 1) + 4 * (tp >> 1)) * 2;
    const v4u pf = cat(trd(pa), trd(pa + 256));
    u32 m1 = 0u - ((lane >> 2) & 1);
    PIN(m1);
    pf1 = pf & m1; pf0 = pf ^ pf1;
    const u32 sg = kh ? 0x80008000u : 0u;
    const v4u pg0 = pf0 ^ v4u{sg, sg, sg, sg}, pg1 = pf1 ^ v4u{sg, sg, sg, sg};
    const v4u k0 = pack8(ak, 0), k1 = pack8(ak, 1);
    sts128(sc + (kh ^ 1) * 2048 + lane * 16, k0);
    sts128(sc + (kh ^ 1) * 2048 + 1024 + lane * 16, k1);
    f32x16 p = mfma32(pg0, k0, zero);
    p = mfma32(pg1, k1, p);
    {
      const u32 row = sc + 4096 + kh * 2048 + c32 * 64 + 8 * hh;
#pragma unroll
      for (int i = 0; i < 4; ++i)
        sts64(row + ((i ^ ((c32 >> 1) & 3)) << 4), v2u{pk2(av[4 * i], av[4 * i + 1]), pk2(av[4 * i + 2], av[4 * i + 3])});
    }
    BAR();
    {
      const v4u o0 = lds128(sc + kh * 2048 + lane * 16), o1 = lds128(sc + kh * 2048 + 1024 + lane * 16);
      p = mfma32(pg0, o0, p);
      p = mfma32(pg1, o1, p);
      u32 ph[8];
      softmax16(p, ph);
      const u32 row = sc + kh * 2048 + c32 * 64;
      sts128(row + (((2 * hh) ^ ((c32 >> 1) & 3)) << 4), v4u{ph[0], ph[1], ph[2], ph[3]});
      sts128(row + (((2 * hh + 1) ^ ((c32 >> 1) & 3)) << 4), v4u{ph[4], ph[5], ph[6], ph[7]});
    }
    u32 aa[2], ba[2];
#pragma unroll
    for (int t = 0; t < 2; ++t) {
      const u32 tok = 8 * g4 + 4 * t + tq;
      const u32 o = LB + 8192 + tok * 64 + (((2 * (tg >> 1) + (tp >> 1)) ^ ((tok >> 1) & 3)) << 4) + 8 * (tp & 1);
      aa[t] = o + (tg & 1) * 2048; ba[t] = o + 4096;
      PIN(aa[t]); PIN(ba[t]);
    }
    if (kh == 0) WAITV(0);
    BAR();
    if (kh == 0) {
      PRIO(3);
      f32x4 kv0 = {0.f, 0.f, 0.f, 0.f}, kv1 = kv0, zz = kv0;
      const v4u ones = v4u{0x3f803f80u, 0x3f803f80u, 0x3f803f80u, 0x3f803f80u};
      v4u a[4], b0[4], b1[4];
#pragma unroll
      for (int ks = 0; ks < 4; ++ks) {
        a[ks] = cat(trd(aa[0] + ks * 16384), trd(aa[1] + ks * 16384));
        b0[ks] = cat(trd(ba[0] + ks * 16384), trd(ba[1] + ks * 16384));
        b1[ks] = cat(trd(ba[0] + ks * 16384 + 2048), trd(ba[1] + ks * 16384 + 2048));
      }
      SB();
#pragma unroll
      for (int ks = 0; ks < 4; ++ks) {
        kv0 = mfma16(a[ks], b0[ks], kv0); kv1 = mfma16(a[ks], b1[ks], kv1); zz = mfma16(a[ks], ones, zz);
      }
      kv0 += kv1;
      st64c(v2u{pk2(kv0[0], kv0[1]), pk2(kv0[2], kv0[3])}, rs_ws, lane * 8, WS_SLAB + ((j2 * 4 + w) * 16 + m2) * 512);
      if (l15 == 0) st64c(v2u{pk2(zz[0], zz[1]), pk2(zz[2], zz[3])}, rs_ws, g4 * 8, WS_Z + ((j2 * 4 + w) * 16 + m2) * 32);
      sfor<0, 8>([&](auto ic) AI { WO_ISSUE(ic.v); });
      WAITV(8);
      PRIO(0);
    } else {
      const u32 gb = LB + L_T + (4 * g4 + tq) * 192 + 8 * (tp & 1);
      const u32 ga = gb + (((2 * tg + (tp >> 1)) ^ (g4 & 1)) << 4), gc = gb + (tp >> 1) * 6144 + (((8 + tg) ^ (g4 & 1) ^ (tp & 2)) << 4);
      const v4u gt0 = cat(trd(ga), trd(ga + 3072)), gt1 = cat(trd((ga ^ 32) + 6144), trd((ga ^ 32) + 9216)), gt2 = cat(trd(gc), trd(gc + 3072));
      const u32 gto = p4 ? WS_GT + j2 * (12 * 16384) + m2 * 1024 : WS_BYTES;
      st128c(gt0, rs_ws, lane * 16, gto + tg * 32768);
      st128c(gt1, rs_ws, lane * 16, gto + tg * 32768 + 16384);
      st128c(gt2, rs_ws, lane * 16, gto + (8 + tg) * 16384);
      WAITV(0);
    }
    RBAR();
    SIGNAL(1, 0 * 16 + j2, m2);
  }
  const u32 orow = (128 * pnl + 64 * rh + 16 * tg + l15) * 1024 + (64 * cb + 32 * kh + 16 * (g4 & 1) + 8 * (g4 >> 1)) * 2;
  v4u xres;
  u32 phq[16];
  {
    if (kh == 1) sfor<8, 16>([&](auto ic) AI { WO_ISSUE(ic.v); });
    xres = ld128(rs_x, orow, 0);
    gu32* const e2 = ctl + (WS_CNT + (0 * 16 + j2) * CNT_STRIDE + (m2 & 3) * 128) / 4;
    const f32x16 aq = proj(wv + 8192, [&](auto ic) AI {
      if constexpr (ic.v == 7 || ic.v == 11 || ic.v == 15) { if (w == 0) poll_kick<((ic.v - 7) >> 2) & 3, 16>(e2); }
    });
    const v4u q0 = pack8(aq, 0), q1 = pack8(aq, 1);
    const u32 ea = LB + L_QEX + tg * 2048 + lane * 16;
    f32x16 p;
    if (kh == 0) { sts128(ea, q0); sts128(ea + 1024, q1); }
    else {
      p = mfma32(pf0, q0, zero);
      p = mfma32(pf1, q1, p);
      if (lane < 16) *(LAS u32*)(size_t)(LB + L_KVT + 34 * 64 + lane * 4) = 0u;
    }
    if (w == 0) poll_wait<16>(e2, tag);
    BAR();
    if (kh == 1) {
      const v4u o0 = lds128(ea), o1 = lds128(ea + 1024);
      p = mfma32(pf0, o0, p);
      p = mfma32(pf1, o1, p);
      hedgehog(p, phq);
    }
  }
  if (kh == 0) {
    PRIO(3);
    v4u sl[8];
    v2u zl = ld64c(rs_ws, lane * 8, WS_Z + (j2 * 4 + w) * 512);
#pragma unroll
    for (int m = 0; m < 8; ++m) sl[m] = ld128c(rs_ws, lane * 16, WS_SLAB + ((j2 * 4 + w) * 8 + m) * 1024);
    WF_GET(0, 0, 4); WF_GET(1, 0, 4);
    v4u sel;
    {
      const u32 hit = ((l15 >> 3) == (g4 & 1)) ? (0x3f80u << (16 * (l15 & 1))) : 0u, q = (l15 >> 1) & 3;
      sel = v4u{q == 0 ? hit : 0u, q == 1 ? hit : 0u, q == 2 ? hit : 0u, q == 3 ? hit : 0u};
    }
    pin4(sel);
    asm volatile("s_waitcnt vmcnt(16)" : "+v"(zl)::"memory");
    const int hd = w >> 1, fh = w & 1;
    {
      auto r = __builtin_amdgcn_permlane32_swap(zl.x, zl.y, false, false);
      float z0 = bflo(r[0]) + bflo(r[1]), z1 = bfhi(r[0]) + bfhi(r[1]);
      z0 = sum16(sum8(sum4(z0))); z1 = sum16(sum8(sum4(z1)));
      *(LAS u32*)(size_t)(LB + L_KVT + (hd * 17 + 16) * 64 + (16 * fh + 4 * tp + 2 * hh) * 2) = pk2(z0, z1);
    }
    f32x4 s0 = {0.f, 0.f, 0.f, 0.f}, s1 = s0;
    sfor<0, 4>([&](auto ic) AI {
      constexpr int m = 2 * ic.v;
      waitv<14 - m>();
      pin4(sl[m]); pin4(sl[m + 1]);
      s0 = mfma16(sel, sl[m], s0);
      s1 = mfma16(sel, sl[m + 1], s1);
    });
    s0 += s1;
    sts64(LB + L_KVT + (hd * 17 + 2 * (l15 & 7) + (g4 & 1)) * 64 + (16 * fh + 4 * (2 * (g4 >> 1) + (l15 >> 3))) * 2,
          v2u{pk2(s0[0], s0[1]), pk2(s0[2], s0[3])});
    PRIO(0);
  }
  u32 ka, za;
  {
    const bool own = ((lane >> 2) & 1) == hh;
    const u32 d = (c32 & 3) + 4 * (c32 >> 3);
    ka = LB + L_KVT + (own ? hh * 17 + d : 34) * 64; za = LB + L_KVT + (own ? hh * 17 + 16 : 34) * 64;
    PIN(ka); PIN(za);
  }
  BAR();
  if (kh == 1) {
    PRIO(3);
    f32x16 num = zero, den = zero;
    v4u a[4], z[4];
#pragma unroll
    for (int s = 0; s < 4; ++s) z[s] = lds128(za + s * 16);
#pragma unroll
    for (int s = 0; s < 4; ++s) a[s] = lds128(ka + s * 16);
    SB();
#pragma unroll
    for (int s = 0; s < 4; ++s) den = mfma32(z[s], v4u{phq[4 * s], phq[4 * s + 1], phq[4 * s + 2], phq[4 * s + 3]}, den);
    SB();
#pragma unroll
    for (int s = 0; s < 4; ++s) num = mfma32(a[s], v4u{phq[4 * s], phq[4 * s + 1], phq[4 * s + 2], phq[4 * s + 3]}, num);
    const f32x2 r = dup(__builtin_amdgcn_rcpf(den[0] + 1e-6f));
    f32x16 o;
#pragma unroll
    for (int i = 0; i < 8; ++i) { const f32x2 t = f32x2{num[2 * i], num[2 * i + 1]} * r; o[2 * i] = t[0]; o[2 * i + 1] = t[1]; }
    const u32 vo = (c32 >> 4) * 16384 + hh * 512 + l15 * 16;
    const u32 so = WS_O + ((8 * m2 + 2 * tg) * 16 + j2) * 1024;
    st128c(pack8(o, 0), rs_ws, vo, so);
    st128c(pack8(o, 1), rs_ws, vo + 256, so);
    WF_GET(0, 0, 8);
    WAITV(8);
    PRIO(0);
  } else {
    v4u gr[5];
    for (u32 spins = 0;; ++spins) {
#pragma unroll
      for (int n = 0; n < 4; ++n) gr[n] = ld128c(rs_ws, lane * 16, WS_GRAN + (n & 1) * 24576 + (12 + tg * 2 + (n >> 1)) * 1024);
      gr[4] = ld128c(rs_ws, c32 * 16 + hh * 24576, WS_GRAN + ((tg & 1) ? 40 + cb : 16 + cb) * 512);
      asm volatile("s_waitcnt vmcnt(0)" : "+v"(gr[0]), "+v"(gr[1]), "+v"(gr[2]), "+v"(gr[3]), "+v"(gr[4])::"memory");
      bool ok = true;
#pragma unroll
      for (int n = 0; n < 5; ++n) ok = ok && gr[n].y == tag && gr[n].w == tag;
      if (__all(ok) || spins >= SPIN_CAP) break;
    }
    const f32x2 one = dup(tg >= 2 ? 1.0f : 0.0f);
    const u32 vperm = g4 * 64 + ((lane >> 1) & 3) * 16 + ((lane >> 3) & 1) * 8 + (lane & 1) * 4;
#pragma unroll
    for (int n = 0; n < 2; ++n) {
      const u32 p = pk2(asf(gr[2 * n].x) + asf(gr[2 * n + 1].x), asf(gr[2 * n].z) + asf(gr[2 * n + 1].z));
      *(LAS u32*)(size_t)(LB + L_VEC + 1024 + (tg * 2 + n) * 256 + vperm) = pk2v(unp(p) + one);
    }
    *(LAS u32*)(size_t)(LB + L_VEC + (tg & 1) * 128 + (vperm & 127)) = pk2(sum32(asf(gr[4].x)), sum32(asf(gr[4].z)));
  }
  BAR();
  SIGNAL(1, 1 * 16 + m2, j2);
  gu32* const e3 = ctl + (WS_CNT + (1 * 16 + pnl) * CNT_STRIDE + (bi & 3) * 128) / 4;
  if (kh == 0) { WF_GET(0, 4, 8); }
  if (w == 0) poll_kick<0, 16>(e3);
  u32 wra[4], wo_a[4];
#pragma unroll
  for (int c = 0; c < 4; ++c) {
    wra[c] = LB + (8 * g4 + tq) * 128 + tp * 8 + ((c ^ (2 * kh) ^ (((tq >> 1) & 1) | ((g4 & 1) << 1))) << 5);
    wo_a[c] = wra[c] + kh * 65536;
    PIN(wo_a[c]);
  }
  f32x4 cs0 = {0.f, 0.f, 0.f, 0.f}, cs1 = {0.f, 0.f, 0.f, 0.f};
  u32 cva = LB + L_VEC + 2048 - (l15 >> 3) * 1024 + g4 * 16;
  u32 x1p[4];
  {
    v4u of[8];
#define WO_FRAG(i, c) cat(trd(wo_a[c] + ((i) >> 1) * 16384 + ((i) & 1) * 4096), trd(wo_a[c] + ((i) >> 1) * 16384 + ((i) & 1) * 4096 + 512))
    const u32 va = LB + L_VEC + kh * 64 + g4 * 16;
    const v4u ga4 = lds128(va), cb4 = lds128(va + 2048 + cb * 128);
    v4u fa[6][4];
#pragma unroll
    for (int i = 0; i < 6; ++i)
#pragma unroll
      for (int c = 0; c < 4; ++c) fa[i][c] = WO_FRAG(i, c);
    if (w == 0) poll_kick<1, 16>(e3);
    PIN(xres);
    auto r0 = __builtin_amdgcn_permlane16_swap(xres.x, xres.z, false, false);
    auto r1 = __builtin_amdgcn_permlane16_swap(xres.y, xres.w, false, false);
    f32x2 xv[4] = {unp(r0[0]), unp(r1[0]), unp(r0[1]), unp(r1[1])};
    f32x2 gv[4] = {unp(ga4.x), unp(ga4.y), unp(ga4.z), unp(ga4.w)};
    f32x2 cv[4] = {unp(cb4.x), unp(cb4.y), unp(cb4.z), unp(cb4.w)};
#pragma unroll
    for (int e = 0; e < 4; ++e) { PIN(xv[e]); PIN(gv[e]); PIN(cv[e]); }
    u32 ea = LB + L_EX3 + tg * 4096 + lane * 16 + kh * 2048;
    PIN(ea);
    if (w == 0) { poll_kick<2, 16>(e3); poll_wait<16>(e3, tag); }
    RBAR();
#pragma unroll
    for (int i = 0; i < 8; ++i) of[i] = ld128c(rs_ws, lane * 16, WS_O + ((8 * pnl + 4 * rh + tg) * 16 + 8 * kh + i) * 1024);
    f32x4 acc[4];
    sfor<0, 8>([&](auto ic) AI {
      constexpr int i = ic.v;
      waitv<7 - i>();
      pin4(of[i]);
      SB();
#pragma unroll
      for (int c = 3; c >= 0; --c) {
        if constexpr (i == 0) mfma16_z(acc[c], fa[0][c], of[0]);
        else mfma16_ip(acc[c], fa[i % 6][c], of[i]);
        if constexpr (i + 6 < 8) fa[i % 6][c] = WO_FRAG(i + 6, c);
        SB();
      }
    });
    asm volatile("s_nop 4" ::: "memory");
    SB();
    sts128(ea + 1024, __builtin_bit_cast(v4u, acc[3]));
    sts128(ea, __builtin_bit_cast(v4u, acc[2]));
    WF_GET(0, 8, 12);
    BAR();
    const f32x4 t0 = acc[0] + __builtin_bit_cast(f32x4, lds128(ea ^ 2048));
    const f32x4 t1 = acc[1] + __builtin_bit_cast(f32x4, lds128((ea ^ 2048) + 1024));
    f32x2 sum = {0.f, 0.f}, sq = {0.f, 0.f};
    v4u y;
#pragma unroll
    for (int e = 0; e < 4; ++e) {
      const f32x2 at = unp(e < 2 ? pk2(t0[2 * e], t0[2 * e + 1]) : pk2(t1[2 * e - 4], t1[2 * e - 3]));
      const f32x2 gp = gv[e] * at, xs = xv[e] + unp(pk2(gp[0], gp[1]));
      x1p[e] = pk2(xs[0], xs[1]);
      const f32x2 x1 = unp(x1p[e]), yy = x1 * cv[e];
      y[e] = pk2(yy[0], yy[1]);
      sum += x1; sq = fma2(x1, x1, sq);
    }
    st128c(y, rs_ws, lane * 16, WS_Y1 + ((8 * pnl + 4 * rh + tg) * 16 + 2 * cb + kh) * 1024);
    const float s1 = sum32(sum16(hadd(sum))), q1 = sum32(sum16(hadd(sq)));
    st64c(v2u{asu(s1), asu(q1)}, rs_ws, lane < 16 ? lane * 8 : WS_BYTES, WS_STAT + ((8 * pnl + 4 * rh + tg) * 16 + 2 * cb + kh) * 128);
    WF_GET(0, 12, 16);
    WAITV(4);
  }
  BAR();
  SIGNAL(4, 2 * 16 + pnl, bi & 15);
  const v4u rs_wd = mk_srd(wd, F_ * D_ * 2);
  const u32 dso = tg * 8192 + cb * 128, dld = LB + tg * 1024;
#define WD_ISSUE(i, h) gldsi<(22 * (h) + (i)) * 32768, (2 * wd_sg(i) + (h)) * 4096>(rs_wd, dvo, dso, dld)
#define WD_ISSUE_K(i) gldsi<(i) * 32768, 2 * wd_sg(i) * 4096>(rs_wd, dvo, dso + kh * (22 * 32768), dld + kh * 4096)
  if (p4) {
    auto cs_part = [&](auto lo, auto hi) AI {
      v4u cf[hi.v - lo.v];
#pragma unroll
      for (int ks = lo.v; ks < hi.v; ++ks) { pin4(wf[0][ks]); cf[ks - lo.v] = lds128(cva + ks * 64); }
      SB();
#pragma unroll
      for (int ks = lo.v; ks < hi.v; ++ks) cs0 = mfma16(wf[0][ks], cf[ks - lo.v], cs0);
      pinf(cs0);
    };
    PIN(cva);
    if (kh == 0) { WF_GET(1, 4, 10); }
    gu32* const e4 = ctl + (WS_CNT + (2 * 16 + m2) * CNT_STRIDE + (bi & 3) * 128) / 4;
    if (w == 7) poll_kick<0, 16>(e4);
    cs_part(IC<0>{}, IC<6>{});
    if (w == 7) poll_kick<1, 16>(e4);
    cs_part(IC<6>{}, IC<12>{});
    if (w == 7) { poll_kick<2, 16>(e4); poll_wait<16>(e4, tag); }
    RBAR();
    u32 ya0 = LB + lane * 16, ya1 = ya0 + 65536;
    PIN(ya0); PIN(ya1);
#define Y_READ(i) lds128(((i) < 64 ? ya0 : ya1) + ((i) & 63) * 1024)
    constexpr int PD = 6, RING = 7;
    u32 sta = LB + L_STAT2 + l15 * 16;
    PIN(sta);
    const u32 hvo = l15 * 16, hso = WS_HH + 8 * m2 * (43 * 1024);
    if (w >= 4) {
      f32x4 acc0[8];
      v4u st[4], yr[RING];
      const u32 yso = WS_Y1 + (8 * m2 + 2 * tg) * 16384, yld = LB + tg * 2048;
#define Y_ISSUE(n) gldsic<((n) & 1) * 16384 + ((n) >> 1) * 1024, (((n) >> 1) * 8 + ((n) & 1)) * 1024>(rs_ws, lane * 16, yso, yld)
      PRIO(2);
      sfor<0, 12>([&](auto ic) AI { Y_ISSUE(ic.v); });
      WAITV(12);
      cs_part(IC<12>{}, IC<16>{});
      WAITV(8);
      RBAR();
#pragma unroll
      for (int i = 0; i < PD; ++i) yr[i] = Y_READ(i);
      sfor<0, 128>([&](auto ic) AI {
        constexpr int i = ic.v, rd = i + PD;
        if constexpr (rd < 128) {
          if constexpr ((rd & 15) == 0) { waitv<ops_before(i) - (rd == 112 ? 46 : 4 * (rd >> 4) + 4)>(); RBAR(); }
          yr[rd % RING] = Y_READ(rd);
        }
        if constexpr (i < 20) Y_ISSUE(12 + i);
        if constexpr (i >= 20 && i < 24) st[i - 20] = ld128c(rs_ws, lane * 16, WS_STAT + (8 * m2 + 2 * tg) * 2048 + (i - 20) * 1024);
        if constexpr (wd_slot(i) >= 0) {
          static_assert((wd_slot(i) >> 1) - 3 <= 2 * (rd >> 4) - 2, "slot still holds activations");
          WD_ISSUE(wd_slot(i) >> 1, wd_slot(i) & 1);
        }
        if constexpr (i == 76) {
          waitv<ops_before(i) - 36>();
          pin4(st[0]); pin4(st[1]); pin4(st[2]); pin4(st[3]);
          const f32x4 t0 = __builtin_bit_cast(f32x4, st[0]) + __builtin_bit_cast(f32x4, st[1]);
          const f32x4 t1 = __builtin_bit_cast(f32x4, st[2]) + __builtin_bit_cast(f32x4, st[3]);
          float t[4], q[2];
#pragma unroll
          for (int e = 0; e < 4; ++e) {
            auto r = __builtin_amdgcn_permlane32_swap(asu(t0[e]), asu(t1[e]), false, false);
            t[e] = asf(r[0]) + asf(r[1]);
          }
#pragma unroll
          for (int e = 0; e < 2; ++e) {
            auto r = __builtin_amdgcn_permlane16_swap(asu(t[e]), asu(t[e + 2]), false, false);
            q[e] = sum8(asf(r[0]) + asf(r[1]));
          }
          float m0, d0;
          ln_stats(q[0], q[1], m0, d0);
          const float ri = __builtin_amdgcn_rcpf(d0);
          sts128(LB + L_STAT2 + (2 * tg + hh) * 256 + (lane & 7) * 32 + (g4 & 1) * 16, v4u{asu(-m0), asu(-m0), asu(ri), asu(ri)});
        }
        if constexpr ((i & 1) == 0) pin4(yr[(i + 1) % RING]);
        SB();
        if constexpr (i < 8) mfma16_z(acc0[i], wf[0][0], yr[i % RING]);
        else mfma16_ip(acc0[i & 7], wf[0][i >> 3], yr[i % RING]);
        SB();
      });
      SB();
      asm volatile("s_nop 7" ::: "memory");
      SB();
      PRIO(0);
      {
        v4u sv[8];
#pragma unroll
        for (int t = 0; t < 8; ++t) sv[t] = lds128(sta + t * 256);
        const f32x2 cwa = {bcast<0>(cs0[0]), bcast<0>(cs0[1])}, cwb = {bcast<0>(cs0[2]), bcast<0>(cs0[3])};
        const f32x2 swa = {bcast<8>(cs0[0]), bcast<8>(cs0[1])}, swb = {bcast<8>(cs0[2]), bcast<8>(cs0[3])};
        f32x2 va[8], vb[8];
#pragma unroll
        for (int t = 0; t < 8; ++t) {
          const f32x2 m = {asf(sv[t].x), asf(sv[t].y)}, d = {asf(sv[t].z), asf(sv[t].w)};
          va[t] = fma2(fma2(m, cwa, f32x2{acc0[t][0], acc0[t][1]}), d, swa);
          vb[t] = fma2(fma2(m, cwb, f32x2{acc0[t][2], acc0[t][3]}), d, swb);
        }
        u32 hp[4][2];
#pragma unroll
        for (int t2 = 0; t2 < 4; ++t2) {
          auto r0 = __builtin_amdgcn_permlane32_swap(asu(va[2 * t2][0]), asu(va[2 * t2 + 1][0]), false, false);
          auto r1 = __builtin_amdgcn_permlane32_swap(asu(va[2 * t2][1]), asu(va[2 * t2 + 1][1]), false, false);
          auto r2 = __builtin_amdgcn_permlane32_swap(asu(vb[2 * t2][0]), asu(vb[2 * t2 + 1][0]), false, false);
          auto r3 = __builtin_amdgcn_permlane32_swap(asu(vb[2 * t2][1]), asu(vb[2 * t2 + 1][1]), false, false);
          hp[t2][0] = pk2(asf(r0[0]) * asf(r0[1]), asf(r1[0]) * asf(r1[1]));
          hp[t2][1] = pk2(asf(r2[0]) * asf(r2[1]), asf(r3[0]) * asf(r3[1]));
        }
        const int ug = 12 * j2 + un0;
        const u32 so = ug < 172 ? hso + ug * 256 : WS_BYTES;
#pragma unroll
        for (int qd = 0; qd < 2; ++qd) {
          auto r0 = __builtin_amdgcn_permlane16_swap(hp[2 * qd][0], hp[2 * qd + 1][0], false, false);
          auto r1 = __builtin_amdgcn_permlane16_swap(hp[2 * qd][1], hp[2 * qd + 1][1], false, false);
          st128c(v4u{r0[0], r1[0], r0[1], r1[1]}, rs_ws, hvo + (2 * (g4 & 1) + (g4 >> 1)) * (43 * 1024), so + qd * (4 * 43 * 1024));
        }
      }
#pragma unroll
      for (int ks = 0; ks < 16; ++ks) { asm volatile("" : "=v"(wf[0][ks])); asm volatile("" : "=v"(wf[1][ks])); }
      asm volatile("" : "=v"(cs0), "=v"(cs1));
    }
    int w2 = w;
    asm volatile("" : "+s"(w2));
    if (w2 < 4) {
      f32x4 acc0[8], acc1[8];
      v4u yr[RING], cfr, sv[8];
      WAITV(6);
      cs_part(IC<12>{}, IC<16>{});
      WAITV(0);
#pragma unroll
      for (int ks = 0; ks < 10; ++ks) PIN(wf[1][ks]);
      RBAR();
#pragma unroll
      for (int i = 0; i < PD; ++i) yr[i] = Y_READ(i);
      sfor<0, 128>([&](auto ic) AI {
        constexpr int i = ic.v, rd = i + PD;
        if constexpr (i == 80) { WAITV(0); _Pragma("unroll") for (int ks = 10; ks < 16; ++ks) pin4(wf[1][ks]); }
        if constexpr ((i & 1) == 0) pin4(yr[(i + 1) % RING]);
        SB();
        if constexpr (i < 8) mfma16_z(acc0[i], wf[0][0], yr[i % RING]);
        else mfma16_ip(acc0[i & 7], wf[0][i >> 3], yr[i % RING]);
        SB();
        if constexpr (rd < 128) {
          if constexpr ((rd & 15) == 0) RBAR();
          yr[rd % RING] = Y_READ(rd);
        }
        SB();
        if constexpr (i < 8) mfma16_z(acc1[i], wf[1][0], yr[i % RING]);
        else mfma16_ip(acc1[i & 7], wf[1][i >> 3], yr[i % RING]);
        SB();
        if constexpr ((i & 7) == 0) cfr = lds128(cva + (i >> 3) * 64);
        if constexpr ((i & 7) == 6) mfma16_ip(cs1, wf[1][i >> 3], cfr);
        if constexpr (i >= 120) sv[i - 120] = lds128(sta + (i - 120) * 256);
        if constexpr (i >= 36 && i < 60 && (i & 3) == 2) { WF_GET(1, 10 + ((i - 36) >> 2), 11 + ((i - 36) >> 2)); }
        SB();
      });
      SB();
      asm volatile("s_nop 7" ::: "memory");
      SB();
      {
        const f32x2 cga = {bcast<0>(cs0[0]), bcast<0>(cs0[1])}, cgb = {bcast<0>(cs0[2]), bcast<0>(cs0[3])};
        const f32x2 sga = {bcast<8>(cs0[0]), bcast<8>(cs0[1])}, sgb = {bcast<8>(cs0[2]), bcast<8>(cs0[3])};
        const f32x2 cua = {bcast<0>(cs1[0]), bcast<0>(cs1[1])}, cub = {bcast<0>(cs1[2]), bcast<0>(cs1[3])};
        const f32x2 sua = {bcast<8>(cs1[0]), bcast<8>(cs1[1])}, sub = {bcast<8>(cs1[2]), bcast<8>(cs1[3])};
        const int ug = 12 * j2 + un0;
        const u32 so = ug < 172 ? hso + ug * 256 : WS_BYTES;
        const u32 vo = hvo + (g4 & 1) * (43 * 1024) + (g4 >> 1) * 256;
#pragma unroll
        for (int t2 = 0; t2 < 4; ++t2) {
          u32 hp[2][2];
#pragma unroll
          for (int k = 0; k < 2; ++k) {
            const int t = 2 * t2 + k;
            const f32x2 m = {asf(sv[t].x), asf(sv[t].y)}, d = {asf(sv[t].z), asf(sv[t].w)};
            const f32x2 pa = fma2(fma2(m, cga, f32x2{acc0[t][0], acc0[t][1]}), d, sga) * fma2(fma2(m, cua, f32x2{acc1[t][0], acc1[t][1]}), d, sua);
            const f32x2 pb = fma2(fma2(m, cgb, f32x2{acc0[t][2], acc0[t][3]}), d, sgb) * fma2(fma2(m, cub, f32x2{acc1[t][2], acc1[t][3]}), d, sub);
            hp[k][0] = pk2(pa[0], pa[1]); hp[k][1] = pk2(pb[0], pb[1]);
          }
          auto r0 = __builtin_amdgcn_permlane16_swap(hp[0][0], hp[1][0], false, false);
          auto r1 = __builtin_amdgcn_permlane16_swap(hp[0][1], hp[1][1], false, false);
          st128c(v4u{r0[0], r1[0], r0[1], r1[1]}, rs_ws, vo, so + t2 * (2 * 43 * 1024));
        }
      }
    }
  } else {
    sfor<0, 16>([&](auto ic) AI { WD_ISSUE_K(ic.v); });
    WAITV(0);
    RBAR();
  }
  {
    u32 da0[4], da1[4], da2[4];
#pragma unroll
    for (int c = 0; c < 4; ++c) { da0[c] = wra[c] + kh * 4096; da1[c] = da0[c] + 65536; da2[c] = da0[c] + 131072; PIN(da0[c]); PIN(da1[c]); PIN(da2[c]); }
#define D_ADDR(i, c) (wd_sg(i) < 8 ? da0[c] + wd_sg(i) * 8192 : (wd_sg(i) < 16 ? da1[c] + (wd_sg(i) - 8) * 8192 : da2[c] + (wd_sg(i) - 16) * 8192))
#define D_READ(i, c) cat(trd(D_ADDR(i, c)), trd(D_ADDR(i, c) + 512))
    constexpr int NP = 10;
    v4u fa[NP][4];
#define D_PRE() _Pragma("unroll") for (int i = 5; i < NP; ++i) _Pragma("unroll") for (int c = 0; c < 4; ++c) fa[i][c] = D_READ(i, c)
#pragma unroll
    for (int i = 0; i < 5; ++i)
#pragma unroll
      for (int c = 0; c < 4; ++c) fa[i][c] = D_READ(i, c);
    const v4u gb4 = lds128(LB + L_VEC + 128 + kh * 64 + g4 * 16);
    WAITV(0);
    BAR();
    if (p4) SIGNAL(1, 3 * 16 + m2, j2);
    if (w == 0) POLL(3 * 16 + pnl, bi & 3, 15);
    sfor<16, 22>([&](auto ic) AI { WD_ISSUE_K(ic.v); });
    if (w != 0) { D_PRE(); }
    RBAR();
    constexpr int HA = 5;
    v4u hf2[HA + 1];
    const u32 hso = WS_HH + (8 * pnl + 4 * rh + tg) * (43 * 1024) + kh * (22 * 1024);
#define H_GET(i) hf2[(i) % (HA + 1)] = ld128c(rs_ws, lane * 16, (i) < 21 ? hso + (i) * 1024 : hso + (21 - kh) * 1024)
#pragma unroll
    for (int i = 0; i < HA; ++i) H_GET(i);
    if (w == 0) { D_PRE(); }
    f32x2 gv[4] = {unp(gb4.x), unp(gb4.y), unp(gb4.z), unp(gb4.w)}, xv[4] = {unp(x1p[0]), unp(x1p[1]), unp(x1p[2]), unp(x1p[3])};
#pragma unroll
    for (int e = 0; e < 4; ++e) { PIN(gv[e]); PIN(xv[e]); }
    u32 ea = LB + tg * 4096 + lane * 16 + kh * 2048;
    PIN(ea);
    f32x4 acc[4];
    sfor<0, 22>([&](auto ic) AI {
      constexpr int i = ic.v;
      if constexpr (i == 8) RBAR();
      if constexpr (i + HA < 22) H_GET(i + HA);
      waitv<(i + HA < 22 ? HA : 21 - i)>();
      pin4(hf2[i % (HA + 1)]);
      SB();
      constexpr int NR = (22 - NP) * 4, n0 = i < 20 ? i * NR / 20 : NR, n1 = i < 20 ? (i + 1) * NR / 20 : NR;
      sfor<0, 4>([&](auto mc) AI {
        constexpr int c = 3 - mc.v, n = n1 - (4 - mc.v);
        if constexpr (i == 0) mfma16_z(acc[c], fa[0][c], hf2[0]);
        else mfma16_ip(acc[c], fa[i % NP][c], hf2[i % (HA + 1)]);
        if constexpr (n >= n0) {
          static_assert(n / 4 < i || (n / 4 == i && (n & 3) <= mc.v), "slot still needed");
          static_assert(NP + n / 4 > i + 1 && (NP + n / 4 < 16 || i >= 8), "too late / not yet visible");
          fa[(n / 4) % NP][3 - (n & 3)] = D_READ(NP + n / 4, 3 - (n & 3));
        }
        SB();
      });
    });
    asm volatile("s_nop 4" ::: "memory");
    SB();
    sts128(ea + 1024, __builtin_bit_cast(v4u, acc[3]));
    sts128(ea, __builtin_bit_cast(v4u, acc[2]));
    BAR();
    const f32x4 t0 = acc[0] + __builtin_bit_cast(f32x4, lds128(ea ^ 2048));
    const f32x4 t1 = acc[1] + __builtin_bit_cast(f32x4, lds128((ea ^ 2048) + 1024));
    u32 op[4];
#pragma unroll
    for (int e = 0; e < 4; ++e) {
      const f32x2 gp = gv[e] * unp(e < 2 ? pk2(t0[2 * e], t0[2 * e + 1]) : pk2(t1[2 * e - 4], t1[2 * e - 3]));
      const f32x2 o2 = xv[e] + unp(pk2(gp[0], gp[1]));
      op[e] = pk2(o2[0], o2[1]);
    }
    auto r0 = __builtin_amdgcn_permlane16_swap(op[0], op[2], false, false);
    auto r1 = __builtin_amdgcn_permlane16_swap(op[1], op[3], false, false);
    *(GAS v4u*)((GAS char*)out + orow) = v4u{r0[0], r1[0], r0[1], r1[1]};
  }
  asm volatile(".Lfb_code_end:");
  if (b == 0 && tid == 0) __hip_atomic_store(ctl + WS_EPOCH / 4, tag, RLX_AGENT);
}

extern "C" void kernel_launch(void* const* d_in, const int* in_sizes, int n_in, void* d_out, int out_size, void* d_ws, size_t ws_size,
                              hipStream_t stream) {
  (void)in_sizes; (void)n_in; (void)out_size; (void)ws_size;
  (void)hipFuncSetAttribute((const void*)fused_block, hipFuncAttributeMaxDynamicSharedMemorySize, L_TOTAL);
  fused_block<<<256, 512, L_TOTAL, stream>>>((const u16*)d_in[0], (const u16*)d_in[1], (const u16*)d_in[2], (const u16*)d_in[3],
                                             (const u16*)d_in[5], (const u16*)d_in[6], (const u16*)d_in[7], (const u16*)d_in[8],
                                             (const u16*)d_in[9], (u16*)d_out, (char*)d_ws);
}
```
